```python
import torch

def _softmax(x, axis=-1):
    return torch.softmax(x, dim=axis)

def _gelu(x):
    return torch.nn.functional.gelu(x, approximate='tanh')

def _silu(x):
    return torch.nn.functional.silu(x)

def _sigmoid(x):
    return torch.sigmoid(x)

def _layernorm(x, axis=-1, eps=1e-05):
    m = x.mean(dim=axis, keepdim=True)
    v = x.var(dim=axis, keepdim=True, unbiased=False)
    return (x - m) / torch.sqrt(v + eps)

def _scatter_add(tgt, idx, src, axis=0):
    return tgt.clone().index_add_(axis, idx, src)

def _segment_sum(src, seg, n):
    out = torch.zeros((n,) + src.shape[1:], dtype=src.dtype)
    return out.index_add_(0, seg, src)

def _pad(x, pw):
    flat = tuple((v for pair in reversed(pw) for v in pair))
    return torch.nn.functional.pad(x, flat)
kernel_name = 'adaLN_cond__softmax_full_mha__dense_activation_silu__prompt'

def setup_inputs():

    _w = lambda a: (lambda t: t.to(torch.bfloat16) if t.is_floating_point() else t)(torch.as_tensor(a))

    def _A_setup():
        B, S, D = (1, 2048, 576)
        return {'x': _w((lambda *s: torch.randn(*s, dtype=torch.bfloat16) * 0.05)(B, S, D)), 'wqkv': _w((lambda *s: torch.randn(*s, dtype=torch.bfloat16) * 0.05)(D, 3 * D)), 'wo': _w((lambda *s: torch.randn(*s, dtype=torch.bfloat16) * 0.05)(D, D))}
    _A_in = _A_setup()

    def _B_setup():
        B, S, D, F = (1, 2048, 576, 4608)
        return {'x': _w((lambda *s: torch.randn(*s, dtype=torch.bfloat16) * 0.05)(B, S, D)), 'w1': _w((lambda *s: torch.randn(*s, dtype=torch.bfloat16) * 3.2)(D, F) * D ** (-0.5)), 'w2': _w((lambda *s: torch.randn(*s, dtype=torch.bfloat16) * 0.4)(F, D) * F ** (-0.5))}
    _B_in = _B_setup()
    return {'A_x': _A_in['x'], 'A_wqkv': _A_in['wqkv'], 'A_wo': _A_in['wo'], 'B_x': _B_in['x'], 'B_w1': _B_in['w1'], 'B_w2': _B_in['w2'], 'c': _w((lambda *s: torch.randn(*s, dtype=torch.bfloat16) * 0.1)(1, 576) * 8.0), 'w_mod': _w((lambda *s: torch.randn(*s, dtype=torch.bfloat16) * 0.1)(576, 6 * 576) * 576 ** (-0.5) * 8.0)}

def reference(A_x, A_wqkv, A_wo, B_x, B_w1, B_w2, c, w_mod):

    def _A_ref(x, wqkv, wo):
        B, S, D = x.shape
        H = 32
        Dh = D // H
        qkv = (lambda x, *d: x.permute(*d))((x @ wqkv).reshape(B, S, 3, H, Dh), 2, 0, 3, 1, 4)
        q, k, v = (qkv[0], qkv[1], qkv[2])
        scale = Dh ** (-0.5)
        scores = torch.einsum('bhqd,bhkd->bhqk', q, k) * scale
        p = _softmax(scores, axis=-1)
        o = torch.einsum('bhqk,bhkd->bhqd', p, v)
        return (lambda x, *d: x.permute(*d))(o, 0, 2, 1, 3).reshape(B, S, D) @ wo

    def _B_ref(x, w1, w2):
        h = torch.einsum('bsd,df->bsf', x, w1)
        h = _silu(h)
        out = torch.einsum('bsf,fd->bsd', h, w2)
        return out
    x = A_x
    mod = (_silu(c) @ w_mod).reshape(c.shape[0], 6, 1, -1)
    sa, ca, ga, sb, cb, gb = (mod[:, 0].clone(), mod[:, 1].clone(), mod[:, 2].clone(), mod[:, 3].clone(), mod[:, 4].clone(), mod[:, 5].clone())
    h = _layernorm(x, axis=-1) * (1 + ca) + sa
    x = x + ga * _A_ref(h, A_wqkv, A_wo)
    h = _layernorm(x, axis=-1) * (1 + cb) + sb
    x = x + gb * _B_ref(h, B_w1, B_w2)
    return x
```

```cpp
#include <hip/hip_runtime.h>
#include <hip/hip_bf16.h>

typedef __attribute__((ext_vector_type(8))) __bf16 bf16x8;
typedef __attribute__((ext_vector_type(2))) __bf16 bf16x2;
typedef __attribute__((ext_vector_type(4))) short s16x4;
typedef __attribute__((ext_vector_type(2))) float f32x2;
typedef __attribute__((ext_vector_type(4))) float f32x4;
typedef __attribute__((ext_vector_type(16))) float f32x16;
typedef __attribute__((ext_vector_type(32))) float f32x32;
typedef __attribute__((ext_vector_type(4))) unsigned u32x4;
typedef __attribute__((ext_vector_type(2))) unsigned u32x2;
typedef unsigned short u16;
#define DI __device__ __forceinline__
#define LDSP(T) __attribute__((address_space(3))) T*
#define GLBP(T) __attribute__((address_space(1))) T*

constexpr int S_ = 2048, D_ = 576, H_ = 32, F_ = 4608;
constexpr float LN_EPS = 1e-5f;
constexpr float LOG2E = 1.4426950408889634f;
constexpr float QSCALE = 0.23570226039551584f * LOG2E;

constexpr size_t WS_FLAGS = 0;
constexpr size_t WS_MOD   = 65536;
constexpr size_t WS_XN    = WS_MOD + 16384;
constexpr size_t SZ_ACT   = 2048 * 576 * 2;
constexpr size_t WS_WQT   = WS_XN + SZ_ACT;
constexpr size_t WS_QM    = WS_WQT + 32 * 72 * 1024;
constexpr size_t WS_QX    = WS_QM + 32 * 64 * 1024;
constexpr size_t WS_KV    = WS_QX + 32 * 2048 * 4;
constexpr size_t WS_OF    = WS_KV + 32 * 8 * 18432;
constexpr size_t WS_WOT   = WS_OF + SZ_ACT;
constexpr size_t WS_X1    = WS_WOT + 8 * 5 * 18 * 1024;
constexpr size_t WS_W1T   = WS_X1 + SZ_ACT;
constexpr size_t WS_W2T   = WS_W1T + 8 * 36 * 18 * 1024;
constexpr size_t WS_PART  = WS_W2T + 8 * 36 * 18 * 1024;
constexpr int KVBLK = 18432, KV_V = 8192, KV_X = 8192 + 9216;

DI unsigned cvtpk(float lo, float hi) { f32x2 v = {lo, hi}; return __builtin_bit_cast(unsigned, __builtin_convertvector(v, bf16x2)); }
DI float bflo(unsigned u) { return __builtin_bit_cast(float, u << 16); }
DI float bfhi(unsigned u) { return __builtin_bit_cast(float, u & 0xffff0000u); }
DI float bf2f(u16 u) { return __builtin_bit_cast(float, (unsigned)u << 16); }
DI float rbf(float x) { return bflo(cvtpk(x, x)); }
DI bf16x8 asbf(u32x4 v) { return __builtin_bit_cast(bf16x8, v); }
DI f32x16 mfma32(u32x4 a, u32x4 b, f32x16 c) { return __builtin_amdgcn_mfma_f32_32x32x16_bf16(asbf(a), asbf(b), c, 0, 0, 0); }
DI f32x4 mfma16(u32x4 a, u32x4 b, f32x4 c) { return __builtin_amdgcn_mfma_f32_16x16x32_bf16(asbf(a), asbf(b), c, 0, 0, 0); }
DI float swap32(float v) {
  unsigned u = __builtin_bit_cast(unsigned, v);
  auto r = __builtin_amdgcn_permlane32_swap(u, u, false, false);
  return __builtin_bit_cast(float, (threadIdx.x & 32) ? r[0] : r[1]);
}
DI void glds16(const void* g, void* l) {
  __builtin_amdgcn_global_load_lds((const GLBP(void))g, (LDSP(void))l, 16, 0, 0);
}
DI float sum8(float v) {
  v += __builtin_bit_cast(float, __builtin_amdgcn_update_dpp(0, __builtin_bit_cast(int, v), 0xB1, 0xf, 0xf, true));
  v += __builtin_bit_cast(float, __builtin_amdgcn_update_dpp(0, __builtin_bit_cast(int, v), 0x4E, 0xf, 0xf, true));
  v += __builtin_bit_cast(float, __builtin_amdgcn_update_dpp(0, __builtin_bit_cast(int, v), 0x141, 0xf, 0xf, true));
  return v;
}
DI void ln_stats(const u32x4 (&v)[9], float& mean, float& rstd) {
  float s = 0;
#pragma unroll
  for (int i = 0; i < 9; i++)
#pragma unroll
    for (int j = 0; j < 4; j++) s += bflo(v[i][j]) + bfhi(v[i][j]);
  mean = sum8(s) * (1.0f / D_);
  float q = 0;
#pragma unroll
  for (int i = 0; i < 9; i++)
#pragma unroll
    for (int j = 0; j < 4; j++) { float a = bflo(v[i][j]) - mean, b = bfhi(v[i][j]) - mean; q += a * a + b * b; }
  rstd = __builtin_amdgcn_rsqf(sum8(q) * (1.0f / D_) + LN_EPS);
}

DI int wq_col(int head, int t, int rho) {
  if (t == 0) {
    int hh = (rho >> 2) & 1, reg = (rho & 3) + 4 * (rho >> 3);
    return reg < 8 ? head * 18 + 8 * hh + reg : 576 + head * 18 + 8 * hh + reg - 8;
  }
  if (rho < 18) return 1152 + head * 18 + rho;
  if (rho < 20) return head * 18 + rho - 2;
  if (rho < 22) return 576 + head * 18 + rho - 4;
  return -1;
}

__global__ void __launch_bounds__(512) k_prep(const u16* __restrict__ x, const u16* __restrict__ wqkv, const u16* __restrict__ c,
                                              const u16* __restrict__ wmod, char* __restrict__ ws) {
  __shared__ float red[8][64];
  const int b = blockIdx.x, tid = threadIdx.x, wave = tid >> 6, lane = tid & 63;
  if (b < 54) {
    const int cg = tid & 7, kr = tid >> 3;
    u32x4 w[9]; float sc[9];
#pragma unroll
    for (int i = 0; i < 9; i++) {
      w[i] = *(const u32x4*)(wmod + (size_t)(kr + 64 * i) * 3456 + b * 64 + cg * 8);
      float cv = bf2f(c[kr + 64 * i]);
      sc[i] = rbf(cv * __builtin_amdgcn_rcpf(1.0f + __builtin_amdgcn_exp2f(-cv * LOG2E)));
    }
    float a[8] = {0, 0, 0, 0, 0, 0, 0, 0};
#pragma unroll
    for (int i = 0; i < 9; i++)
#pragma unroll
      for (int j = 0; j < 4; j++) { a[2 * j] += sc[i] * bflo(w[i][j]); a[2 * j + 1] += sc[i] * bfhi(w[i][j]); }
#pragma unroll
    for (int j = 0; j < 8; j++) { a[j] += __shfl_xor(a[j], 8); a[j] += __shfl_xor(a[j], 16); a[j] += __shfl_xor(a[j], 32); }
    if (lane < 8)
#pragma unroll
      for (int j = 0; j < 8; j++) red[wave][lane * 8 + j] = a[j];
    __syncthreads();
    if (tid < 64) {
      float s = 0;
#pragma unroll
      for (int i = 0; i < 8; i++) s += red[i][tid];
      s = rbf(s);
      int col = b * 64 + tid, which = col / 576;
      if (which == 1 || which == 4) s = rbf(1.0f + s);
      ((float*)(ws + WS_MOD))[col] = s;
    }
  }
  if (wave == 7) {
    const int row = 8 * b + (lane >> 3), c8 = lane & 7;
    u32x4 v[9];
#pragma unroll
    for (int i = 0; i < 9; i++) v[i] = *(const u32x4*)(x + (size_t)row * D_ + (8 * i + c8) * 8);
    float mean, rstd; ln_stats(v, mean, rstd);
    char* dst = ws + WS_XN + (size_t)(row >> 5) * 36 * 1024 + (row & 31) * 16;
#pragma unroll
    for (int i = 0; i < 9; i++) {
      u32x4 o;
#pragma unroll
      for (int j = 0; j < 4; j++) o[j] = cvtpk((bflo(v[i][j]) - mean) * rstd, (bfhi(v[i][j]) - mean) * rstd);
      *(u32x4*)(dst + (8 * i + c8) * 512) = o;
    }
  } else {
    for (int f = wave; f < 9; f += 7) {
      int fr = b * 9 + f, head = fr / 72, t = (fr / 36) & 1, s = fr % 36;
      int col = wq_col(head, t, lane & 31);
      u32x4 o = {0, 0, 0, 0};
      if (col >= 0) {
        const u16* p = wqkv + (size_t)(16 * s + 8 * (lane >> 5)) * 1728 + col;
#pragma unroll
        for (int j = 0; j < 4; j++) o[j] = (unsigned)p[(2 * j) * 1728] | ((unsigned)p[(2 * j + 1) * 1728] << 16);
      }
      *(u32x4*)(ws + WS_WQT + (size_t)fr * 1024 + lane * 16) = o;
    }
  }
}

DI int wo_row(int s, int g4, int j) {
  if (s < 16) return (2 * s + (g4 >> 1)) * 18 + (j & 3) + 8 * (j >> 2) + 4 * (g4 & 1);
  return (16 * (s - 16) + 4 * g4 + (j >> 1)) * 18 + 16 + (j & 1);
}
__global__ void __launch_bounds__(256) k_pretile(const u16* __restrict__ wo, const u16* __restrict__ w1, const u16* __restrict__ w2, char* __restrict__ ws) {
  const int lane = threadIdx.x & 63, fr = blockIdx.x * 4 + (threadIdx.x >> 6), g4 = lane >> 4, r = lane & 15;
  u32x4 o = {0, 0, 0, 0};
  if (fr < 5184) {
    int g = fr / 648, ft = (fr / 18) % 36, s = fr % 18;
    const u16* p = w1 + (size_t)(32 * s + 8 * g4) * F_ + g * 576 + ft * 16 + r;
#pragma unroll
    for (int j = 0; j < 4; j++) o[j] = (unsigned)p[(2 * j) * F_] | ((unsigned)p[(2 * j + 1) * F_] << 16);
    *(u32x4*)(ws + WS_W1T + (size_t)fr * 1024 + lane * 16) = o;
  } else if (fr < 10368) {
    int f2 = fr - 5184, g = f2 / 648, dt = (f2 / 18) % 36, s = f2 % 18;
    int col = 32 * (dt >> 1) + 8 * (r >> 2) + 4 * (dt & 1) + (r & 3);
    const u16* p = w2 + (size_t)(g * 576 + 32 * s + 4 * g4) * D_ + col;
    o[0] = (unsigned)p[0] | ((unsigned)p[D_] << 16);
    o[1] = (unsigned)p[2 * D_] | ((unsigned)p[3 * D_] << 16);
    o[2] = (unsigned)p[16 * D_] | ((unsigned)p[17 * D_] << 16);
    o[3] = (unsigned)p[18 * D_] | ((unsigned)p[19 * D_] << 16);
    *(u32x4*)(ws + WS_W2T + (size_t)f2 * 1024 + lane * 16) = o;
  } else if (fr < 10368 + 720) {
    int f3 = fr - 10368, cs = f3 / 90, ct = (f3 / 18) % 5, s = f3 % 18;
    if (16 * ct + r < 72) {
      const u16* p = wo + 72 * cs + 16 * ct + r;
#pragma unroll
      for (int j = 0; j < 4; j++) o[j] = (unsigned)p[wo_row(s, g4, 2 * j) * D_] | ((unsigned)p[wo_row(s, g4, 2 * j + 1) * D_] << 16);
    }
    *(u32x4*)(ws + WS_WOT + (size_t)f3 * 1024 + lane * 16) = o;
  }
}

DI void attn_map(int b, int& head, int& rb) { int x = b & 7, i = b >> 3; head = 8 * (x & 3) + (i & 7); rb = 4 * (x >> 2) + (i >> 3); }

__global__ void __launch_bounds__(512) k_qkv(char* __restrict__ ws) {
  extern __shared__ __attribute__((aligned(16))) char lds[];
  char* wl = lds;
  float* biasp = (float*)(lds + 73728);
  u16* xtr = (u16*)(lds + 73728 + 4096);
  const int tid = threadIdx.x, wave = __builtin_amdgcn_readfirstlane(tid >> 6), lane = tid & 63, h = lane >> 5, r = lane & 31;
  int head, rb; attn_map(blockIdx.x, head, rb);
  const float* mod = (const float*)(ws + WS_MOD);
  const int rt = rb * 8 + wave;
  u32x4 wr[9];
#pragma unroll
  for (int i = 0; i < 9; i++) wr[i] = *(const u32x4*)(ws + WS_WQT + (size_t)(head * 72 + wave * 9 + i) * 1024 + lane * 16);
  u32x4 xf[36];
#pragma unroll
  for (int s = 0; s < 36; s++) xf[s] = *(const u32x4*)(ws + WS_XN + (size_t)(rt * 36 + s) * 1024 + lane * 16);
  float bs = 0;
#pragma unroll
  for (int i = 0; i < 9; i++) {
    int s = 9 * (wave & 3) + i, k0 = 16 * s + 8 * h;
    f32x4 c0 = *(const f32x4*)(mod + 576 + k0), c1 = *(const f32x4*)(mod + 576 + k0 + 4);
    f32x4 s0 = *(const f32x4*)(mod + k0), s1 = *(const f32x4*)(mod + k0 + 4);
    u32x4 o;
    o[0] = cvtpk(bflo(wr[i][0]) * c0[0], bfhi(wr[i][0]) * c0[1]);
    o[1] = cvtpk(bflo(wr[i][1]) * c0[2], bfhi(wr[i][1]) * c0[3]);
    o[2] = cvtpk(bflo(wr[i][2]) * c1[0], bfhi(wr[i][2]) * c1[1]);
    o[3] = cvtpk(bflo(wr[i][3]) * c1[2], bfhi(wr[i][3]) * c1[3]);
    bs += bflo(wr[i][0]) * s0[0] + bfhi(wr[i][0]) * s0[1] + bflo(wr[i][1]) * s0[2] + bfhi(wr[i][1]) * s0[3]
        + bflo(wr[i][2]) * s1[0] + bfhi(wr[i][2]) * s1[1] + bflo(wr[i][3]) * s1[2] + bfhi(wr[i][3]) * s1[3];
    *(u32x4*)(wl + (wave * 9 + i) * 1024 + lane * 16) = o;
  }
  biasp[wave * 64 + lane] = bs;
  __syncthreads();
  f32x16 a0 = {0}, a1 = {0};
#pragma unroll
  for (int s = 0; s < 36; s++) {
    u32x4 w0 = *(const u32x4*)(wl + s * 1024 + lane * 16);
    u32x4 w1 = *(const u32x4*)(wl + (36 + s) * 1024 + lane * 16);
    a0 = mfma32(w0, xf[s], a0);
    a1 = mfma32(xf[s], w1, a1);
  }
  float b0 = 0, b1 = 0;
#pragma unroll
  for (int i = 0; i < 4; i++) { b0 += biasp[i * 64 + r] + biasp[i * 64 + 32 + r]; b1 += biasp[(4 + i) * 64 + r] + biasp[(4 + i) * 64 + 32 + r]; }
#pragma unroll
  for (int g = 0; g < 16; g++) { a0[g] += __shfl(b0, (g & 3) + 8 * (g >> 2) + 4 * h); a1[g] += b1; }
  u32x4 qm, km;
#pragma unroll
  for (int j = 0; j < 4; j++) { qm[j] = cvtpk(a0[2 * j] * QSCALE, a0[2 * j + 1] * QSCALE); km[j] = cvtpk(a0[8 + 2 * j], a0[9 + 2 * j]); }
  char* kvb = ws + WS_KV + (size_t)(head * 8 + rb) * KVBLK;
  *(u32x4*)(ws + WS_QM + (size_t)(head * 64 + rt) * 1024 + lane * 16) = qm;
  *(u32x4*)(kvb + wave * 1024 + lane * 16) = km;
  if (r < 18) {
#pragma unroll
    for (int s = 0; s < 2; s++) {
      u32x4 v;
#pragma unroll
      for (int j = 0; j < 4; j++) v[j] = cvtpk(a1[8 * s + 2 * j], a1[8 * s + 2 * j + 1]);
      *(u32x4*)(kvb + KV_V + wave * 1152 + s * 576 + h * 288 + r * 16) = v;
    }
  } else if (r < 22) {
    float sc = r < 20 ? QSCALE : 1.0f;
    u16* xp = xtr + wave * 128 + ((r - 18) >> 1) * 64 + (r & 1);
#pragma unroll
    for (int g = 0; g < 16; g++) xp[((g & 3) + 8 * (g >> 2) + 4 * h) * 2] = (u16)cvtpk(a1[g] * sc, 0.f);
  }
  __builtin_amdgcn_fence(__ATOMIC_RELEASE, "wavefront");
  __builtin_amdgcn_wave_barrier();
  unsigned xv = *(const unsigned*)(xtr + wave * 128 + lane * 2);
  if (h == 0) *(unsigned*)(ws + WS_QX + (size_t)(head * 2048 + rt * 32 + r) * 4) = xv;
  else *(unsigned*)(kvb + KV_X + (wave * 32 + r) * 4) = xv;
}

DI void attn_scores(const char* kb, int tl, int lane, u32x4 q0, u32x4 q1, u32x2 qx, f32x16& s0, f32x16& s1) {
  u32x4 km = *(const u32x4*)(kb + tl * 1024 + lane * 16);
  unsigned kx = *(const unsigned*)(kb + KV_X + (tl * 32 + (lane & 31)) * 4);
  u32x2 ka = {kx, 0x3f803f80u};
  f32x32 z = {0};
  f32x32 e = __builtin_amdgcn_mfma_f32_32x32x4bf16_1k(__builtin_bit_cast(s16x4, ka), __builtin_bit_cast(s16x4, qx), z, 0, 0, 0);
  f32x16 e0 = __builtin_shufflevector(e, e, 0, 1, 2, 3, 4, 5, 6, 7, 8, 9, 10, 11, 12, 13, 14, 15);
  f32x16 e1 = __builtin_shufflevector(e, e, 16, 17, 18, 19, 20, 21, 22, 23, 24, 25, 26, 27, 28, 29, 30, 31);
  s0 = mfma32(km, q0, e0);
  s1 = mfma32(km, q1, e1);
}
DI float vmax16(const f32x16& s) {
  float m = s[0];
#pragma unroll
  for (int i = 1; i < 16; i++) m = fmaxf(m, s[i]);
  return fmaxf(m, swap32(m));
}
DI unsigned negm_slots(float m) { float hi = rbf(m); return cvtpk(-hi, hi - m); }
DI void attn_pv(const char* kb, int tl, int voff, bool vact, const f32x16& s0, const f32x16& s1, f32x16& o0, f32x16& o1) {
  u32x4 va = {0x3f803f80u, 0x3f803f80u, 0x3f803f80u, 0x3f803f80u}, vb = va;
  if (vact) { va = *(const u32x4*)(kb + KV_V + tl * 1152 + voff); vb = *(const u32x4*)(kb + KV_V + tl * 1152 + 576 + voff); }
  u32x4 p0a, p0b, p1a, p1b;
#pragma unroll
  for (int j = 0; j < 4; j++) {
    p0a[j] = cvtpk(__builtin_amdgcn_exp2f(s0[2 * j]), __builtin_amdgcn_exp2f(s0[2 * j + 1]));
    p0b[j] = cvtpk(__builtin_amdgcn_exp2f(s0[8 + 2 * j]), __builtin_amdgcn_exp2f(s0[9 + 2 * j]));
    p1a[j] = cvtpk(__builtin_amdgcn_exp2f(s1[2 * j]), __builtin_amdgcn_exp2f(s1[2 * j + 1]));
    p1b[j] = cvtpk(__builtin_amdgcn_exp2f(s1[8 + 2 * j]), __builtin_amdgcn_exp2f(s1[9 + 2 * j]));
  }
  o0 = mfma32(va, p0a, o0); o0 = mfma32(vb, p0b, o0);
  o1 = mfma32(va, p1a, o1); o1 = mfma32(vb, p1b, o1);
}
DI bool attn_bad(const f32x16& o, int h) {
  float l = o[10];
  bool ok = l > 1e-30f && l < 1e30f;
#pragma unroll
  for (int i = 0; i < 10; i++) ok = ok && (fabsf(o[i]) < 3e38f);
  return !ok;
}

__global__ void __launch_bounds__(512) k_attn(char* __restrict__ ws) {
  extern __shared__ __attribute__((aligned(16))) char lds[];
  const int tid = threadIdx.x, wave = __builtin_amdgcn_readfirstlane(tid >> 6), lane = tid & 63, h = lane >> 5, r = lane & 31;
  int head, rb; attn_map(blockIdx.x, head, rb);
  const char* kvg = ws + WS_KV + (size_t)head * 8 * KVBLK;
#pragma unroll
  for (int i = 0; i < 18; i++) glds16(kvg + (wave * 18 + i) * 1024 + lane * 16, lds + (wave * 18 + i) * 1024);
  const int qg = wave & 3, kh = wave >> 2, rt0 = rb * 8 + 2 * qg;
  u32x4 q0 = *(const u32x4*)(ws + WS_QM + (size_t)(head * 64 + rt0) * 1024 + lane * 16);
  u32x4 q1 = *(const u32x4*)(ws + WS_QM + (size_t)(head * 64 + rt0 + 1) * 1024 + lane * 16);
  u32x2 qx = {*(const unsigned*)(ws + WS_QX + (size_t)(head * 2048 + rt0 * 32 + lane) * 4), 0u};
  __syncthreads();
  const bool vact = r < 18;
  const int voff = h * 288 + r * 16;
  f32x16 s0, s1, o0 = {0}, o1 = {0};
  attn_scores(lds + rb * KVBLK, 4 * kh, lane, q0, q1, qx, s0, s1);
  float m0 = vmax16(s0), m1 = vmax16(s1);
  qx[1] = negm_slots(h ? m1 : m0);
  for (int t = 0; t < 32; t++) {
    const char* kb = lds + ((rb + (t >> 2)) & 7) * KVBLK;
    int tl = 4 * kh + (t & 3);
    attn_scores(kb, tl, lane, q0, q1, qx, s0, s1);
    attn_pv(kb, tl, voff, vact, s0, s1, o0, o1);
  }
  if (__any(attn_bad(o0, h) || attn_bad(o1, h))) {
    qx[1] = 0; m0 = m1 = -3e38f;
    for (int t = 0; t < 32; t++) {
      attn_scores(lds + (t >> 2) * KVBLK, 4 * kh + (t & 3), lane, q0, q1, qx, s0, s1);
      m0 = fmaxf(m0, vmax16(s0)); m1 = fmaxf(m1, vmax16(s1));
    }
    o0 = f32x16{0}; o1 = f32x16{0};
    for (int t = 0; t < 32; t++) {
      const char* kb = lds + (t >> 2) * KVBLK;
      attn_scores(kb, 4 * kh + (t & 3), lane, q0, q1, qx, s0, s1);
      s0 -= m0; s1 -= m1;
      attn_pv(kb, 4 * kh + (t & 3), voff, vact, s0, s1, o0, o1);
    }
  } else {
    float hi0 = rbf(m0), hi1 = rbf(m1);
    m0 = hi0 - rbf(hi0 - m0); m1 = hi1 - rbf(hi1 - m1);
  }
  __syncthreads();
  float* xb = (float*)lds + wave * 12 * 64 + lane;
  {
    const f32x16& og = kh ? o0 : o1;
#pragma unroll
    for (int i = 0; i < 11; i++) xb[i * 64] = og[i];
    xb[11 * 64] = kh ? m0 : m1;
  }
  __syncthreads();
  const float* pb = (const float*)lds + (wave ^ 4) * 12 * 64 + lane;
  const f32x16& ok = kh ? o1 : o0;
  float ma = kh ? m1 : m0, mb = pb[11 * 64], mm = fmaxf(ma, mb);
  float fa = __builtin_amdgcn_exp2f(ma - mm), fb = __builtin_amdgcn_exp2f(mb - mm);
  float inv = 1.0f / (ok[10] * fa + pb[10 * 64] * fb);
  fa *= inv; fb *= inv;
  float o[10];
#pragma unroll
  for (int i = 0; i < 10; i++) o[i] = ok[i] * fa + pb[i * 64] * fb;
  const int row = (rt0 + kh) * 32 + r;
  char* ob = ws + WS_OF + (size_t)(row >> 4) * 18 * 1024 + (row & 15) * 16;
  u32x4 om = {cvtpk(o[0], o[1]), cvtpk(o[2], o[3]), cvtpk(o[4], o[5]), cvtpk(o[6], o[7])};
  *(u32x4*)(ob + (head >> 1) * 1024 + (2 * (head & 1) + h) * 256) = om;
  if (h == 0) *(unsigned*)(ob + (16 + (head >> 4)) * 1024 + ((head >> 2) & 3) * 256 + (head & 3) * 4) = cvtpk(o[8], o[9]);
}

__global__ void __launch_bounds__(512) k_wo(const u16* __restrict__ x, char* __restrict__ ws) {
  extern __shared__ __attribute__((aligned(16))) char lds[];
  float* pt = (float*)(lds + 92160);
  const int tid = threadIdx.x, wave = __builtin_amdgcn_readfirstlane(tid >> 6), lane = tid & 63;
  const int b = blockIdx.x, r64 = 4 * (b & 7) + (b >> 6), cs = (b >> 3) & 7;
  for (int i = wave; i < 90; i += 8) glds16(ws + WS_WOT + (size_t)(cs * 90 + i) * 1024 + lane * 16, lds + i * 1024);
  const int rt = wave & 3, kh = wave >> 2;
  u32x4 of[9];
#pragma unroll
  for (int i = 0; i < 9; i++) of[i] = *(const u32x4*)(ws + WS_OF + (size_t)((r64 * 4 + rt) * 18 + 9 * kh + i) * 1024 + lane * 16);
  __syncthreads();
  f32x4 acc[5];
#pragma unroll
  for (int ct = 0; ct < 5; ct++) acc[ct] = f32x4{0, 0, 0, 0};
#pragma unroll
  for (int i = 0; i < 9; i++)
#pragma unroll
    for (int ct = 0; ct < 5; ct++) acc[ct] = mfma16(*(const u32x4*)(lds + (ct * 18 + 9 * kh + i) * 1024 + lane * 16), of[i], acc[ct]);
#pragma unroll
  for (int ct = 0; ct < 5; ct++) *(f32x4*)(pt + (kh * 64 + rt * 16 + (lane & 15)) * 84 + ct * 16 + (lane >> 4) * 4) = acc[ct];
  __syncthreads();
  const float* ga = (const float*)(ws + WS_MOD) + 2 * 576;
  for (int it = tid; it < 576; it += 512) {
    int row = it / 9, ch = it % 9, col = cs * 72 + ch * 8;
    const float* p = pt + row * 84 + ch * 8;
    f32x4 u0 = *(const f32x4*)p + *(const f32x4*)(p + 64 * 84), u1 = *(const f32x4*)(p + 4) + *(const f32x4*)(p + 64 * 84 + 4);
    f32x4 g0 = *(const f32x4*)(ga + col), g1 = *(const f32x4*)(ga + col + 4);
    size_t off = (size_t)(r64 * 64 + row) * D_ + col;
    u32x4 xv = *(const u32x4*)(x + off), o;
    o[0] = cvtpk(bflo(xv[0]) + g0[0] * u0[0], bfhi(xv[0]) + g0[1] * u0[1]);
    o[1] = cvtpk(bflo(xv[1]) + g0[2] * u0[2], bfhi(xv[1]) + g0[3] * u0[3]);
    o[2] = cvtpk(bflo(xv[2]) + g1[0] * u1[0], bfhi(xv[2]) + g1[1] * u1[1]);
    o[3] = cvtpk(bflo(xv[3]) + g1[2] * u1[2], bfhi(xv[3]) + g1[3] * u1[3]);
    *(u32x4*)(ws + WS_X1 + off * 2) = o;
  }
}

DI void ffn_map(int b, int& r64, int& g) { int x = b & 7, i = b >> 3; g = 2 * (x & 3) + (i & 1); r64 = 16 * (x >> 2) + (i >> 1); }

template <int NT>
DI void ffn_gemm(const char* wt, const char* bl0, const char* bl1, f32x4 (&acc)[5][4], const int (&tile)[5]) {
  constexpr int PD = 3;
  u32x4 wr[PD][NT];
#pragma unroll
  for (int p = 0; p < PD; p++)
#pragma unroll
    for (int t = 0; t < NT; t++) wr[p][t] = *(const u32x4*)(wt + (tile[t] * 18 + p) * 1024);
#pragma unroll
  for (int s = 0; s < 18; s++) {
    u32x4 bf[4];
#pragma unroll
    for (int rt = 0; rt < 4; rt++) bf[rt] = *(const u32x4*)(((s & 1) ? bl1 : bl0) + (s * 4 + rt) * 1024);
#pragma unroll
    for (int t = 0; t < NT; t++) {
#pragma unroll
      for (int rt = 0; rt < 4; rt++) acc[t][rt] = mfma16(wr[s % PD][t], bf[rt], acc[t][rt]);
      if (s + PD < 18) wr[s % PD][t] = *(const u32x4*)(wt + (tile[t] * 18 + s + PD) * 1024);
    }
  }
}
template <int NT>
DI void ffn_body(char* ws, char* lds, int r64, int g, int wave, int lane) {
  char* h2 = lds; char* hb = lds + 73728;
  int tile[5];
#pragma unroll
  for (int t = 0; t < 4; t++) tile[t] = 4 * wave + t;
  tile[4] = 32 + wave;
  f32x4 acc[5][4];
#pragma unroll
  for (int t = 0; t < NT; t++)
#pragma unroll
    for (int rt = 0; rt < 4; rt++) acc[t][rt] = f32x4{0, 0, 0, 0};
  const int r = lane & 15, g4 = lane >> 4;
  ffn_gemm<NT>(ws + WS_W1T + (size_t)g * 648 * 1024 + lane * 16, h2 + (g4 * 16 + (r ^ g4)) * 16, h2 + (g4 * 16 + (r ^ (4 + g4))) * 16, acc, tile);
#pragma unroll
  for (int t = 0; t < NT; t++)
#pragma unroll
    for (int rt = 0; rt < 4; rt++) {
      float y[4];
#pragma unroll
      for (int i = 0; i < 4; i++) y[i] = acc[t][rt][i] * __builtin_amdgcn_rcpf(1.0f + __builtin_amdgcn_exp2f(acc[t][rt][i]));
      u32x2 o = {cvtpk(y[0], y[1]), cvtpk(y[2], y[3])};
      *(u32x2*)(hb + ((tile[t] >> 1) * 4 + rt) * 1024 + lane * 16 + (tile[t] & 1) * 8) = o;
      acc[t][rt] = f32x4{0, 0, 0, 0};
    }
  __syncthreads();
  ffn_gemm<NT>(ws + WS_W2T + (size_t)g * 648 * 1024 + lane * 16, hb + lane * 16, hb + lane * 16, acc, tile);
  char* pp = ws + WS_PART + (size_t)(r64 * 8 + g) * 72 * 1024 + lane * 16;
#pragma unroll
  for (int rt = 0; rt < 4; rt++) {
#pragma unroll
    for (int u = 0; u < 2; u++) {
      u32x4 o = {cvtpk(acc[2 * u][rt][0], acc[2 * u][rt][1]), cvtpk(acc[2 * u][rt][2], acc[2 * u][rt][3]),
                 cvtpk(acc[2 * u + 1][rt][0], acc[2 * u + 1][rt][1]), cvtpk(acc[2 * u + 1][rt][2], acc[2 * u + 1][rt][3])};
      *(u32x4*)(pp + ((2 * wave + u) * 4 + rt) * 1024) = o;
    }
    if (NT == 5) {
      u32x2 o = {cvtpk(acc[4][rt][0], acc[4][rt][1]), cvtpk(acc[4][rt][2], acc[4][rt][3])};
      *(u32x2*)(pp + ((16 + (wave >> 1)) * 4 + rt) * 1024 + (wave & 1) * 8) = o;
    }
  }
}
__global__ void __launch_bounds__(512) k_ffn(char* __restrict__ ws) {
  extern __shared__ __attribute__((aligned(16))) char lds[];
  const int tid = threadIdx.x, wave = __builtin_amdgcn_readfirstlane(tid >> 6), lane = tid & 63;
  int r64, g; ffn_map(blockIdx.x, r64, g);
  {
    const int rl = 8 * wave + (lane >> 3), c8 = lane & 7;
    const float* mod = (const float*)(ws + WS_MOD);
    u32x4 v[9];
#pragma unroll
    for (int i = 0; i < 9; i++) v[i] = *(const u32x4*)(ws + WS_X1 + (size_t)(r64 * 64 + rl) * 1152 + (8 * i + c8) * 16);
    float mean, rstd; ln_stats(v, mean, rstd);
    rstd *= -LOG2E;
#pragma unroll
    for (int i = 0; i < 9; i++) {
      int c = 8 * i + c8;
      f32x4 c0 = *(const f32x4*)(mod + 4 * 576 + c * 8), c1 = *(const f32x4*)(mod + 4 * 576 + c * 8 + 4);
      f32x4 s0 = *(const f32x4*)(mod + 3 * 576 + c * 8), s1 = *(const f32x4*)(mod + 3 * 576 + c * 8 + 4);
      u32x4 o;
      o[0] = cvtpk((bflo(v[i][0]) - mean) * rstd * c0[0] - LOG2E * s0[0], (bfhi(v[i][0]) - mean) * rstd * c0[1] - LOG2E * s0[1]);
      o[1] = cvtpk((bflo(v[i][1]) - mean) * rstd * c0[2] - LOG2E * s0[2], (bfhi(v[i][1]) - mean) * rstd * c0[3] - LOG2E * s0[3]);
      o[2] = cvtpk((bflo(v[i][2]) - mean) * rstd * c1[0] - LOG2E * s1[0], (bfhi(v[i][2]) - mean) * rstd * c1[1] - LOG2E * s1[1]);
      o[3] = cvtpk((bflo(v[i][3]) - mean) * rstd * c1[2] - LOG2E * s1[2], (bfhi(v[i][3]) - mean) * rstd * c1[3] - LOG2E * s1[3]);
      *(u32x4*)(lds + (((c >> 2) * 4 + (rl >> 4)) * 64 + (c & 3) * 16 + ((rl & 15) ^ c8)) * 16) = o;
    }
  }
  __syncthreads();
  if (wave < 4) ffn_body<5>(ws, lds, r64, g, wave, lane); else ffn_body<4>(ws, lds, r64, g, wave, lane);
}

__global__ void __launch_bounds__(512) k_red(char* __restrict__ ws, u16* __restrict__ out) {
  const int tid = threadIdx.x, wave = tid >> 6, lane = tid & 63;
  const int r64 = blockIdx.x >> 3, g = blockIdx.x & 7;
  const float* gb = (const float*)(ws + WS_MOD) + 5 * 576;
  for (int ci = wave; ci < 9; ci += 8) {
    int ch = g * 9 + ci, u = ch >> 2, rt = ch & 3;
    float a[8] = {0, 0, 0, 0, 0, 0, 0, 0};
    u32x4 p[8];
#pragma unroll
    for (int i = 0; i < 8; i++) p[i] = *(const u32x4*)(ws + WS_PART + (size_t)((r64 * 8 + i) * 72 + ch) * 1024 + lane * 16);
    int row = r64 * 64 + rt * 16 + (lane & 15), col = 32 * u + 8 * (lane >> 4);
    u32x4 xv = *(const u32x4*)(ws + WS_X1 + ((size_t)row * D_ + col) * 2);
    f32x4 g0 = *(const f32x4*)(gb + col), g1 = *(const f32x4*)(gb + col + 4);
#pragma unroll
    for (int i = 0; i < 8; i++)
#pragma unroll
      for (int j = 0; j < 4; j++) { a[2 * j] += bflo(p[i][j]); a[2 * j + 1] += bfhi(p[i][j]); }
    constexpr float NL = -0.6931471805599453f;
    u32x4 o;
    o[0] = cvtpk(bflo(xv[0]) + g0[0] * NL * a[0], bfhi(xv[0]) + g0[1] * NL * a[1]);
    o[1] = cvtpk(bflo(xv[1]) + g0[2] * NL * a[2], bfhi(xv[1]) + g0[3] * NL * a[3]);
    o[2] = cvtpk(bflo(xv[2]) + g1[0] * NL * a[4], bfhi(xv[2]) + g1[1] * NL * a[5]);
    o[3] = cvtpk(bflo(xv[3]) + g1[2] * NL * a[6], bfhi(xv[3]) + g1[3] * NL * a[7]);
    *(u32x4*)(out + (size_t)row * D_ + col) = o;
  }
}

extern "C" void kernel_launch(void* const* d_in, const int* in_sizes, int n_in, void* d_out, int out_size, void* d_ws, size_t ws_size,
                              hipStream_t stream) {
  const u16* x = (const u16*)d_in[0];
  const u16* wqkv = (const u16*)d_in[1];
  const u16* wo = (const u16*)d_in[2];
  const u16* w1 = (const u16*)d_in[4];
  const u16* w2 = (const u16*)d_in[5];
  const u16* c = (const u16*)d_in[6];
  const u16* wmod = (const u16*)d_in[7];
  char* ws = (char*)d_ws;
  (void)hipFuncSetAttribute((const void*)k_qkv, hipFuncAttributeMaxDynamicSharedMemorySize, 81920);
  (void)hipFuncSetAttribute((const void*)k_attn, hipFuncAttributeMaxDynamicSharedMemorySize, 147456);
  (void)hipFuncSetAttribute((const void*)k_wo, hipFuncAttributeMaxDynamicSharedMemorySize, 92160 + 43008);
  (void)hipFuncSetAttribute((const void*)k_ffn, hipFuncAttributeMaxDynamicSharedMemorySize, 147456);
  k_prep<<<256, 512, 0, stream>>>(x, wqkv, c, wmod, ws);
  k_pretile<<<(10368 + 720) / 4, 256, 0, stream>>>(wo, w1, w2, ws);
  k_qkv<<<256, 512, 81920, stream>>>(ws);
  k_attn<<<256, 512, 147456, stream>>>(ws);
  k_wo<<<256, 512, 92160 + 43008, stream>>>(x, ws);
  k_ffn<<<256, 512, 147456, stream>>>(ws);
  k_red<<<256, 512, 0, stream>>>(ws, (u16*)d_out);
}
```

```cpp
#include <hip/hip_runtime.h>
typedef unsigned short u16;

__global__ void __launch_bounds__(512, 2) k2(const u16* __restrict__ x, const u16* __restrict__ wq, const u16* __restrict__ wo,
                                             const u16* __restrict__ w1, const u16* __restrict__ w2, const u16* __restrict__ c,
                                             const u16* __restrict__ wm, u16* __restrict__ out, char* __restrict__ ws) {
  asm volatile(
    "s_nop 1\n"
    "s_load_dwordx16 s[4:19], %[ka], 0x0\n"
    "s_load_dwordx2 s[20:21], %[ka], 0x40\n"
    "v_lshrrev_b32 v8, 6, v0\n"
    "v_and_b32 v2, 63, v0\n"
    "s_getpc_b64 s[28:29]\n"
    "s_mov_b32 s30, 0xf6f8\n"
    "s_mov_b32 s31, 0x20000\n"
    "s_andn2_b32 s41, %[bid], 7\n"
    "v_add_u32 v9, s41, v8\n"
    "v_lshl_add_u32 v9, v2, 8, v9\n"
    "v_lshlrev_b32 v9, 7, v9\n"
    "buffer_load_dword v255, v9, s[28:31], 0 offen\n"
    "v_readfirstlane_b32 s36, v8\n"
    "s_mov_b32 s22, 46608384\n"
    "s_mov_b32 s23, 0x20000\n"
    "v_lshlrev_b32 v1, 4, v2\n"
    "s_and_b32 s37, %[bid], 3\n"
    "s_lshl_b32 s37, s37, 1\n"
    "s_bfe_u32 s41, %[bid], 0x10003\n"
    "s_or_b32 s37, s37, s41\n"
    "s_bfe_u32 s38, %[bid], 0x10002\n"
    "s_lshl_b32 s38, s38, 4\n"
    "s_lshr_b32 s41, %[bid], 4\n"
    "s_or_b32 s38, s38, s41\n"
    "s_and_b32 s39, s36, 3\n"
    "s_lshr_b32 s40, s36, 2\n"
    "v_mov_b32 v11, 0x40000000\n"
    "s_and_b32 s44, s38, 15\n"
    "s_lshr_b32 s45, s38, 4\n"
    "s_cmp_lt_u32 s44, 7\n"
    "s_cbranch_scc1 .Lk_m_1\n"
    "s_mul_i32 s46, s45, 9\n"
    "s_add_u32 s46, s46, s44\n"
    "s_sub_u32 s46, s46, 7\n"
    "s_mul_i32 s48, s37, 144\n"
    "s_mul_i32 s47, s46, 110592\n"
    "s_add_u32 s47, s47, s48\n"
    "s_lshl_b32 s49, s36, 7\n"
    "v_add_u32 v16, s49, v2\n"
    "s_lshl_b32 s49, s36, 11\n"
    "s_add_u32 m0, s49, 92160\n"
    "s_movk_i32 s49, 1152\n"
    "v_mul_u32_u24 v17, 7282, v16\n"
    "v_lshrrev_b32 v17, 16, v17\n"
    "v_mad_i32_i24 v18, v17, -9, v16\n"
    "v_lshrrev_b32 v19, 5, v17\n"
    "v_and_b32 v17, 31, v17\n"
    "v_mul_u32_u24 v17, 3456, v17\n"
    "v_mad_u32_u24 v17, v19, s49, v17\n"
    "v_lshl_add_u32 v17, v18, 4, v17\n"
    "v_cmp_gt_u32 vcc, 864, v16\n"
    "s_nop 1\n"
    "v_cndmask_b32 v20, v11, v17, vcc\n"
    "v_add_u32 v16, 64, v16\n"
    "v_mul_u32_u24 v17, 7282, v16\n"
    "v_lshrrev_b32 v17, 16, v17\n"
    "v_mad_i32_i24 v18, v17, -9, v16\n"
    "v_lshrrev_b32 v19, 5, v17\n"
    "v_and_b32 v17, 31, v17\n"
    "v_mul_u32_u24 v17, 3456, v17\n"
    "v_mad_u32_u24 v17, v19, s49, v17\n"
    "v_lshl_add_u32 v17, v18, 4, v17\n"
    "v_cmp_gt_u32 vcc, 864, v16\n"
    "s_nop 1\n"
    "v_cndmask_b32 v21, v11, v17, vcc\n"
    "s_mov_b32 s30, 1990656\n"
    "s_mov_b32 s31, 0x20000\n"
    "s_waitcnt lgkmcnt(0)\n"
    "s_mov_b64 s[28:29], s[6:7]\n"
    "buffer_load_dwordx4 v20, s[28:31], s47 offen lds\n"
    "s_add_u32 m0, m0, 1024\n"
    "s_nop 0\n"
    "buffer_load_dwordx4 v21, s[28:31], s47 offen lds\n"
    "s_branch .Lk_x_2\n"
    ".Lk_m_1:\n"
    "s_mul_i32 s46, s45, 56\n"
    "s_add_u32 s46, s46, s44\n"
    "s_mul_i32 s47, s37, 7\n"
    "s_add_u32 s46, s46, s47\n"
    "s_cmp_ge_u32 s46, 108\n"
    "s_cbranch_scc1 .Lk_x_2\n"
    "v_lshrrev_b32 v16, 2, v2\n"
    "v_and_b32 v17, 3, v2\n"
    "v_lshlrev_b32 v18, 1, v16\n"
    "v_cmp_gt_u32 vcc, 32, v2\n"
    "s_nop 1\n"
    "v_cndmask_b32 v19, v11, v18, vcc\n"
    "s_mul_i32 s48, s36, 144\n"
    "v_mul_u32_u24 v16, 6912, v16\n"
    "v_lshl_add_u32 v16, v17, 4, v16\n"
    "v_cndmask_b32 v17, v11, v16, vcc\n"
    "s_lshl_b32 s49, s46, 6\n"
    "s_mul_i32 s47, s36, 497664\n"
    "s_add_u32 s47, s47, s49\n"
    "s_mov_b32 s34, 1152\n"
    "s_mov_b32 s35, 0x20000\n"
    "s_waitcnt lgkmcnt(0)\n"
    "s_mov_b64 s[32:33], s[14:15]\n"
    "s_mov_b32 s30, 3981312\n"
    "s_mov_b32 s31, 0x20000\n"
    "s_mov_b64 s[28:29], s[16:17]\n"
    "buffer_load_ushort v64, v18, s[32:35], s48 offen offset:0\n"
    "buffer_load_ushort v65, v18, s[32:35], s48 offen offset:32\n"
    "buffer_load_ushort v66, v18, s[32:35], s48 offen offset:64\n"
    "buffer_load_ushort v67, v18, s[32:35], s48 offen offset:96\n"
    "buffer_load_ushort v68, v19, s[32:35], s48 offen offset:128\n"
    "buffer_load_dwordx4 v[72:75], v16, s[28:31], s47 offen\n"
    "s_add_u32 s47, s47, 110592\n"
    "buffer_load_dwordx4 v[76:79], v16, s[28:31], s47 offen\n"
    "s_add_u32 s47, s47, 110592\n"
    "buffer_load_dwordx4 v[80:83], v16, s[28:31], s47 offen\n"
    "s_add_u32 s47, s47, 110592\n"
    "buffer_load_dwordx4 v[84:87], v16, s[28:31], s47 offen\n"
    "s_add_u32 s47, s47, 110592\n"
    "buffer_load_dwordx4 v[88:91], v17, s[28:31], s47 offen\n"
    ".Lk_x_2:\n"
    "s_mov_b32 s60, 253952\n"
    "s_waitcnt lgkmcnt(0)\n"
    "buffer_load_dword v15, off, s[20:23], s60 sc1\n"
    "s_cmp_lg_u32 s36, 7\n"
    "s_cbranch_scc1 .Lk_tl_6\n"
    "s_lshl_b32 s47, s37, 5\n"
    "s_add_u32 s47, s47, s38\n"
    "s_lshl_b32 s47, s47, 7\n"
    "v_lshlrev_b32 v126, 15, v2\n"
    "v_mov_b32 v127, 163840\n"
    "v_cmp_gt_u32 vcc, 2, v2\n"
    "s_nop 1\n"
    "v_cndmask_b32 v126, v127, v126, vcc\n"
    "v_cmp_gt_u32 vcc, 3, v2\n"
    "s_nop 1\n"
    "v_cndmask_b32 v126, v11, v126, vcc\n"
    "buffer_load_dword v127, v126, s[20:23], s47 offen sc1\n"
    ".Lk_tl_6:\n"
    "s_mov_b32 s26, 2359296\n"
    "s_mov_b32 s27, 0x20000\n"
    "s_mov_b64 s[24:25], s[4:5]\n"
    "v_lshrrev_b32 v16, 3, v2\n"
    "v_and_b32 v17, 7, v2\n"
    "v_mul_u32_u24 v12, 1152, v16\n"
    "v_lshl_add_u32 v12, v17, 4, v12\n"
    "s_lshl_b32 s50, s38, 3\n"
    "s_add_u32 s50, s50, s36\n"
    "s_mul_i32 s50, s50, 9216\n"
    "v_mul_u32_u24 v18, 1040, v17\n"
    "v_lshl_add_u32 v18, v16, 4, v18\n"
    "s_lshl_b32 s47, s36, 7\n"
    "v_add_u32 v13, s47, v18\n"
    "v_add_u32 v25, 33280, v13\n"
    "v_lshlrev_b32 v17, 4, v17\n"
    "v_add_u32 v14, 81920, v17\n"
    "v_lshlrev_b32 v6, 2, v2\n"
    "s_cmp_lt_u32 s44, 7\n"
    "s_cbranch_scc1 .Lk_m2_3\n"
    "s_cmp_lt_u32 s36, 3\n"
    "s_cbranch_scc1 .Lk_p_7\n"
    "s_mov_b32 s30, 663552\n"
    "s_mov_b32 s31, 0x20000\n"
    "s_mov_b64 s[28:29], s[8:9]\n"
    "s_sub_u32 s49, s36, 3\n"
    "s_lshl_b32 s47, s49, 6\n"
    "v_add_u32 v16, s47, v2\n"
    "s_lshl_b32 s49, s49, 10\n"
    "s_add_u32 m0, s49, 147456\n"
    "v_mul_u32_u24 v17, 7282, v16\n"
    "v_lshrrev_b32 v17, 16, v17\n"
    "v_mad_i32_i24 v18, v17, -9, v16\n"
    "s_cmp_lt_u32 s46, 16\n"
    "s_cbranch_scc1 .Lk_p_8\n"
    "v_lshrrev_b32 v19, 1, v17\n"
    "v_and_b32 v20, 1, v17\n"
    "v_mad_u32_u24 v20, v19, 18, v20\n"
    "v_and_b32 v19, 1, v19\n"
    "v_xor_b32 v19, 1, v19\n"
    "v_lshl_add_u32 v17, v19, 4, v20\n"
    "s_mul_i32 s47, s46, 331776\n"
    "s_add_u32 s47, s47, -5308416\n"
    "s_branch .Lk_p_9\n"
    ".Lk_p_8:\n"
    "v_lshrrev_b32 v19, 4, v17\n"
    "v_and_b32 v20, 3, v17\n"
    "v_mad_u32_u24 v20, v19, 20, v20\n"
    "v_bfe_u32 v19, v17, 2, 1\n"
    "v_lshl_add_u32 v20, v19, 3, v20\n"
    "v_bfe_u32 v19, v17, 3, 1\n"
    "v_lshl_add_u32 v17, v19, 2, v20\n"
    "s_mul_i32 s47, s46, 41472\n"
    ".Lk_p_9:\n"
    "v_mul_u32_u24 v17, 1152, v17\n"
    "v_lshl_add_u32 v17, v18, 4, v17\n"
    "v_cmp_gt_u32 vcc, 288, v16\n"
    "s_nop 1\n"
    "v_cndmask_b32 v21, v11, v17, vcc\n"
    "s_add_u32 s61, s47, s48\n"
    ".Lk_p_7:\n"
    "v_bfe_u32 v16, v2, 2, 2\n"
    "v_lshrrev_b32 v17, 5, v2\n"
    "v_lshl_add_u32 v17, v17, 3, v16\n"
    "v_mul_u32_u24 v22, 144, v17\n"
    "v_lshrrev_b32 v17, 4, v2\n"
    "v_lshl_add_u32 v17, v17, 3, v16\n"
    "v_mul_u32_u24 v23, 144, v17\n"
    "v_and_b32 v16, 3, v2\n"
    "v_bfe_u32 v17, v2, 4, 1\n"
    "s_lshr_b32 s47, s39, 1\n"
    "s_mul_i32 s47, s47, 72\n"
    "s_add_u32 s47, s47, 92160\n"
    "s_and_b32 s48, s39, 1\n"
    "s_mul_i32 s48, s48, 40\n"
    "s_add_u32 s48, s48, s47\n"
    "s_cmp_eq_u32 s40, 0\n"
    "s_cbranch_scc1 .Lk_p_10\n"
    "v_subrev_u32 v18, 1, v16\n"
    "v_min_u32 v18, 2, v18\n"
    "v_mul_u32_u24 v18, 4608, v18\n"
    "s_add_u32 s47, s47, 32\n"
    "v_add_u32 v18, s47, v18\n"
    "s_add_u32 s48, s48, 9216\n"
    "v_lshl_add_u32 v19, v16, 3, s48\n"
    "v_cmp_eq_u32 vcc, 0, v17\n"
    "s_nop 1\n"
    "v_cndmask_b32 v18, v18, v19, vcc\n"
    "v_subrev_u32 v20, 64, v1\n"
    "v_xor_b32 v20, 32, v20\n"
    "s_mov_b32 vcc_lo, 0xffffff\n"
    "s_mov_b32 vcc_hi, vcc_lo\n"
    "v_cndmask_b32 v20, v20, v1, vcc\n"
    "s_and_b32 s49, s39, 1\n"
    "s_lshl_b32 s49, s49, 1\n"
    "s_lshl_b32 vcc_lo, 0x3300000, s49\n"
    "s_or_b32 vcc_lo, vcc_lo, 0xfffff\n"
    "s_mov_b32 vcc_hi, vcc_lo\n"
    "v_cndmask_b32 v20, v11, v20, vcc\n"
    "s_branch .Lk_p_11\n"
    ".Lk_p_10:\n"
    "v_mov_b32 v20, v1\n"
    "v_mul_u32_u24 v18, 4608, v17\n"
    "v_and_b32 v19, 1, v16\n"
    "v_lshl_add_u32 v18, v19, 4, v18\n"
    "v_lshrrev_b32 v19, 1, v16\n"
    "v_lshl_add_u32 v18, v19, 3, v18\n"
    "v_add_u32 v18, s48, v18\n"
    ".Lk_p_11:\n"
    "v_add_u32 v22, v22, v18\n"
    "s_lshr_b32 s47, s36, 1\n"
    "s_lshl_b32 s47, s47, 6\n"
    "s_and_b32 s48, s36, 1\n"
    "s_mul_i32 s48, s48, 8\n"
    "s_add_u32 s48, s48, s47\n"
    "s_add_u32 s48, s48, 147456\n"
    "s_cmp_lt_u32 s36, 4\n"
    "s_cselect_b32 s47, 3, 1\n"
    "s_cselect_b32 s49, 4, 3\n"
    "v_and_b32 v16, s47, v16\n"
    "v_lshlrev_b32 v16, s49, v16\n"
    "v_add3_u32 v23, v23, v16, s48\n"
    "s_lshl_b32 s47, s37, 3\n"
    "s_add_u32 s47, s47, s36\n"
    "s_mul_i32 s47, s47, 18\n"
    "s_add_u32 s47, s47, s46\n"
    "s_mul_i32 s47, s47, 2048\n"
    "s_add_u32 s47, s47, 258048\n"
    "s_waitcnt vmcnt(1)\n"
    "s_barrier\n"
    "ds_read_b64_tr_b16 v[64:65], v22 offset:0\n"
    "ds_read_b64_tr_b16 v[66:67], v22 offset:576\n"
    "ds_read_b64_tr_b16 v[68:69], v22 offset:2304\n"
    "ds_read_b64_tr_b16 v[70:71], v22 offset:2880\n"
    "s_waitcnt lgkmcnt(2)\n"
    "buffer_store_dwordx4 v[64:67], v20, s[20:23], s47 offen sc1\n"
    "s_waitcnt lgkmcnt(0)\n"
    "buffer_store_dwordx4 v[68:71], v20, s[20:23], s47 offen offset:1024 sc1\n"
    "buffer_load_dwordx4 v[28:31], v12, s[24:27], s50 offen\n"
    "buffer_load_dwordx4 v[32:35], v12, s[24:27], s50 offen offset:128\n"
    "buffer_load_dwordx4 v[36:39], v12, s[24:27], s50 offen offset:256\n"
    "v_lshlrev_b32 v16, 7, v2\n"
    "v_cmp_gt_u32 vcc, 32, v2\n"
    "s_nop 1\n"
    "v_cndmask_b32 v16, v11, v16, vcc\n"
    "s_lshl_b32 s47, s37, 12\n"
    "s_mul_i32 s48, s46, 4\n"
    "s_add_u32 s48, s48, s47\n"
    "s_add_u32 s48, s48, 163840\n"
    "s_waitcnt vmcnt(5)\n"
    "v_readfirstlane_b32 s95, v15\n"
    "s_mul_i32 s95, s95, 0x9e3779b1\n"
    "s_add_u32 s95, s95, 0x7f4a7c15\n"
    "v_mov_b32 v17, s95\n"
    "s_mov_b32 s59, 0x3f803f80\n"
    "s_waitcnt vmcnt(3)\n"
    "s_barrier\n"
    "s_cmp_lg_u32 s36, 0\n"
    "s_cbranch_scc1 .Lk_p_12\n"
    "buffer_store_dword v17, v16, s[20:23], s48 offen sc1\n"
    ".Lk_p_12:\n"
    "buffer_load_dwordx4 v[40:43], v12, s[24:27], s50 offen offset:384\n"
    "buffer_load_dwordx4 v[44:47], v12, s[24:27], s50 offen offset:512\n"
    "buffer_load_dwordx4 v[48:51], v12, s[24:27], s50 offen offset:640\n"
    "buffer_load_dwordx4 v[52:55], v12, s[24:27], s50 offen offset:768\n"
    "buffer_load_dwordx4 v[56:59], v12, s[24:27], s50 offen offset:896\n"
    "buffer_load_dwordx4 v[60:63], v12, s[24:27], s50 offen offset:1024\n"
    "s_branch .Lk_mid_4\n"
    ".Lk_m2_3:\n"
    "s_cmp_ge_u32 s46, 108\n"
    "s_cbranch_scc1 .Lk_m_13\n"
    "s_waitcnt vmcnt(10)\n"
    "v_lshlrev_b32 v64, 16, v64\n"
    "s_waitcnt vmcnt(9)\n"
    "v_lshlrev_b32 v65, 16, v65\n"
    "s_waitcnt vmcnt(8)\n"
    "v_lshlrev_b32 v66, 16, v66\n"
    "s_waitcnt vmcnt(7)\n"
    "v_lshlrev_b32 v67, 16, v67\n"
    "s_waitcnt vmcnt(6)\n"
    "v_lshlrev_b32 v68, 16, v68\n"
    "v_mul_f32 v92, 0xbfb8aa3b, v64\n"
    "v_mul_f32 v93, 0xbfb8aa3b, v65\n"
    "v_mul_f32 v94, 0xbfb8aa3b, v66\n"
    "v_mul_f32 v95, 0xbfb8aa3b, v67\n"
    "v_mul_f32 v96, 0xbfb8aa3b, v68\n"
    "v_exp_f32 v92, v92\n"
    "v_exp_f32 v93, v93\n"
    "v_exp_f32 v94, v94\n"
    "v_exp_f32 v95, v95\n"
    "v_exp_f32 v96, v96\n"
    "v_add_f32 v92, 1.0, v92\n"
    "v_add_f32 v93, 1.0, v93\n"
    "v_add_f32 v94, 1.0, v94\n"
    "v_add_f32 v95, 1.0, v95\n"
    "v_add_f32 v96, 1.0, v96\n"
    "v_rcp_f32 v92, v92\n"
    "v_rcp_f32 v93, v93\n"
    "v_rcp_f32 v94, v94\n"
    "v_rcp_f32 v95, v95\n"
    "v_rcp_f32 v96, v96\n"
    "v_mul_f32 v64, v64, v92\n"
    "v_mul_f32 v65, v65, v93\n"
    "v_mul_f32 v66, v66, v94\n"
    "v_mul_f32 v67, v67, v95\n"
    "v_mul_f32 v68, v68, v96\n"
    "v_cvt_pk_bf16_f32 v92, v64, 0\n"
    "v_cvt_pk_bf16_f32 v93, v65, 0\n"
    "v_cvt_pk_bf16_f32 v94, v66, 0\n"
    "v_cvt_pk_bf16_f32 v95, v67, 0\n"
    "v_cvt_pk_bf16_f32 v96, v68, 0\n"
    "v_lshlrev_b32 v97, 16, v92\n"
    "v_lshlrev_b32 v98, 16, v93\n"
    "v_lshlrev_b32 v99, 16, v94\n"
    "v_lshlrev_b32 v100, 16, v95\n"
    "v_lshlrev_b32 v101, 16, v96\n"
    "v_and_b32 v16, 3, v2\n"
    "v_lshrrev_b32 v17, 4, v2\n"
    "v_lshlrev_b32 v16, 5, v16\n"
    "v_lshl_add_u32 v16, v17, 3, v16\n"
    "s_mul_i32 s47, s36, 128\n"
    "s_add_u32 s47, s47, 92160\n"
    "v_add_u32 v16, s47, v16\n"
    "v_and_b32 v18, 15, v2\n"
    "v_lshlrev_b32 v18, 3, v18\n"
    "v_add_u32 v19, 92160, v18\n"
    "v_mul_u32_u24 v20, 13824, v17\n"
    "v_add_u32 v18, v20, v18\n"
    "s_mul_i32 s48, s46, 128\n"
    "s_add_u32 s48, s48, 196608\n"
    "s_waitcnt vmcnt(5)\n"
    "v_dot2_f32_bf16 v104, v72, v92, 0\n"
    "v_dot2_f32_bf16 v105, v72, v97, 0\n"
    "v_dot2_f32_bf16 v106, v73, v92, 0\n"
    "v_dot2_f32_bf16 v107, v73, v97, 0\n"
    "v_dot2_f32_bf16 v108, v74, v92, 0\n"
    "v_dot2_f32_bf16 v109, v74, v97, 0\n"
    "v_dot2_f32_bf16 v110, v75, v92, 0\n"
    "v_dot2_f32_bf16 v111, v75, v97, 0\n"
    "s_waitcnt vmcnt(4)\n"
    "v_dot2_f32_bf16 v104, v76, v93, v104\n"
    "v_dot2_f32_bf16 v105, v76, v98, v105\n"
    "v_dot2_f32_bf16 v106, v77, v93, v106\n"
    "v_dot2_f32_bf16 v107, v77, v98, v107\n"
    "v_dot2_f32_bf16 v108, v78, v93, v108\n"
    "v_dot2_f32_bf16 v109, v78, v98, v109\n"
    "v_dot2_f32_bf16 v110, v79, v93, v110\n"
    "v_dot2_f32_bf16 v111, v79, v98, v111\n"
    "s_waitcnt vmcnt(3)\n"
    "v_dot2_f32_bf16 v104, v80, v94, v104\n"
    "v_dot2_f32_bf16 v105, v80, v99, v105\n"
    "v_dot2_f32_bf16 v106, v81, v94, v106\n"
    "v_dot2_f32_bf16 v107, v81, v99, v107\n"
    "v_dot2_f32_bf16 v108, v82, v94, v108\n"
    "v_dot2_f32_bf16 v109, v82, v99, v109\n"
    "v_dot2_f32_bf16 v110, v83, v94, v110\n"
    "v_dot2_f32_bf16 v111, v83, v99, v111\n"
    "s_waitcnt vmcnt(2)\n"
    "v_dot2_f32_bf16 v104, v84, v95, v104\n"
    "v_dot2_f32_bf16 v105, v84, v100, v105\n"
    "v_dot2_f32_bf16 v106, v85, v95, v106\n"
    "v_dot2_f32_bf16 v107, v85, v100, v107\n"
    "v_dot2_f32_bf16 v108, v86, v95, v108\n"
    "v_dot2_f32_bf16 v109, v86, v100, v109\n"
    "v_dot2_f32_bf16 v110, v87, v95, v110\n"
    "v_dot2_f32_bf16 v111, v87, v100, v111\n"
    "s_waitcnt vmcnt(1)\n"
    "v_dot2_f32_bf16 v104, v88, v96, v104\n"
    "v_dot2_f32_bf16 v105, v88, v101, v105\n"
    "v_dot2_f32_bf16 v106, v89, v96, v106\n"
    "v_dot2_f32_bf16 v107, v89, v101, v107\n"
    "v_dot2_f32_bf16 v108, v90, v96, v108\n"
    "v_dot2_f32_bf16 v109, v90, v101, v109\n"
    "v_dot2_f32_bf16 v110, v91, v96, v110\n"
    "v_dot2_f32_bf16 v111, v91, v101, v111\n"
    "buffer_load_dwordx4 v[28:31], v12, s[24:27], s50 offen\n"
    "buffer_load_dwordx4 v[32:35], v12, s[24:27], s50 offen offset:128\n"
    "buffer_load_dwordx4 v[36:39], v12, s[24:27], s50 offen offset:256\n"
    "v_permlane32_swap_b32 v104, v108\n"
    "v_permlane32_swap_b32 v105, v109\n"
    "v_permlane32_swap_b32 v106, v110\n"
    "v_permlane32_swap_b32 v107, v111\n"
    "v_add_f32 v104, v104, v108\n"
    "v_add_f32 v105, v105, v109\n"
    "v_add_f32 v106, v106, v110\n"
    "v_add_f32 v107, v107, v111\n"
    "s_nop 0\n"
    "v_permlane16_swap_b32 v104, v106\n"
    "v_permlane16_swap_b32 v105, v107\n"
    "v_add_f32 v104, v104, v106\n"
    "v_add_f32 v105, v105, v107\n"
    "s_nop 0\n"
    "v_add_f32_dpp v104, v104, v104 row_ror:8 row_mask:0xf bank_mask:0xf\n"
    "v_add_f32_dpp v105, v105, v105 row_ror:8 row_mask:0xf bank_mask:0xf\n"
    "s_nop 0\n"
    "v_add_f32_dpp v104, v104, v104 row_ror:4 row_mask:0xf bank_mask:0xf\n"
    "v_add_f32_dpp v105, v105, v105 row_ror:4 row_mask:0xf bank_mask:0xf\n"
    "ds_write_b64 v16, v[104:105]\n"
    "s_waitcnt vmcnt(3)\n"
    "v_readfirstlane_b32 s95, v15\n"
    "s_mul_i32 s95, s95, 0x9e3779b1\n"
    "s_add_u32 s95, s95, 0x7f4a7c15\n"
    "s_waitcnt lgkmcnt(0)\n"
    "s_barrier\n"
    "s_cmp_lg_u32 s36, 0\n"
    "s_cbranch_scc1 .Lk_m_14\n"
    "ds_read_b64 v[64:65], v19\n"
    "ds_read_b64 v[66:67], v19 offset:128\n"
    "ds_read_b64 v[68:69], v19 offset:256\n"
    "ds_read_b64 v[70:71], v19 offset:384\n"
    "ds_read_b64 v[72:73], v19 offset:512\n"
    "ds_read_b64 v[74:75], v19 offset:640\n"
    "ds_read_b64 v[76:77], v19 offset:768\n"
    "ds_read_b64 v[78:79], v19 offset:896\n"
    "v_mov_b32 v104, s95\n"
    "s_waitcnt lgkmcnt(6)\n"
    "v_pk_add_f32 v[64:65], v[64:65], v[66:67]\n"
    "s_waitcnt lgkmcnt(4)\n"
    "v_pk_add_f32 v[68:69], v[68:69], v[70:71]\n"
    "s_waitcnt lgkmcnt(2)\n"
    "v_pk_add_f32 v[72:73], v[72:73], v[74:75]\n"
    "s_waitcnt lgkmcnt(0)\n"
    "v_pk_add_f32 v[76:77], v[76:77], v[78:79]\n"
    "v_pk_add_f32 v[64:65], v[64:65], v[68:69]\n"
    "v_pk_add_f32 v[72:73], v[72:73], v[76:77]\n"
    "v_pk_add_f32 v[64:65], v[64:65], v[72:73]\n"
    "v_cvt_pk_bf16_f32 v105, v64, v65\n"
    "buffer_store_dwordx2 v[104:105], v18, s[20:23], s48 offen sc1\n"
    ".Lk_m_14:\n"
    "buffer_load_dwordx4 v[40:43], v12, s[24:27], s50 offen offset:384\n"
    "buffer_load_dwordx4 v[44:47], v12, s[24:27], s50 offen offset:512\n"
    "buffer_load_dwordx4 v[48:51], v12, s[24:27], s50 offen offset:640\n"
    "buffer_load_dwordx4 v[52:55], v12, s[24:27], s50 offen offset:768\n"
    "buffer_load_dwordx4 v[56:59], v12, s[24:27], s50 offen offset:896\n"
    "buffer_load_dwordx4 v[60:63], v12, s[24:27], s50 offen offset:1024\n"
    "s_branch .Lk_ln_5\n"
    ".Lk_m_13:\n"
    "buffer_load_dwordx4 v[28:31], v12, s[24:27], s50 offen\n"
    "buffer_load_dwordx4 v[32:35], v12, s[24:27], s50 offen offset:128\n"
    "buffer_load_dwordx4 v[36:39], v12, s[24:27], s50 offen offset:256\n"
    "buffer_load_dwordx4 v[40:43], v12, s[24:27], s50 offen offset:384\n"
    "buffer_load_dwordx4 v[44:47], v12, s[24:27], s50 offen offset:512\n"
    "buffer_load_dwordx4 v[48:51], v12, s[24:27], s50 offen offset:640\n"
    "buffer_load_dwordx4 v[52:55], v12, s[24:27], s50 offen offset:768\n"
    "buffer_load_dwordx4 v[56:59], v12, s[24:27], s50 offen offset:896\n"
    "buffer_load_dwordx4 v[60:63], v12, s[24:27], s50 offen offset:1024\n"
    "s_waitcnt vmcnt(9)\n"
    "v_readfirstlane_b32 s95, v15\n"
    "s_mul_i32 s95, s95, 0x9e3779b1\n"
    "s_add_u32 s95, s95, 0x7f4a7c15\n"
    ".Lk_ln_5:\n"
    "s_mov_b32 s59, 0x3f803f80\n"
    ".Lk_mid_4:\n"
    "s_waitcnt vmcnt(8)\n"
    "v_dot2_f32_bf16 v208, v28, s59, 0\n"
    "v_dot2_f32_bf16 v210, v28, v28, 0\n"
    "v_dot2_f32_bf16 v209, v29, s59, 0\n"
    "v_dot2_f32_bf16 v211, v29, v29, 0\n"
    "v_dot2_f32_bf16 v208, v30, s59, v208\n"
    "v_dot2_f32_bf16 v210, v30, v30, v210\n"
    "v_dot2_f32_bf16 v209, v31, s59, v209\n"
    "v_dot2_f32_bf16 v211, v31, v31, v211\n"
    "v_lshlrev_b32 v136, 16, v28\n"
    "v_and_b32 v137, 0xffff0000, v28\n"
    "v_lshlrev_b32 v138, 16, v29\n"
    "v_and_b32 v139, 0xffff0000, v29\n"
    "v_lshlrev_b32 v140, 16, v30\n"
    "v_and_b32 v141, 0xffff0000, v30\n"
    "v_lshlrev_b32 v142, 16, v31\n"
    "v_and_b32 v143, 0xffff0000, v31\n"
    "s_waitcnt vmcnt(7)\n"
    "v_dot2_f32_bf16 v208, v32, s59, v208\n"
    "v_dot2_f32_bf16 v210, v32, v32, v210\n"
    "v_dot2_f32_bf16 v209, v33, s59, v209\n"
    "v_dot2_f32_bf16 v211, v33, v33, v211\n"
    "v_dot2_f32_bf16 v208, v34, s59, v208\n"
    "v_dot2_f32_bf16 v210, v34, v34, v210\n"
    "v_dot2_f32_bf16 v209, v35, s59, v209\n"
    "v_dot2_f32_bf16 v211, v35, v35, v211\n"
    "v_lshlrev_b32 v144, 16, v32\n"
    "v_and_b32 v145, 0xffff0000, v32\n"
    "v_lshlrev_b32 v146, 16, v33\n"
    "v_and_b32 v147, 0xffff0000, v33\n"
    "v_lshlrev_b32 v148, 16, v34\n"
    "v_and_b32 v149, 0xffff0000, v34\n"
    "v_lshlrev_b32 v150, 16, v35\n"
    "v_and_b32 v151, 0xffff0000, v35\n"
    "s_waitcnt vmcnt(6)\n"
    "v_dot2_f32_bf16 v208, v36, s59, v208\n"
    "v_dot2_f32_bf16 v210, v36, v36, v210\n"
    "v_dot2_f32_bf16 v209, v37, s59, v209\n"
    "v_dot2_f32_bf16 v211, v37, v37, v211\n"
    "v_dot2_f32_bf16 v208, v38, s59, v208\n"
    "v_dot2_f32_bf16 v210, v38, v38, v210\n"
    "v_dot2_f32_bf16 v209, v39, s59, v209\n"
    "v_dot2_f32_bf16 v211, v39, v39, v211\n"
    "v_lshlrev_b32 v152, 16, v36\n"
    "v_and_b32 v153, 0xffff0000, v36\n"
    "v_lshlrev_b32 v154, 16, v37\n"
    "v_and_b32 v155, 0xffff0000, v37\n"
    "v_lshlrev_b32 v156, 16, v38\n"
    "v_and_b32 v157, 0xffff0000, v38\n"
    "v_lshlrev_b32 v158, 16, v39\n"
    "v_and_b32 v159, 0xffff0000, v39\n"
    "s_waitcnt vmcnt(5)\n"
    "v_dot2_f32_bf16 v208, v40, s59, v208\n"
    "v_dot2_f32_bf16 v210, v40, v40, v210\n"
    "v_dot2_f32_bf16 v209, v41, s59, v209\n"
    "v_dot2_f32_bf16 v211, v41, v41, v211\n"
    "v_dot2_f32_bf16 v208, v42, s59, v208\n"
    "v_dot2_f32_bf16 v210, v42, v42, v210\n"
    "v_dot2_f32_bf16 v209, v43, s59, v209\n"
    "v_dot2_f32_bf16 v211, v43, v43, v211\n"
    "v_lshlrev_b32 v160, 16, v40\n"
    "v_and_b32 v161, 0xffff0000, v40\n"
    "v_lshlrev_b32 v162, 16, v41\n"
    "v_and_b32 v163, 0xffff0000, v41\n"
    "v_lshlrev_b32 v164, 16, v42\n"
    "v_and_b32 v165, 0xffff0000, v42\n"
    "v_lshlrev_b32 v166, 16, v43\n"
    "v_and_b32 v167, 0xffff0000, v43\n"
    "s_waitcnt vmcnt(4)\n"
    "v_dot2_f32_bf16 v208, v44, s59, v208\n"
    "v_dot2_f32_bf16 v210, v44, v44, v210\n"
    "v_dot2_f32_bf16 v209, v45, s59, v209\n"
    "v_dot2_f32_bf16 v211, v45, v45, v211\n"
    "v_dot2_f32_bf16 v208, v46, s59, v208\n"
    "v_dot2_f32_bf16 v210, v46, v46, v210\n"
    "v_dot2_f32_bf16 v209, v47, s59, v209\n"
    "v_dot2_f32_bf16 v211, v47, v47, v211\n"
    "v_lshlrev_b32 v168, 16, v44\n"
    "v_and_b32 v169, 0xffff0000, v44\n"
    "v_lshlrev_b32 v170, 16, v45\n"
    "v_and_b32 v171, 0xffff0000, v45\n"
    "v_lshlrev_b32 v172, 16, v46\n"
    "v_and_b32 v173, 0xffff0000, v46\n"
    "v_lshlrev_b32 v174, 16, v47\n"
    "v_and_b32 v175, 0xffff0000, v47\n"
    "s_waitcnt vmcnt(3)\n"
    "v_dot2_f32_bf16 v208, v48, s59, v208\n"
    "v_dot2_f32_bf16 v210, v48, v48, v210\n"
    "v_dot2_f32_bf16 v209, v49, s59, v209\n"
    "v_dot2_f32_bf16 v211, v49, v49, v211\n"
    "v_dot2_f32_bf16 v208, v50, s59, v208\n"
    "v_dot2_f32_bf16 v210, v50, v50, v210\n"
    "v_dot2_f32_bf16 v209, v51, s59, v209\n"
    "v_dot2_f32_bf16 v211, v51, v51, v211\n"
    "v_lshlrev_b32 v176, 16, v48\n"
    "v_and_b32 v177, 0xffff0000, v48\n"
    "v_lshlrev_b32 v178, 16, v49\n"
    "v_and_b32 v179, 0xffff0000, v49\n"
    "v_lshlrev_b32 v180, 16, v50\n"
    "v_and_b32 v181, 0xffff0000, v50\n"
    "v_lshlrev_b32 v182, 16, v51\n"
    "v_and_b32 v183, 0xffff0000, v51\n"
    "s_waitcnt vmcnt(2)\n"
    "v_dot2_f32_bf16 v208, v52, s59, v208\n"
    "v_dot2_f32_bf16 v210, v52, v52, v210\n"
    "v_dot2_f32_bf16 v209, v53, s59, v209\n"
    "v_dot2_f32_bf16 v211, v53, v53, v211\n"
    "v_dot2_f32_bf16 v208, v54, s59, v208\n"
    "v_dot2_f32_bf16 v210, v54, v54, v210\n"
    "v_dot2_f32_bf16 v209, v55, s59, v209\n"
    "v_dot2_f32_bf16 v211, v55, v55, v211\n"
    "v_lshlrev_b32 v184, 16, v52\n"
    "v_and_b32 v185, 0xffff0000, v52\n"
    "v_lshlrev_b32 v186, 16, v53\n"
    "v_and_b32 v187, 0xffff0000, v53\n"
    "v_lshlrev_b32 v188, 16, v54\n"
    "v_and_b32 v189, 0xffff0000, v54\n"
    "v_lshlrev_b32 v190, 16, v55\n"
    "v_and_b32 v191, 0xffff0000, v55\n"
    "s_waitcnt vmcnt(1)\n"
    "v_dot2_f32_bf16 v208, v56, s59, v208\n"
    "v_dot2_f32_bf16 v210, v56, v56, v210\n"
    "v_dot2_f32_bf16 v209, v57, s59, v209\n"
    "v_dot2_f32_bf16 v211, v57, v57, v211\n"
    "v_dot2_f32_bf16 v208, v58, s59, v208\n"
    "v_dot2_f32_bf16 v210, v58, v58, v210\n"
    "v_dot2_f32_bf16 v209, v59, s59, v209\n"
    "v_dot2_f32_bf16 v211, v59, v59, v211\n"
    "v_lshlrev_b32 v192, 16, v56\n"
    "v_and_b32 v193, 0xffff0000, v56\n"
    "v_lshlrev_b32 v194, 16, v57\n"
    "v_and_b32 v195, 0xffff0000, v57\n"
    "v_lshlrev_b32 v196, 16, v58\n"
    "v_and_b32 v197, 0xffff0000, v58\n"
    "v_lshlrev_b32 v198, 16, v59\n"
    "v_and_b32 v199, 0xffff0000, v59\n"
    "s_waitcnt vmcnt(0)\n"
    "v_dot2_f32_bf16 v208, v60, s59, v208\n"
    "v_dot2_f32_bf16 v210, v60, v60, v210\n"
    "v_dot2_f32_bf16 v209, v61, s59, v209\n"
    "v_dot2_f32_bf16 v211, v61, v61, v211\n"
    "v_dot2_f32_bf16 v208, v62, s59, v208\n"
    "v_dot2_f32_bf16 v210, v62, v62, v210\n"
    "v_dot2_f32_bf16 v209, v63, s59, v209\n"
    "v_dot2_f32_bf16 v211, v63, v63, v211\n"
    "v_lshlrev_b32 v200, 16, v60\n"
    "v_and_b32 v201, 0xffff0000, v60\n"
    "v_lshlrev_b32 v202, 16, v61\n"
    "v_and_b32 v203, 0xffff0000, v61\n"
    "v_lshlrev_b32 v204, 16, v62\n"
    "v_and_b32 v205, 0xffff0000, v62\n"
    "v_lshlrev_b32 v206, 16, v63\n"
    "v_and_b32 v207, 0xffff0000, v63\n"
    "v_add_f32 v208, v208, v209\n"
    "v_add_f32 v210, v210, v211\n"
    "s_nop 0\n"
    "v_add_f32_dpp v208, v208, v208 quad_perm:[1,0,3,2] row_mask:0xf bank_mask:0xf\n"
    "v_add_f32_dpp v210, v210, v210 quad_perm:[1,0,3,2] row_mask:0xf bank_mask:0xf\n"
    "s_nop 0\n"
    "v_add_f32_dpp v208, v208, v208 quad_perm:[2,3,0,1] row_mask:0xf bank_mask:0xf\n"
    "s_cmp_eq_u32 s40, 1\n"
    "s_cbranch_scc1 .Lk_ep_15\n"
    "s_lshl_b32 s52, s37, 12\n"
    "s_mul_i32 s41, s38, 128\n"
    "s_add_u32 s41, s41, s52\n"
    "s_add_u32 s52, s41, 163840\n"
    "v_min_u32 v16, 17, v2\n"
    "v_lshlrev_b32 v16, 2, v16\n"
    "buffer_load_dword v24, v16, s[20:23], s52 offen sc1\n"
    "s_branch .Lk_ep_16\n"
    ".Lk_ep_15:\n"
    "v_min_u32 v17, 35, v2\n"
    "s_mul_i32 s53, s39, 36\n"
    "v_add_u32 v17, s53, v17\n"
    "v_lshlrev_b32 v18, 4, v17\n"
    "s_and_b32 s53, %[bid], 3\n"
    "s_mul_i32 s53, s53, 13824\n"
    "s_add_u32 s53, s53, 196608\n"
    "buffer_load_dwordx4 v[32:35], v18, s[20:23], s53 offen sc1\n"
    "buffer_load_dwordx4 v[36:39], v18, s[20:23], s53 offen offset:2304 sc1\n"
    ".Lk_ep_16:\n"
    "v_add_f32_dpp v210, v210, v210 quad_perm:[2,3,0,1] row_mask:0xf bank_mask:0xf\n"
    "v_add_f32_dpp v208, v208, v208 row_half_mirror row_mask:0xf bank_mask:0xf\n"
    "s_nop 0\n"
    "v_add_f32_dpp v210, v210, v210 row_half_mirror row_mask:0xf bank_mask:0xf\n"
    "v_mul_f32 v208, 0xbae38e39, v208\n"
    "v_mul_f32 v210, 0x3ae38e39, v210\n"
    "v_fma_f32 v210, -v208, v208, v210\n"
    "v_max_f32 v210, 0, v210\n"
    "v_add_f32 v248, 0x3727c5ac, v210\n"
    "v_rsq_f32 v248, v248\n"
    "s_nop 0\n"
    "v_mov_b32 v249, v248\n"
    "v_mul_f32 v250, v208, v248\n"
    "v_mul_f32 v251, v208, v248\n"
    "v_pk_fma_f32 v[136:137], v[136:137], v[248:249], v[250:251]\n"
    "v_pk_fma_f32 v[138:139], v[138:139], v[248:249], v[250:251]\n"
    "v_pk_fma_f32 v[140:141], v[140:141], v[248:249], v[250:251]\n"
    "v_pk_fma_f32 v[142:143], v[142:143], v[248:249], v[250:251]\n"
    "v_pk_fma_f32 v[144:145], v[144:145], v[248:249], v[250:251]\n"
    "v_pk_fma_f32 v[146:147], v[146:147], v[248:249], v[250:251]\n"
    "v_pk_fma_f32 v[148:149], v[148:149], v[248:249], v[250:251]\n"
    "v_pk_fma_f32 v[150:151], v[150:151], v[248:249], v[250:251]\n"
    "v_pk_fma_f32 v[152:153], v[152:153], v[248:249], v[250:251]\n"
    "v_pk_fma_f32 v[154:155], v[154:155], v[248:249], v[250:251]\n"
    "v_pk_fma_f32 v[156:157], v[156:157], v[248:249], v[250:251]\n"
    "v_pk_fma_f32 v[158:159], v[158:159], v[248:249], v[250:251]\n"
    "v_pk_fma_f32 v[160:161], v[160:161], v[248:249], v[250:251]\n"
    "v_pk_fma_f32 v[162:163], v[162:163], v[248:249], v[250:251]\n"
    "v_pk_fma_f32 v[164:165], v[164:165], v[248:249], v[250:251]\n"
    "v_pk_fma_f32 v[166:167], v[166:167], v[248:249], v[250:251]\n"
    "v_pk_fma_f32 v[168:169], v[168:169], v[248:249], v[250:251]\n"
    "v_pk_fma_f32 v[170:171], v[170:171], v[248:249], v[250:251]\n"
    "v_pk_fma_f32 v[172:173], v[172:173], v[248:249], v[250:251]\n"
    "v_pk_fma_f32 v[174:175], v[174:175], v[248:249], v[250:251]\n"
    "v_pk_fma_f32 v[176:177], v[176:177], v[248:249], v[250:251]\n"
    "v_pk_fma_f32 v[178:179], v[178:179], v[248:249], v[250:251]\n"
    "v_pk_fma_f32 v[180:181], v[180:181], v[248:249], v[250:251]\n"
    "v_pk_fma_f32 v[182:183], v[182:183], v[248:249], v[250:251]\n"
    "v_pk_fma_f32 v[184:185], v[184:185], v[248:249], v[250:251]\n"
    "v_pk_fma_f32 v[186:187], v[186:187], v[248:249], v[250:251]\n"
    "v_pk_fma_f32 v[188:189], v[188:189], v[248:249], v[250:251]\n"
    "v_pk_fma_f32 v[190:191], v[190:191], v[248:249], v[250:251]\n"
    "v_pk_fma_f32 v[192:193], v[192:193], v[248:249], v[250:251]\n"
    "v_pk_fma_f32 v[194:195], v[194:195], v[248:249], v[250:251]\n"
    "v_pk_fma_f32 v[196:197], v[196:197], v[248:249], v[250:251]\n"
    "v_pk_fma_f32 v[198:199], v[198:199], v[248:249], v[250:251]\n"
    "v_pk_fma_f32 v[200:201], v[200:201], v[248:249], v[250:251]\n"
    "v_pk_fma_f32 v[202:203], v[202:203], v[248:249], v[250:251]\n"
    "v_pk_fma_f32 v[204:205], v[204:205], v[248:249], v[250:251]\n"
    "v_pk_fma_f32 v[206:207], v[206:207], v[248:249], v[250:251]\n"
    "s_cmp_eq_u32 s40, 1\n"
    "s_cbranch_scc1 .Lk_t1_17\n"
    "s_lshl_b32 s51, s37, 3\n"
    "s_add_u32 s51, s51, s36\n"
    "s_mul_i32 s51, s51, 36864\n"
    "s_add_u32 s51, s51, 258048\n"
    "s_mov_b32 s34, 2359296\n"
    "s_mov_b32 s35, 0x20000\n"
    "s_mov_b64 s[32:33], s[18:19]\n"
    "v_add_u32 v7, 36864, v1\n"
    "v_and_b32 v8, 31, v2\n"
    "v_lshrrev_b32 v9, 5, v2\n"
    "v_lshlrev_b32 v4, 2, v8\n"
    "v_mul_u32_u24 v26, 1040, v9\n"
    "v_lshl_add_u32 v26, v8, 4, v26\n"
    "v_add_u32 v27, 37440, v26\n"
    "v_mul_u32_u24 v10, 320, v9\n"
    "v_lshl_add_u32 v12, v8, 4, v10\n"
    "v_cmp_gt_u32 vcc, 19, v8\n"
    "s_nop 1\n"
    "v_cndmask_b32 v3, v11, v12, vcc\n"
    "s_lshl_b32 s41, s37, 2\n"
    "s_add_u32 s41, s41, s39\n"
    "s_lshl_b32 s41, s41, 6\n"
    "s_add_u32 s41, s41, s38\n"
    "s_mul_i32 s41, s41, 2432\n"
    "s_add_u32 s41, s41, 2617344\n"
    "s_add_u32 s42, s41, 77824\n"
    "s_mul_i32 s43, s39, 8704\n"
    "s_add_u32 s43, s43, 92160\n"
    "v_add_u32 v12, s43, v1\n"
    "s_mov_b32 s60, 0x8000\n"
    ".Lk_sp_19:\n"
    "s_waitcnt vmcnt(0)\n"
    "v_cmp_ne_u32 vcc, s95, v24\n"
    "s_cbranch_vccz .Lk_spd_20\n"
    "s_sub_u32 s60, s60, 1\n"
    "s_cbranch_scc1 .Lk_spd_20\n"
    "s_sleep 1\n"
    "buffer_load_dword v24, v16, s[20:23], s52 offen sc1\n"
    "s_branch .Lk_sp_19\n"
    ".Lk_spd_20:\n"
    "buffer_load_dwordx4 v[64:67], v1, s[20:23], s51 offen sc1\n"
    "buffer_load_dwordx4 v[68:71], v1, s[20:23], s51 offen offset:1024 sc1\n"
    "buffer_load_dwordx4 v[72:75], v1, s[20:23], s51 offen offset:2048 sc1\n"
    "buffer_load_dwordx4 v[76:79], v1, s[20:23], s51 offen offset:3072 sc1\n"
    "s_barrier\n"
    "ds_read_b128 v[208:211], v14\n"
    "ds_read_b128 v[248:251], v14 offset:2304\n"
    "ds_read_b128 v[212:215], v14 offset:1152\n"
    "ds_read_b128 v[252:255], v14 offset:3456\n"
    "s_waitcnt lgkmcnt(2)\n"
    "v_pk_fma_f32 v[136:137], v[136:137], v[208:209], v[248:249]\n"
    "v_pk_fma_f32 v[138:139], v[138:139], v[210:211], v[250:251]\n"
    "ds_read_b128 v[208:211], v14 offset:128\n"
    "ds_read_b128 v[248:251], v14 offset:2432\n"
    "v_cvt_pk_bf16_f32 v136, v136, v137\n"
    "v_cvt_pk_bf16_f32 v137, v138, v139\n"
    "s_add_u32 s51, s51, 4096\n"
    "buffer_load_dwordx4 v[80:83], v1, s[20:23], s51 offen sc1\n"
    "s_waitcnt lgkmcnt(2)\n"
    "v_pk_fma_f32 v[140:141], v[140:141], v[212:213], v[252:253]\n"
    "v_pk_fma_f32 v[142:143], v[142:143], v[214:215], v[254:255]\n"
    "ds_read_b128 v[212:215], v14 offset:1280\n"
    "ds_read_b128 v[252:255], v14 offset:3584\n"
    "v_cvt_pk_bf16_f32 v138, v140, v141\n"
    "v_cvt_pk_bf16_f32 v139, v142, v143\n"
    "buffer_load_dwordx4 v[84:87], v1, s[20:23], s51 offen offset:1024 sc1\n"
    "ds_write_b128 v13, v[136:139]\n"
    "s_waitcnt lgkmcnt(3)\n"
    "v_pk_fma_f32 v[144:145], v[144:145], v[208:209], v[248:249]\n"
    "v_pk_fma_f32 v[146:147], v[146:147], v[210:211], v[250:251]\n"
    "ds_read_b128 v[208:211], v14 offset:256\n"
    "ds_read_b128 v[248:251], v14 offset:2560\n"
    "v_cvt_pk_bf16_f32 v144, v144, v145\n"
    "buffer_load_dwordx4 v[88:91], v1, s[20:23], s51 offen offset:2048 sc1\n"
    "v_cvt_pk_bf16_f32 v145, v146, v147\n"
    "s_waitcnt lgkmcnt(3)\n"
    "v_pk_fma_f32 v[148:149], v[148:149], v[212:213], v[252:253]\n"
    "v_pk_fma_f32 v[150:151], v[150:151], v[214:215], v[254:255]\n"
    "ds_read_b128 v[212:215], v14 offset:1408\n"
    "ds_read_b128 v[252:255], v14 offset:3712\n"
    "v_cvt_pk_bf16_f32 v146, v148, v149\n"
    "buffer_load_dwordx4 v[92:95], v1, s[20:23], s51 offen offset:3072 sc1\n"
    "v_cvt_pk_bf16_f32 v147, v150, v151\n"
    "ds_write_b128 v13, v[144:147] offset:8320\n"
    "s_waitcnt lgkmcnt(3)\n"
    "v_pk_fma_f32 v[152:153], v[152:153], v[208:209], v[248:249]\n"
    "v_pk_fma_f32 v[154:155], v[154:155], v[210:211], v[250:251]\n"
    "ds_read_b128 v[208:211], v14 offset:384\n"
    "ds_read_b128 v[248:251], v14 offset:2688\n"
    "s_add_u32 s51, s51, 4096\n"
    "buffer_load_dwordx4 v[96:99], v1, s[20:23], s51 offen sc1\n"
    "v_cvt_pk_bf16_f32 v152, v152, v153\n"
    "v_cvt_pk_bf16_f32 v153, v154, v155\n"
    "s_waitcnt lgkmcnt(3)\n"
    "v_pk_fma_f32 v[156:157], v[156:157], v[212:213], v[252:253]\n"
    "v_pk_fma_f32 v[158:159], v[158:159], v[214:215], v[254:255]\n"
    "ds_read_b128 v[212:215], v14 offset:1536\n"
    "ds_read_b128 v[252:255], v14 offset:3840\n"
    "buffer_load_dwordx4 v[100:103], v1, s[20:23], s51 offen offset:1024 sc1\n"
    "v_cvt_pk_bf16_f32 v154, v156, v157\n"
    "v_cvt_pk_bf16_f32 v155, v158, v159\n"
    "ds_write_b128 v13, v[152:155] offset:16640\n"
    "s_waitcnt lgkmcnt(0)\n"
    "s_barrier\n"
    "buffer_load_dwordx4 v[104:107], v1, s[20:23], s51 offen offset:2048 sc1\n"
    "buffer_load_dwordx4 v[108:111], v1, s[20:23], s51 offen offset:3072 sc1\n"
    "ds_read_b128 v[32:35], v26\n"
    "ds_read_b128 v[36:39], v26 offset:512\n"
    "ds_read_b128 v[40:43], v26 offset:2080\n"
    "ds_read_b128 v[44:47], v26 offset:2592\n"
    "ds_read_b128 v[48:51], v26 offset:4160\n"
    "ds_read_b128 v[52:55], v26 offset:4672\n"
    "s_waitcnt vmcnt(11) lgkmcnt(5)\n"
    "v_mfma_f32_32x32x16_bf16 v[216:231], v[64:67], v[32:35], 0\n"
    "ds_read_b128 v[56:59], v26 offset:6240\n"
    "v_fma_f32 v160, v160, v208, v248\n"
    "v_fma_f32 v161, v161, v209, v249\n"
    "v_fma_f32 v162, v162, v210, v250\n"
    "s_waitcnt lgkmcnt(5)\n"
    "v_mfma_f32_32x32x16_bf16 v[232:247], v[64:67], v[36:39], 0\n"
    "ds_read_b128 v[60:63], v26 offset:6752\n"
    "v_fma_f32 v163, v163, v211, v251\n"
    "ds_read_b128 v[208:211], v14 offset:512\n"
    "ds_read_b128 v[248:251], v14 offset:2816\n"
    "s_add_u32 s51, s51, 4096\n"
    "buffer_load_dwordx4 v[112:115], v1, s[20:23], s51 offen sc1\n"
    "buffer_load_dwordx4 v[116:119], v1, s[20:23], s51 offen offset:1024 sc1\n"
    "s_waitcnt vmcnt(12) lgkmcnt(7)\n"
    "v_mfma_f32_32x32x16_bf16 v[216:231], v[68:71], v[40:43], v[216:231]\n"
    "ds_read_b128 v[32:35], v26 offset:8320\n"
    "v_cvt_pk_bf16_f32 v160, v160, v161\n"
    "v_cvt_pk_bf16_f32 v161, v162, v163\n"
    "v_fma_f32 v164, v164, v212, v252\n"
    "s_waitcnt lgkmcnt(7)\n"
    "v_mfma_f32_32x32x16_bf16 v[232:247], v[68:71], v[44:47], v[232:247]\n"
    "ds_read_b128 v[36:39], v26 offset:8832\n"
    "v_fma_f32 v165, v165, v213, v253\n"
    "v_fma_f32 v166, v166, v214, v254\n"
    "v_fma_f32 v167, v167, v215, v255\n"
    "buffer_load_dwordx4 v[120:123], v1, s[20:23], s51 offen offset:2048 sc1\n"
    "buffer_load_dwordx4 v[124:127], v1, s[20:23], s51 offen offset:3072 sc1\n"
    "s_waitcnt vmcnt(13) lgkmcnt(7)\n"
    "v_mfma_f32_32x32x16_bf16 v[216:231], v[72:75], v[48:51], v[216:231]\n"
    "ds_read_b128 v[40:43], v26 offset:10400\n"
    "ds_read_b128 v[212:215], v14 offset:1664\n"
    "ds_read_b128 v[252:255], v14 offset:3968\n"
    "v_cvt_pk_bf16_f32 v162, v164, v165\n"
    "s_waitcnt lgkmcnt(9)\n"
    "v_mfma_f32_32x32x16_bf16 v[232:247], v[72:75], v[52:55], v[232:247]\n"
    "ds_read_b128 v[44:47], v26 offset:10912\n"
    "v_cvt_pk_bf16_f32 v163, v166, v167\n"
    "ds_write_b128 v13, v[160:163] offset:24960\n"
    "s_add_u32 s51, s51, 4096\n"
    "buffer_load_dwordx4 v[128:131], v1, s[20:23], s51 offen sc1\n"
    "buffer_load_dwordx4 v[132:135], v1, s[20:23], s51 offen offset:1024 sc1\n"
    "s_waitcnt vmcnt(14) lgkmcnt(10)\n"
    "v_mfma_f32_32x32x16_bf16 v[216:231], v[76:79], v[56:59], v[216:231]\n"
    "ds_read_b128 v[48:51], v26 offset:12480\n"
    "s_waitcnt lgkmcnt(8)\n"
    "v_fma_f32 v168, v168, v208, v248\n"
    "v_fma_f32 v169, v169, v209, v249\n"
    "v_fma_f32 v170, v170, v210, v250\n"
    "v_mfma_f32_32x32x16_bf16 v[232:247], v[76:79], v[60:63], v[232:247]\n"
    "ds_read_b128 v[52:55], v26 offset:12992\n"
    "v_fma_f32 v171, v171, v211, v251\n"
    "ds_read_b128 v[208:211], v14 offset:640\n"
    "ds_read_b128 v[248:251], v14 offset:2944\n"
    "buffer_load_dwordx4 v[136:139], v1, s[20:23], s51 offen offset:2048 sc1\n"
    "s_waitcnt vmcnt(14) lgkmcnt(10)\n"
    "v_mfma_f32_32x32x16_bf16 v[216:231], v[80:83], v[32:35], v[216:231]\n"
    "ds_read_b128 v[56:59], v26 offset:14560\n"
    "v_cvt_pk_bf16_f32 v168, v168, v169\n"
    "v_cvt_pk_bf16_f32 v169, v170, v171\n"
    "s_waitcnt lgkmcnt(7)\n"
    "v_fma_f32 v172, v172, v212, v252\n"
    "v_mfma_f32_32x32x16_bf16 v[232:247], v[80:83], v[36:39], v[232:247]\n"
    "ds_read_b128 v[60:63], v26 offset:15072\n"
    "v_fma_f32 v173, v173, v213, v253\n"
    "v_fma_f32 v174, v174, v214, v254\n"
    "v_fma_f32 v175, v175, v215, v255\n"
    "buffer_load_dwordx4 v[140:143], v1, s[20:23], s51 offen offset:3072 sc1\n"
    "s_cmp_lt_u32 s44, 7\n"
    "s_cbranch_scc1 .Lk_wr_21\n"
    "s_cmp_lt_u32 s36, 3\n"
    "s_cbranch_scc1 .Lk_wr_21\n"
    "buffer_load_dwordx4 v21, s[28:31], s61 offen lds\n"
    ".Lk_wr_21:\n"
    "s_waitcnt vmcnt(14)\n"
    "v_mfma_f32_32x32x16_bf16 v[216:231], v[84:87], v[40:43], v[216:231]\n"
    "ds_read_b128 v[32:35], v26 offset:16640\n"
    "ds_read_b128 v[212:215], v14 offset:1792\n"
    "ds_read_b128 v[252:255], v14 offset:4096\n"
    "v_cvt_pk_bf16_f32 v170, v172, v173\n"
    "s_waitcnt lgkmcnt(10)\n"
    "v_mfma_f32_32x32x16_bf16 v[232:247], v[84:87], v[44:47], v[232:247]\n"
    "ds_read_b128 v[36:39], v26 offset:17152\n"
    "v_cvt_pk_bf16_f32 v171, v174, v175\n"
    "ds_write_b128 v25, v[168:171]\n"
    "s_add_u32 s51, s51, 4096\n"
    "buffer_load_dwordx4 v[144:147], v1, s[20:23], s51 offen sc1\n"
    "s_waitcnt vmcnt(14) lgkmcnt(10)\n"
    "v_mfma_f32_32x32x16_bf16 v[216:231], v[88:91], v[48:51], v[216:231]\n"
    "ds_read_b128 v[40:43], v26 offset:18720\n"
    "s_waitcnt lgkmcnt(8)\n"
    "v_fma_f32 v176, v176, v208, v248\n"
    "v_fma_f32 v177, v177, v209, v249\n"
    "v_fma_f32 v178, v178, v210, v250\n"
    "v_mfma_f32_32x32x16_bf16 v[232:247], v[88:91], v[52:55], v[232:247]\n"
    "ds_read_b128 v[44:47], v26 offset:19232\n"
    "v_fma_f32 v179, v179, v211, v251\n"
    "ds_read_b128 v[208:211], v14 offset:768\n"
    "ds_read_b128 v[248:251], v14 offset:3072\n"
    "buffer_load_dwordx4 v[148:151], v1, s[20:23], s51 offen offset:1024 sc1\n"
    "s_waitcnt vmcnt(14) lgkmcnt(10)\n"
    "v_mfma_f32_32x32x16_bf16 v[216:231], v[92:95], v[56:59], v[216:231]\n"
    "ds_read_b128 v[48:51], v26 offset:20800\n"
    "v_cvt_pk_bf16_f32 v176, v176, v177\n"
    "v_cvt_pk_bf16_f32 v177, v178, v179\n"
    "s_waitcnt lgkmcnt(7)\n"
    "v_fma_f32 v180, v180, v212, v252\n"
    "v_mfma_f32_32x32x16_bf16 v[232:247], v[92:95], v[60:63], v[232:247]\n"
    "ds_read_b128 v[52:55], v26 offset:21312\n"
    "v_fma_f32 v181, v181, v213, v253\n"
    "v_fma_f32 v182, v182, v214, v254\n"
    "v_fma_f32 v183, v183, v215, v255\n"
    "buffer_load_dwordx4 v[152:155], v1, s[20:23], s51 offen offset:2048 sc1\n"
    "s_waitcnt vmcnt(14)\n"
    "v_mfma_f32_32x32x16_bf16 v[216:231], v[96:99], v[32:35], v[216:231]\n"
    "ds_read_b128 v[56:59], v26 offset:22880\n"
    "ds_read_b128 v[212:215], v14 offset:1920\n"
    "ds_read_b128 v[252:255], v14 offset:4224\n"
    "v_cvt_pk_bf16_f32 v178, v180, v181\n"
    "s_waitcnt lgkmcnt(10)\n"
    "v_mfma_f32_32x32x16_bf16 v[232:247], v[96:99], v[36:39], v[232:247]\n"
    "ds_read_b128 v[60:63], v26 offset:23392\n"
    "v_cvt_pk_bf16_f32 v179, v182, v183\n"
    "ds_write_b128 v25, v[176:179] offset:8320\n"
    "buffer_load_dwordx4 v[156:159], v1, s[20:23], s51 offen offset:3072 sc1\n"
    "s_waitcnt lgkmcnt(0)\n"
    "s_barrier\n"
    "s_waitcnt vmcnt(14)\n"
    "v_mfma_f32_32x32x16_bf16 v[216:231], v[100:103], v[40:43], v[216:231]\n"
    "ds_read_b128 v[32:35], v26 offset:24960\n"
    "v_fma_f32 v184, v184, v208, v248\n"
    "v_fma_f32 v185, v185, v209, v249\n"
    "v_mfma_f32_32x32x16_bf16 v[232:247], v[100:103], v[44:47], v[232:247]\n"
    "ds_read_b128 v[36:39], v26 offset:25472\n"
    "v_fma_f32 v186, v186, v210, v250\n"
    "v_fma_f32 v187, v187, v211, v251\n"
    "s_add_u32 s51, s51, 4096\n"
    "buffer_load_dwordx4 v[160:163], v1, s[20:23], s51 offen sc1\n"
    "s_waitcnt vmcnt(14)\n"
    "v_mfma_f32_32x32x16_bf16 v[216:231], v[104:107], v[48:51], v[216:231]\n"
    "ds_read_b128 v[40:43], v26 offset:27040\n"
    "ds_read_b128 v[208:211], v14 offset:896\n"
    "ds_read_b128 v[248:251], v14 offset:3200\n"
    "v_mfma_f32_32x32x16_bf16 v[232:247], v[104:107], v[52:55], v[232:247]\n"
    "ds_read_b128 v[44:47], v26 offset:27552\n"
    "v_cvt_pk_bf16_f32 v184, v184, v185\n"
    "v_cvt_pk_bf16_f32 v185, v186, v187\n"
    "buffer_load_dwordx4 v[164:167], v1, s[20:23], s51 offen offset:1024 sc1\n"
    "s_waitcnt vmcnt(14)\n"
    "v_mfma_f32_32x32x16_bf16 v[216:231], v[108:111], v[56:59], v[216:231]\n"
    "ds_read_b128 v[48:51], v26 offset:29120\n"
    "v_fma_f32 v188, v188, v212, v252\n"
    "v_fma_f32 v189, v189, v213, v253\n"
    "v_mfma_f32_32x32x16_bf16 v[232:247], v[108:111], v[60:63], v[232:247]\n"
    "ds_read_b128 v[52:55], v26 offset:29632\n"
    "v_fma_f32 v190, v190, v214, v254\n"
    "v_fma_f32 v191, v191, v215, v255\n"
    "buffer_load_dwordx4 v[168:171], v1, s[20:23], s51 offen offset:2048 sc1\n"
    "s_waitcnt vmcnt(14) lgkmcnt(7)\n"
    "v_mfma_f32_32x32x16_bf16 v[216:231], v[112:115], v[32:35], v[216:231]\n"
    "ds_read_b128 v[56:59], v26 offset:31200\n"
    "ds_read_b128 v[212:215], v14 offset:2048\n"
    "ds_read_b128 v[252:255], v14 offset:4352\n"
    "s_waitcnt lgkmcnt(9)\n"
    "v_mfma_f32_32x32x16_bf16 v[232:247], v[112:115], v[36:39], v[232:247]\n"
    "ds_read_b128 v[60:63], v26 offset:31712\n"
    "v_cvt_pk_bf16_f32 v186, v188, v189\n"
    "v_cvt_pk_bf16_f32 v187, v190, v191\n"
    "buffer_load_dwordx4 v[172:175], v1, s[20:23], s51 offen offset:3072 sc1\n"
    "s_waitcnt vmcnt(14) lgkmcnt(9)\n"
    "v_mfma_f32_32x32x16_bf16 v[216:231], v[116:119], v[40:43], v[216:231]\n"
    "ds_read_b128 v[32:35], v26 offset:33280\n"
    "ds_write_b128 v25, v[184:187] offset:16640\n"
    "s_waitcnt lgkmcnt(9)\n"
    "v_fma_f32 v192, v192, v208, v248\n"
    "s_waitcnt lgkmcnt(8)\n"
    "v_mfma_f32_32x32x16_bf16 v[232:247], v[116:119], v[44:47], v[232:247]\n"
    "ds_read_b128 v[36:39], v26 offset:33792\n"
    "v_fma_f32 v193, v193, v209, v249\n"
    "v_fma_f32 v194, v194, v210, v250\n"
    "s_add_u32 s51, s51, 4096\n"
    "buffer_load_dwordx4 v[176:179], v1, s[20:23], s51 offen sc1\n"
    "s_waitcnt vmcnt(14) lgkmcnt(8)\n"
    "v_mfma_f32_32x32x16_bf16 v[216:231], v[120:123], v[48:51], v[216:231]\n"
    "ds_read_b128 v[40:43], v26 offset:35360\n"
    "v_fma_f32 v195, v195, v211, v251\n"
    "ds_read_b128 v[208:211], v14 offset:1024\n"
    "s_waitcnt lgkmcnt(9)\n"
    "v_mfma_f32_32x32x16_bf16 v[232:247], v[120:123], v[52:55], v[232:247]\n"
    "ds_read_b128 v[44:47], v26 offset:35872\n"
    "ds_read_b128 v[248:251], v14 offset:3328\n"
    "v_cvt_pk_bf16_f32 v192, v192, v193\n"
    "buffer_load_dwordx4 v[180:183], v1, s[20:23], s51 offen offset:1024 sc1\n"
    "s_waitcnt vmcnt(14) lgkmcnt(10)\n"
    "v_mfma_f32_32x32x16_bf16 v[216:231], v[124:127], v[56:59], v[216:231]\n"
    "ds_read_b128 v[48:51], v27\n"
    "v_cvt_pk_bf16_f32 v193, v194, v195\n"
    "s_waitcnt lgkmcnt(9)\n"
    "v_fma_f32 v196, v196, v212, v252\n"
    "s_waitcnt lgkmcnt(8)\n"
    "v_mfma_f32_32x32x16_bf16 v[232:247], v[124:127], v[60:63], v[232:247]\n"
    "ds_read_b128 v[52:55], v27 offset:512\n"
    "v_fma_f32 v197, v197, v213, v253\n"
    "v_fma_f32 v198, v198, v214, v254\n"
    "buffer_load_dwordx4 v[184:187], v1, s[20:23], s51 offen offset:2048 sc1\n"
    "s_waitcnt vmcnt(14) lgkmcnt(8)\n"
    "v_mfma_f32_32x32x16_bf16 v[216:231], v[128:131], v[32:35], v[216:231]\n"
    "ds_read_b128 v[56:59], v27 offset:2080\n"
    "v_fma_f32 v199, v199, v215, v255\n"
    "ds_read_b128 v[212:215], v14 offset:2176\n"
    "s_waitcnt lgkmcnt(8)\n"
    "v_mfma_f32_32x32x16_bf16 v[232:247], v[128:131], v[36:39], v[232:247]\n"
    "ds_read_b128 v[60:63], v27 offset:2592\n"
    "ds_read_b128 v[252:255], v14 offset:4480\n"
    "v_cvt_pk_bf16_f32 v194, v196, v197\n"
    "buffer_load_dwordx4 v[188:191], v1, s[20:23], s51 offen offset:3072 sc1\n"
    "s_waitcnt vmcnt(14) lgkmcnt(9)\n"
    "v_mfma_f32_32x32x16_bf16 v[216:231], v[132:135], v[40:43], v[216:231]\n"
    "ds_read_b128 v[32:35], v27 offset:4160\n"
    "v_cvt_pk_bf16_f32 v195, v198, v199\n"
    "ds_write_b128 v25, v[192:195] offset:24960\n"
    "s_waitcnt lgkmcnt(9)\n"
    "v_mfma_f32_32x32x16_bf16 v[232:247], v[132:135], v[44:47], v[232:247]\n"
    "ds_read_b128 v[36:39], v27 offset:4672\n"
    "s_waitcnt lgkmcnt(9)\n"
    "v_fma_f32 v200, v200, v208, v248\n"
    "v_fma_f32 v201, v201, v209, v249\n"
    "s_add_u32 s51, s51, 4096\n"
    "buffer_load_dwordx4 v[192:195], v1, s[20:23], s51 offen sc1\n"
    "s_waitcnt vmcnt(14) lgkmcnt(8)\n"
    "v_mfma_f32_32x32x16_bf16 v[216:231], v[136:139], v[48:51], v[216:231]\n"
    "ds_read_b128 v[40:43], v27 offset:6240\n"
    "v_fma_f32 v202, v202, v210, v250\n"
    "v_fma_f32 v203, v203, v211, v251\n"
    "s_waitcnt lgkmcnt(8)\n"
    "v_mfma_f32_32x32x16_bf16 v[232:247], v[136:139], v[52:55], v[232:247]\n"
    "ds_read_b128 v[44:47], v27 offset:6752\n"
    "v_cvt_pk_bf16_f32 v200, v200, v201\n"
    "v_cvt_pk_bf16_f32 v201, v202, v203\n"
    "buffer_load_dwordx4 v[196:199], v1, s[20:23], s51 offen offset:1024 sc1\n"
    "s_waitcnt vmcnt(14) lgkmcnt(8)\n"
    "v_mfma_f32_32x32x16_bf16 v[216:231], v[140:143], v[56:59], v[216:231]\n"
    "ds_read_b128 v[48:51], v27 offset:8320\n"
    "s_waitcnt lgkmcnt(6)\n"
    "v_fma_f32 v204, v204, v212, v252\n"
    "v_fma_f32 v205, v205, v213, v253\n"
    "v_mfma_f32_32x32x16_bf16 v[232:247], v[140:143], v[60:63], v[232:247]\n"
    "ds_read_b128 v[52:55], v27 offset:8832\n"
    "v_fma_f32 v206, v206, v214, v254\n"
    "v_fma_f32 v207, v207, v215, v255\n"
    "s_mov_b32 s30, 5308416\n"
    "s_mov_b32 s31, 0x20000\n"
    "s_mov_b64 s[28:29], s[10:11]\n"
    "s_mul_i32 s47, s38, 162\n"
    "s_mul_i32 s48, s36, 21\n"
    "s_add_u32 s48, s48, s47\n"
    "v_add_u32 v30, s48, v2\n"
    "s_add_u32 s47, s47, 162\n"
    "s_add_u32 s48, s48, 21\n"
    "s_min_u32 s47, s47, s48\n"
    "v_mul_u32_u24 v28, 7282, v30\n"
    "v_lshrrev_b32 v28, 16, v28\n"
    "v_mad_i32_i24 v29, v28, -9, v30\n"
    "v_mul_u32_u24 v28, 9216, v28\n"
    "v_lshl_add_u32 v28, v29, 7, v28\n"
    "s_mul_i32 s61, s37, 1152\n"
    "v_cmp_gt_u32 vcc, s47, v30\n"
    "s_nop 1\n"
    "v_cndmask_b32 v28, v11, v28, vcc\n"
    "s_waitcnt vmcnt(13) lgkmcnt(6)\n"
    "v_mfma_f32_32x32x16_bf16 v[216:231], v[144:147], v[32:35], v[216:231]\n"
    "ds_read_b128 v[56:59], v27 offset:10400\n"
    "v_cvt_pk_bf16_f32 v202, v204, v205\n"
    "v_cvt_pk_bf16_f32 v203, v206, v207\n"
    "s_waitcnt lgkmcnt(5)\n"
    "v_mfma_f32_32x32x16_bf16 v[232:247], v[144:147], v[36:39], v[232:247]\n"
    "ds_read_b128 v[60:63], v27 offset:10912\n"
    "ds_write_b128 v25, v[200:203] offset:33280\n"
    "buffer_load_dwordx4 v[200:203], v1, s[20:23], s51 offen offset:2048 sc1\n"
    "s_waitcnt lgkmcnt(0)\n"
    "s_barrier\n"
    "s_waitcnt vmcnt(13)\n"
    "v_mfma_f32_32x32x16_bf16 v[216:231], v[148:151], v[40:43], v[216:231]\n"
    "ds_read_b128 v[32:35], v27 offset:12480\n"
    "v_mfma_f32_32x32x16_bf16 v[232:247], v[148:151], v[44:47], v[232:247]\n"
    "ds_read_b128 v[36:39], v27 offset:12992\n"
    "buffer_load_dwordx4 v[204:207], v1, s[20:23], s51 offen offset:3072 sc1\n"
    "buffer_load_dword v31, v28, s[28:31], s61 offen\n"
    "s_waitcnt vmcnt(14)\n"
    "v_mfma_f32_32x32x16_bf16 v[216:231], v[152:155], v[48:51], v[216:231]\n"
    "ds_read_b128 v[40:43], v27 offset:14560\n"
    "v_mfma_f32_32x32x16_bf16 v[232:247], v[152:155], v[52:55], v[232:247]\n"
    "ds_read_b128 v[44:47], v27 offset:15072\n"
    "s_waitcnt vmcnt(13)\n"
    "v_mfma_f32_32x32x16_bf16 v[216:231], v[156:159], v[56:59], v[216:231]\n"
    "ds_read_b128 v[48:51], v27 offset:16640\n"
    "v_mfma_f32_32x32x16_bf16 v[232:247], v[156:159], v[60:63], v[232:247]\n"
    "ds_read_b128 v[52:55], v27 offset:17152\n"
    "s_waitcnt vmcnt(12) lgkmcnt(5)\n"
    "v_mfma_f32_32x32x16_bf16 v[216:231], v[160:163], v[32:35], v[216:231]\n"
    "ds_read_b128 v[56:59], v27 offset:18720\n"
    "s_waitcnt lgkmcnt(5)\n"
    "v_mfma_f32_32x32x16_bf16 v[232:247], v[160:163], v[36:39], v[232:247]\n"
    "ds_read_b128 v[60:63], v27 offset:19232\n"
    "s_waitcnt vmcnt(11) lgkmcnt(5)\n"
    "v_mfma_f32_32x32x16_bf16 v[216:231], v[164:167], v[40:43], v[216:231]\n"
    "ds_read_b128 v[32:35], v27 offset:20800\n"
    "s_waitcnt lgkmcnt(5)\n"
    "v_mfma_f32_32x32x16_bf16 v[232:247], v[164:167], v[44:47], v[232:247]\n"
    "ds_read_b128 v[36:39], v27 offset:21312\n"
    "s_waitcnt vmcnt(10) lgkmcnt(5)\n"
    "v_mfma_f32_32x32x16_bf16 v[216:231], v[168:171], v[48:51], v[216:231]\n"
    "ds_read_b128 v[40:43], v27 offset:22880\n"
    "s_waitcnt lgkmcnt(5)\n"
    "v_mfma_f32_32x32x16_bf16 v[232:247], v[168:171], v[52:55], v[232:247]\n"
    "ds_read_b128 v[44:47], v27 offset:23392\n"
    "s_cmp_lt_u32 s44, 7\n"
    "s_cbranch_scc1 .Lk_wo_22\n"
    "s_cmp_gt_u32 s36, 4\n"
    "s_cbranch_scc1 .Lk_wo_22\n"
    "ds_read_b64_tr_b16 v[64:65], v23 offset:0\n"
    "ds_read_b64_tr_b16 v[66:67], v23 offset:576\n"
    "s_mul_i32 s47, s37, 18\n"
    "s_add_u32 s47, s47, s46\n"
    "s_mul_i32 s47, s47, 5\n"
    "s_add_u32 s47, s47, s36\n"
    "s_mul_i32 s47, s47, 1024\n"
    "s_add_u32 s47, s47, 9957376\n"
    "s_cmp_lt_u32 s36, 4\n"
    "s_cselect_b32 s48, 16, 8\n"
    "v_and_b32 v68, 15, v2\n"
    "v_cmp_gt_u32 vcc, s48, v68\n"
    "s_waitcnt lgkmcnt(1)\n"
    "s_nop 0\n"
    "v_cndmask_b32 v64, 0, v64, vcc\n"
    "v_cndmask_b32 v65, 0, v65, vcc\n"
    "s_waitcnt lgkmcnt(0)\n"
    "v_cndmask_b32 v66, 0, v66, vcc\n"
    "v_cndmask_b32 v67, 0, v67, vcc\n"
    "buffer_store_dwordx4 v[64:67], v1, s[20:23], s47 offen sc1\n"
    ".Lk_wo_22:\n"
    "s_waitcnt vmcnt(9) lgkmcnt(5)\n"
    "v_mfma_f32_32x32x16_bf16 v[216:231], v[172:175], v[56:59], v[216:231]\n"
    "ds_read_b128 v[48:51], v27 offset:24960\n"
    "s_waitcnt lgkmcnt(5)\n"
    "v_mfma_f32_32x32x16_bf16 v[232:247], v[172:175], v[60:63], v[232:247]\n"
    "ds_read_b128 v[52:55], v27 offset:25472\n"
    "s_waitcnt vmcnt(8) lgkmcnt(5)\n"
    "v_mfma_f32_32x32x16_bf16 v[216:231], v[176:179], v[32:35], v[216:231]\n"
    "ds_read_b128 v[56:59], v27 offset:27040\n"
    "s_waitcnt lgkmcnt(5)\n"
    "v_mfma_f32_32x32x16_bf16 v[232:247], v[176:179], v[36:39], v[232:247]\n"
    "ds_read_b128 v[60:63], v27 offset:27552\n"
    "s_waitcnt vmcnt(7) lgkmcnt(5)\n"
    "v_mfma_f32_32x32x16_bf16 v[216:231], v[180:183], v[40:43], v[216:231]\n"
    "ds_read_b128 v[32:35], v27 offset:29120\n"
    "s_waitcnt lgkmcnt(5)\n"
    "v_mfma_f32_32x32x16_bf16 v[232:247], v[180:183], v[44:47], v[232:247]\n"
    "ds_read_b128 v[36:39], v27 offset:29632\n"
    "s_waitcnt vmcnt(6) lgkmcnt(5)\n"
    "v_mfma_f32_32x32x16_bf16 v[216:231], v[184:187], v[48:51], v[216:231]\n"
    "ds_read_b128 v[40:43], v27 offset:31200\n"
    "s_waitcnt lgkmcnt(5)\n"
    "v_mfma_f32_32x32x16_bf16 v[232:247], v[184:187], v[52:55], v[232:247]\n"
    "ds_read_b128 v[44:47], v27 offset:31712\n"
    "s_waitcnt vmcnt(5) lgkmcnt(5)\n"
    "v_mfma_f32_32x32x16_bf16 v[216:231], v[188:191], v[56:59], v[216:231]\n"
    "ds_read_b128 v[48:51], v27 offset:33280\n"
    "s_waitcnt lgkmcnt(5)\n"
    "v_mfma_f32_32x32x16_bf16 v[232:247], v[188:191], v[60:63], v[232:247]\n"
    "ds_read_b128 v[52:55], v27 offset:33792\n"
    "s_mov_b64 s[28:29], s[12:13]\n"
    "s_mul_i32 s48, s36, 21\n"
    "v_add_u32 v30, s48, v2\n"
    "s_add_u32 s48, s48, 21\n"
    "s_min_u32 s48, s48, 162\n"
    "v_lshlrev_b32 v29, 7, v30\n"
    "v_cmp_gt_u32 vcc, s48, v30\n"
    "s_nop 1\n"
    "v_cndmask_b32 v29, v11, v29, vcc\n"
    "s_mul_i32 s48, s38, 20736\n"
    "s_mul_i32 s49, s37, 663552\n"
    "s_add_u32 s49, s49, s48\n"
    "buffer_load_dword v30, v29, s[28:31], s49 offen\n"
    "s_waitcnt vmcnt(5) lgkmcnt(5)\n"
    "v_mfma_f32_32x32x16_bf16 v[216:231], v[192:195], v[32:35], v[216:231]\n"
    "ds_read_b128 v[56:59], v27 offset:35360\n"
    "s_waitcnt lgkmcnt(5)\n"
    "v_mfma_f32_32x32x16_bf16 v[232:247], v[192:195], v[36:39], v[232:247]\n"
    "ds_read_b128 v[60:63], v27 offset:35872\n"
    "s_waitcnt vmcnt(4) lgkmcnt(5)\n"
    "v_mfma_f32_32x32x16_bf16 v[216:231], v[196:199], v[40:43], v[216:231]\n"
    "s_waitcnt lgkmcnt(4)\n"
    "v_mfma_f32_32x32x16_bf16 v[232:247], v[196:199], v[44:47], v[232:247]\n"
    "s_waitcnt vmcnt(3) lgkmcnt(3)\n"
    "v_mfma_f32_32x32x16_bf16 v[216:231], v[200:203], v[48:51], v[216:231]\n"
    "s_waitcnt lgkmcnt(2)\n"
    "v_mfma_f32_32x32x16_bf16 v[232:247], v[200:203], v[52:55], v[232:247]\n"
    "s_waitcnt vmcnt(2) lgkmcnt(1)\n"
    "v_mfma_f32_32x32x16_bf16 v[216:231], v[204:207], v[56:59], v[216:231]\n"
    "s_waitcnt lgkmcnt(0)\n"
    "v_mfma_f32_32x32x16_bf16 v[232:247], v[204:207], v[60:63], v[232:247]\n"
    "s_nop 9\n"
    "v_cvt_pk_bf16_f32 v68, v224, v225\n"
    "v_cvt_pk_bf16_f32 v69, v226, v227\n"
    "v_cvt_pk_bf16_f32 v70, v228, v229\n"
    "v_cvt_pk_bf16_f32 v71, v230, v231\n"
    "buffer_store_dwordx4 v[68:71], v1, s[20:23], s41 offen sc1\n"
    "v_mul_f32 v216, 0x3eae1a93, v216\n"
    "v_mul_f32 v217, 0x3eae1a93, v217\n"
    "v_mul_f32 v218, 0x3eae1a93, v218\n"
    "v_mul_f32 v219, 0x3eae1a93, v219\n"
    "v_mul_f32 v220, 0x3eae1a93, v220\n"
    "v_mul_f32 v221, 0x3eae1a93, v221\n"
    "v_mul_f32 v222, 0x3eae1a93, v222\n"
    "v_mul_f32 v223, 0x3eae1a93, v223\n"
    "v_cvt_pk_bf16_f32 v64, v216, v217\n"
    "v_cvt_pk_bf16_f32 v65, v218, v219\n"
    "v_cvt_pk_bf16_f32 v66, v220, v221\n"
    "v_cvt_pk_bf16_f32 v67, v222, v223\n"
    "ds_write_b128 v12, v[64:67]\n"
    "ds_write_b128 v12, v[68:71] offset:2048\n"
    "v_cvt_pk_bf16_f32 v76, v240, v241\n"
    "v_cvt_pk_bf16_f32 v77, v242, v243\n"
    "v_cvt_pk_bf16_f32 v78, v244, v245\n"
    "v_cvt_pk_bf16_f32 v79, v246, v247\n"
    "buffer_store_dwordx4 v[76:79], v1, s[20:23], s42 offen sc1\n"
    "v_mul_f32 v232, 0x3eae1a93, v232\n"
    "v_mul_f32 v233, 0x3eae1a93, v233\n"
    "v_mul_f32 v234, 0x3eae1a93, v234\n"
    "v_mul_f32 v235, 0x3eae1a93, v235\n"
    "v_mul_f32 v236, 0x3eae1a93, v236\n"
    "v_mul_f32 v237, 0x3eae1a93, v237\n"
    "v_mul_f32 v238, 0x3eae1a93, v238\n"
    "v_mul_f32 v239, 0x3eae1a93, v239\n"
    "v_cvt_pk_bf16_f32 v72, v232, v233\n"
    "v_cvt_pk_bf16_f32 v73, v234, v235\n"
    "v_cvt_pk_bf16_f32 v74, v236, v237\n"
    "v_cvt_pk_bf16_f32 v75, v238, v239\n"
    "ds_write_b128 v12, v[72:75] offset:1024\n"
    "ds_write_b128 v12, v[76:79] offset:3072\n"
    "s_branch .Lk_aj_18\n"
    ".Lk_t1_17:\n"
    "s_lshl_b32 s51, s37, 3\n"
    "s_add_u32 s51, s51, s36\n"
    "s_mul_i32 s51, s51, 36864\n"
    "s_add_u32 s51, s51, 258048\n"
    "s_mov_b32 s34, 2359296\n"
    "s_mov_b32 s35, 0x20000\n"
    "s_mov_b64 s[32:33], s[18:19]\n"
    "v_add_u32 v7, 36864, v1\n"
    "v_and_b32 v8, 31, v2\n"
    "v_lshrrev_b32 v9, 5, v2\n"
    "v_lshlrev_b32 v4, 2, v8\n"
    "v_mul_u32_u24 v26, 1040, v9\n"
    "v_lshl_add_u32 v26, v8, 4, v26\n"
    "v_add_u32 v27, 37440, v26\n"
    "v_mul_u32_u24 v10, 320, v9\n"
    "v_lshl_add_u32 v12, v8, 4, v10\n"
    "v_cmp_gt_u32 vcc, 19, v8\n"
    "s_nop 1\n"
    "v_cndmask_b32 v3, v11, v12, vcc\n"
    "s_lshl_b32 s41, s37, 2\n"
    "s_add_u32 s41, s41, s39\n"
    "s_lshl_b32 s41, s41, 6\n"
    "s_add_u32 s41, s41, s38\n"
    "s_mul_i32 s41, s41, 2432\n"
    "s_add_u32 s41, s41, 2617344\n"
    "s_add_u32 s42, s41, 77824\n"
    "s_mul_i32 s43, s39, 8704\n"
    "s_add_u32 s43, s43, 92160\n"
    "v_add_u32 v12, s43, v1\n"
    "v_cmp_gt_u32 vcc, 24, v8\n"
    "s_nop 1\n"
    "v_cndmask_b32 v22, v11, v1, vcc\n"
    "v_and_b32 v19, 1, v17\n"
    "v_mul_u32_u24 v19, 1152, v19\n"
    "v_lshrrev_b32 v17, 1, v17\n"
    "v_lshl_add_u32 v17, v17, 4, v19\n"
    "v_add_u32 v17, 81920, v17\n"
    "s_mov_b32 s60, 0x8000\n"
    ".Lk_sp_23:\n"
    "s_waitcnt vmcnt(1)\n"
    "v_cmp_ne_u32 vcc, s95, v32\n"
    "v_cmp_ne_u32 s[54:55], s95, v34\n"
    "s_or_b64 vcc, vcc, s[54:55]\n"
    "s_waitcnt vmcnt(0)\n"
    "v_cmp_ne_u32 s[54:55], s95, v36\n"
    "s_or_b64 vcc, vcc, s[54:55]\n"
    "v_cmp_ne_u32 s[54:55], s95, v38\n"
    "s_or_b64 vcc, vcc, s[54:55]\n"
    "s_cbranch_scc0 .Lk_spd_24\n"
    "s_sub_u32 s60, s60, 1\n"
    "s_cbranch_scc1 .Lk_spd_24\n"
    "s_sleep 1\n"
    "buffer_load_dwordx4 v[32:35], v18, s[20:23], s53 offen sc1\n"
    "buffer_load_dwordx4 v[36:39], v18, s[20:23], s53 offen offset:2304 sc1\n"
    "s_branch .Lk_sp_23\n"
    ".Lk_spd_24:\n"
    "v_lshlrev_b32 v44, 16, v37\n"
    "v_and_b32 v45, 0xffff0000, v37\n"
    "v_lshlrev_b32 v46, 16, v39\n"
    "v_and_b32 v47, 0xffff0000, v39\n"
    "v_add_f32 v44, 1.0, v44\n"
    "v_add_f32 v45, 1.0, v45\n"
    "v_add_f32 v46, 1.0, v46\n"
    "v_add_f32 v47, 1.0, v47\n"
    "v_cvt_pk_bf16_f32 v37, v44, v45\n"
    "v_cvt_pk_bf16_f32 v39, v46, v47\n"
    "v_lshlrev_b32 v40, 16, v33\n"
    "v_and_b32 v41, 0xffff0000, v33\n"
    "v_lshlrev_b32 v42, 16, v35\n"
    "v_and_b32 v43, 0xffff0000, v35\n"
    "v_lshlrev_b32 v44, 16, v37\n"
    "v_and_b32 v45, 0xffff0000, v37\n"
    "v_lshlrev_b32 v46, 16, v39\n"
    "v_and_b32 v47, 0xffff0000, v39\n"
    "ds_write_b128 v17, v[44:47]\n"
    "ds_write_b128 v17, v[40:43] offset:2304\n"
    "s_waitcnt lgkmcnt(0)\n"
    "s_barrier\n"
    "ds_read_b128 v[208:211], v14\n"
    "ds_read_b128 v[248:251], v14 offset:2304\n"
    "ds_read_b128 v[212:215], v14 offset:1152\n"
    "ds_read_b128 v[252:255], v14 offset:3456\n"
    "buffer_load_dwordx4 v[64:67], v22, s[20:23], s51 offen sc1\n"
    "buffer_load_dwordx4 v[68:71], v22, s[20:23], s51 offen offset:1024 sc1\n"
    "buffer_load_dwordx4 v[72:75], v22, s[20:23], s51 offen offset:2048 sc1\n"
    "buffer_load_dwordx4 v[76:79], v22, s[20:23], s51 offen offset:3072 sc1\n"
    "s_add_u32 s51, s51, 4096\n"
    "buffer_load_dwordx4 v[80:83], v22, s[20:23], s51 offen sc1\n"
    "buffer_load_dwordx4 v[84:87], v22, s[20:23], s51 offen offset:1024 sc1\n"
    "s_setprio 1\n"
    "s_waitcnt lgkmcnt(2)\n"
    "v_pk_fma_f32 v[136:137], v[136:137], v[208:209], v[248:249]\n"
    "v_pk_fma_f32 v[138:139], v[138:139], v[210:211], v[250:251]\n"
    "ds_read_b128 v[208:211], v14 offset:128\n"
    "ds_read_b128 v[248:251], v14 offset:2432\n"
    "v_cvt_pk_bf16_f32 v136, v136, v137\n"
    "v_cvt_pk_bf16_f32 v137, v138, v139\n"
    "buffer_load_dwordx4 v[88:91], v22, s[20:23], s51 offen offset:2048 sc1\n"
    "s_waitcnt lgkmcnt(2)\n"
    "v_pk_fma_f32 v[140:141], v[140:141], v[212:213], v[252:253]\n"
    "v_pk_fma_f32 v[142:143], v[142:143], v[214:215], v[254:255]\n"
    "ds_read_b128 v[212:215], v14 offset:1280\n"
    "ds_read_b128 v[252:255], v14 offset:3584\n"
    "v_cvt_pk_bf16_f32 v138, v140, v141\n"
    "v_cvt_pk_bf16_f32 v139, v142, v143\n"
    "buffer_load_dwordx4 v[92:95], v22, s[20:23], s51 offen offset:3072 sc1\n"
    "ds_write_b128 v13, v[136:139]\n"
    "s_waitcnt lgkmcnt(3)\n"
    "v_pk_fma_f32 v[144:145], v[144:145], v[208:209], v[248:249]\n"
    "v_pk_fma_f32 v[146:147], v[146:147], v[210:211], v[250:251]\n"
    "ds_read_b128 v[208:211], v14 offset:256\n"
    "ds_read_b128 v[248:251], v14 offset:2560\n"
    "v_cvt_pk_bf16_f32 v144, v144, v145\n"
    "s_add_u32 s51, s51, 4096\n"
    "buffer_load_dwordx4 v[96:99], v22, s[20:23], s51 offen sc1\n"
    "v_cvt_pk_bf16_f32 v145, v146, v147\n"
    "s_waitcnt lgkmcnt(3)\n"
    "v_pk_fma_f32 v[148:149], v[148:149], v[212:213], v[252:253]\n"
    "v_pk_fma_f32 v[150:151], v[150:151], v[214:215], v[254:255]\n"
    "ds_read_b128 v[212:215], v14 offset:1408\n"
    "ds_read_b128 v[252:255], v14 offset:3712\n"
    "v_cvt_pk_bf16_f32 v146, v148, v149\n"
    "buffer_load_dwordx4 v[100:103], v22, s[20:23], s51 offen offset:1024 sc1\n"
    "v_cvt_pk_bf16_f32 v147, v150, v151\n"
    "ds_write_b128 v13, v[144:147] offset:8320\n"
    "s_waitcnt lgkmcnt(3)\n"
    "v_pk_fma_f32 v[152:153], v[152:153], v[208:209], v[248:249]\n"
    "v_pk_fma_f32 v[154:155], v[154:155], v[210:211], v[250:251]\n"
    "ds_read_b128 v[208:211], v14 offset:384\n"
    "ds_read_b128 v[248:251], v14 offset:2688\n"
    "buffer_load_dwordx4 v[104:107], v22, s[20:23], s51 offen offset:2048 sc1\n"
    "v_cvt_pk_bf16_f32 v152, v152, v153\n"
    "v_cvt_pk_bf16_f32 v153, v154, v155\n"
    "s_waitcnt lgkmcnt(3)\n"
    "v_pk_fma_f32 v[156:157], v[156:157], v[212:213], v[252:253]\n"
    "v_pk_fma_f32 v[158:159], v[158:159], v[214:215], v[254:255]\n"
    "ds_read_b128 v[212:215], v14 offset:1536\n"
    "ds_read_b128 v[252:255], v14 offset:3840\n"
    "buffer_load_dwordx4 v[108:111], v22, s[20:23], s51 offen offset:3072 sc1\n"
    "v_cvt_pk_bf16_f32 v154, v156, v157\n"
    "v_cvt_pk_bf16_f32 v155, v158, v159\n"
    "ds_write_b128 v13, v[152:155] offset:16640\n"
    "s_waitcnt lgkmcnt(0)\n"
    "s_barrier\n"
    "s_add_u32 s51, s51, 4096\n"
    "buffer_load_dwordx4 v[112:115], v22, s[20:23], s51 offen sc1\n"
    "buffer_load_dwordx4 v[116:119], v22, s[20:23], s51 offen offset:1024 sc1\n"
    "ds_read_b128 v[32:35], v26\n"
    "ds_read_b128 v[36:39], v26 offset:512\n"
    "ds_read_b128 v[40:43], v26 offset:2080\n"
    "ds_read_b128 v[44:47], v26 offset:2592\n"
    "ds_read_b128 v[48:51], v26 offset:4160\n"
    "ds_read_b128 v[52:55], v26 offset:4672\n"
    "s_waitcnt vmcnt(13) lgkmcnt(5)\n"
    "v_mfma_f32_32x32x16_bf16 v[216:231], v[32:35], v[64:67], 0\n"
    "ds_read_b128 v[56:59], v26 offset:6240\n"
    "v_fma_f32 v160, v160, v208, v248\n"
    "v_fma_f32 v161, v161, v209, v249\n"
    "v_fma_f32 v162, v162, v210, v250\n"
    "s_waitcnt lgkmcnt(5)\n"
    "v_mfma_f32_32x32x16_bf16 v[232:247], v[36:39], v[64:67], 0\n"
    "ds_read_b128 v[60:63], v26 offset:6752\n"
    "v_fma_f32 v163, v163, v211, v251\n"
    "ds_read_b128 v[208:211], v14 offset:512\n"
    "ds_read_b128 v[248:251], v14 offset:2816\n"
    "buffer_load_dwordx4 v[120:123], v22, s[20:23], s51 offen offset:2048 sc1\n"
    "s_waitcnt vmcnt(13) lgkmcnt(7)\n"
    "v_mfma_f32_32x32x16_bf16 v[216:231], v[40:43], v[68:71], v[216:231]\n"
    "ds_read_b128 v[32:35], v26 offset:8320\n"
    "v_cvt_pk_bf16_f32 v160, v160, v161\n"
    "v_cvt_pk_bf16_f32 v161, v162, v163\n"
    "v_fma_f32 v164, v164, v212, v252\n"
    "s_waitcnt lgkmcnt(7)\n"
    "v_mfma_f32_32x32x16_bf16 v[232:247], v[44:47], v[68:71], v[232:247]\n"
    "ds_read_b128 v[36:39], v26 offset:8832\n"
    "v_fma_f32 v165, v165, v213, v253\n"
    "v_fma_f32 v166, v166, v214, v254\n"
    "v_fma_f32 v167, v167, v215, v255\n"
    "buffer_load_dwordx4 v[124:127], v22, s[20:23], s51 offen offset:3072 sc1\n"
    "s_waitcnt vmcnt(13) lgkmcnt(7)\n"
    "v_mfma_f32_32x32x16_bf16 v[216:231], v[48:51], v[72:75], v[216:231]\n"
    "ds_read_b128 v[40:43], v26 offset:10400\n"
    "ds_read_b128 v[212:215], v14 offset:1664\n"
    "ds_read_b128 v[252:255], v14 offset:3968\n"
    "v_cvt_pk_bf16_f32 v162, v164, v165\n"
    "s_waitcnt lgkmcnt(9)\n"
    "v_mfma_f32_32x32x16_bf16 v[232:247], v[52:55], v[72:75], v[232:247]\n"
    "ds_read_b128 v[44:47], v26 offset:10912\n"
    "v_cvt_pk_bf16_f32 v163, v166, v167\n"
    "ds_write_b128 v13, v[160:163] offset:24960\n"
    "s_add_u32 s51, s51, 4096\n"
    "buffer_load_dwordx4 v[128:131], v22, s[20:23], s51 offen sc1\n"
    "s_waitcnt vmcnt(13) lgkmcnt(10)\n"
    "v_mfma_f32_32x32x16_bf16 v[216:231], v[56:59], v[76:79], v[216:231]\n"
    "ds_read_b128 v[48:51], v26 offset:12480\n"
    "s_waitcnt lgkmcnt(8)\n"
    "v_fma_f32 v168, v168, v208, v248\n"
    "v_fma_f32 v169, v169, v209, v249\n"
    "v_fma_f32 v170, v170, v210, v250\n"
    "v_mfma_f32_32x32x16_bf16 v[232:247], v[60:63], v[76:79], v[232:247]\n"
    "ds_read_b128 v[52:55], v26 offset:12992\n"
    "v_fma_f32 v171, v171, v211, v251\n"
    "ds_read_b128 v[208:211], v14 offset:640\n"
    "ds_read_b128 v[248:251], v14 offset:2944\n"
    "buffer_load_dwordx4 v[132:135], v22, s[20:23], s51 offen offset:1024 sc1\n"
    "s_waitcnt vmcnt(13) lgkmcnt(10)\n"
    "v_mfma_f32_32x32x16_bf16 v[216:231], v[32:35], v[80:83], v[216:231]\n"
    "ds_read_b128 v[56:59], v26 offset:14560\n"
    "v_cvt_pk_bf16_f32 v168, v168, v169\n"
    "v_cvt_pk_bf16_f32 v169, v170, v171\n"
    "s_waitcnt lgkmcnt(7)\n"
    "v_fma_f32 v172, v172, v212, v252\n"
    "v_mfma_f32_32x32x16_bf16 v[232:247], v[36:39], v[80:83], v[232:247]\n"
    "ds_read_b128 v[60:63], v26 offset:15072\n"
    "v_fma_f32 v173, v173, v213, v253\n"
    "v_fma_f32 v174, v174, v214, v254\n"
    "v_fma_f32 v175, v175, v215, v255\n"
    "buffer_load_dwordx4 v[136:139], v22, s[20:23], s51 offen offset:2048 sc1\n"
    "s_cmp_lt_u32 s44, 7\n"
    "s_cbranch_scc1 .Lk_wr_25\n"
    "s_cmp_lt_u32 s36, 3\n"
    "s_cbranch_scc1 .Lk_wr_25\n"
    "buffer_load_dwordx4 v21, s[28:31], s61 offen lds\n"
    ".Lk_wr_25:\n"
    "s_waitcnt vmcnt(13)\n"
    "v_mfma_f32_32x32x16_bf16 v[216:231], v[40:43], v[84:87], v[216:231]\n"
    "ds_read_b128 v[32:35], v26 offset:16640\n"
    "ds_read_b128 v[212:215], v14 offset:1792\n"
    "ds_read_b128 v[252:255], v14 offset:4096\n"
    "v_cvt_pk_bf16_f32 v170, v172, v173\n"
    "s_waitcnt lgkmcnt(10)\n"
    "v_mfma_f32_32x32x16_bf16 v[232:247], v[44:47], v[84:87], v[232:247]\n"
    "ds_read_b128 v[36:39], v26 offset:17152\n"
    "v_cvt_pk_bf16_f32 v171, v174, v175\n"
    "ds_write_b128 v25, v[168:171]\n"
    "buffer_load_dwordx4 v[140:143], v22, s[20:23], s51 offen offset:3072 sc1\n"
    "s_waitcnt vmcnt(13) lgkmcnt(10)\n"
    "v_mfma_f32_32x32x16_bf16 v[216:231], v[48:51], v[88:91], v[216:231]\n"
    "ds_read_b128 v[40:43], v26 offset:18720\n"
    "s_waitcnt lgkmcnt(8)\n"
    "v_fma_f32 v176, v176, v208, v248\n"
    "v_fma_f32 v177, v177, v209, v249\n"
    "v_fma_f32 v178, v178, v210, v250\n"
    "v_mfma_f32_32x32x16_bf16 v[232:247], v[52:55], v[88:91], v[232:247]\n"
    "ds_read_b128 v[44:47], v26 offset:19232\n"
    "v_fma_f32 v179, v179, v211, v251\n"
    "ds_read_b128 v[208:211], v14 offset:768\n"
    "ds_read_b128 v[248:251], v14 offset:3072\n"
    "s_add_u32 s51, s51, 4096\n"
    "buffer_load_dwordx4 v[144:147], v22, s[20:23], s51 offen sc1\n"
    "s_waitcnt vmcnt(13) lgkmcnt(10)\n"
    "v_mfma_f32_32x32x16_bf16 v[216:231], v[56:59], v[92:95], v[216:231]\n"
    "ds_read_b128 v[48:51], v26 offset:20800\n"
    "v_cvt_pk_bf16_f32 v176, v176, v177\n"
    "v_cvt_pk_bf16_f32 v177, v178, v179\n"
    "s_waitcnt lgkmcnt(7)\n"
    "v_fma_f32 v180, v180, v212, v252\n"
    "v_mfma_f32_32x32x16_bf16 v[232:247], v[60:63], v[92:95], v[232:247]\n"
    "ds_read_b128 v[52:55], v26 offset:21312\n"
    "v_fma_f32 v181, v181, v213, v253\n"
    "v_fma_f32 v182, v182, v214, v254\n"
    "v_fma_f32 v183, v183, v215, v255\n"
    "buffer_load_dwordx4 v[148:151], v22, s[20:23], s51 offen offset:1024 sc1\n"
    "s_waitcnt vmcnt(13)\n"
    "v_mfma_f32_32x32x16_bf16 v[216:231], v[32:35], v[96:99], v[216:231]\n"
    "ds_read_b128 v[56:59], v26 offset:22880\n"
    "ds_read_b128 v[212:215], v14 offset:1920\n"
    "ds_read_b128 v[252:255], v14 offset:4224\n"
    "v_cvt_pk_bf16_f32 v178, v180, v181\n"
    "s_waitcnt lgkmcnt(10)\n"
    "v_mfma_f32_32x32x16_bf16 v[232:247], v[36:39], v[96:99], v[232:247]\n"
    "ds_read_b128 v[60:63], v26 offset:23392\n"
    "v_cvt_pk_bf16_f32 v179, v182, v183\n"
    "ds_write_b128 v25, v[176:179] offset:8320\n"
    "buffer_load_dwordx4 v[152:155], v22, s[20:23], s51 offen offset:2048 sc1\n"
    "s_waitcnt lgkmcnt(0)\n"
    "s_barrier\n"
    "s_waitcnt vmcnt(13)\n"
    "v_mfma_f32_32x32x16_bf16 v[216:231], v[40:43], v[100:103], v[216:231]\n"
    "ds_read_b128 v[32:35], v26 offset:24960\n"
    "v_fma_f32 v184, v184, v208, v248\n"
    "v_fma_f32 v185, v185, v209, v249\n"
    "v_mfma_f32_32x32x16_bf16 v[232:247], v[44:47], v[100:103], v[232:247]\n"
    "ds_read_b128 v[36:39], v26 offset:25472\n"
    "v_fma_f32 v186, v186, v210, v250\n"
    "v_fma_f32 v187, v187, v211, v251\n"
    "buffer_load_dwordx4 v[156:159], v22, s[20:23], s51 offen offset:3072 sc1\n"
    "s_waitcnt vmcnt(13)\n"
    "v_mfma_f32_32x32x16_bf16 v[216:231], v[48:51], v[104:107], v[216:231]\n"
    "ds_read_b128 v[40:43], v26 offset:27040\n"
    "ds_read_b128 v[208:211], v14 offset:896\n"
    "ds_read_b128 v[248:251], v14 offset:3200\n"
    "v_mfma_f32_32x32x16_bf16 v[232:247], v[52:55], v[104:107], v[232:247]\n"
    "ds_read_b128 v[44:47], v26 offset:27552\n"
    "v_cvt_pk_bf16_f32 v184, v184, v185\n"
    "v_cvt_pk_bf16_f32 v185, v186, v187\n"
    "s_add_u32 s51, s51, 4096\n"
    "buffer_load_dwordx4 v[160:163], v22, s[20:23], s51 offen sc1\n"
    "s_waitcnt vmcnt(13)\n"
    "v_mfma_f32_32x32x16_bf16 v[216:231], v[56:59], v[108:111], v[216:231]\n"
    "ds_read_b128 v[48:51], v26 offset:29120\n"
    "v_fma_f32 v188, v188, v212, v252\n"
    "v_fma_f32 v189, v189, v213, v253\n"
    "v_mfma_f32_32x32x16_bf16 v[232:247], v[60:63], v[108:111], v[232:247]\n"
    "ds_read_b128 v[52:55], v26 offset:29632\n"
    "v_fma_f32 v190, v190, v214, v254\n"
    "v_fma_f32 v191, v191, v215, v255\n"
    "buffer_load_dwordx4 v[164:167], v22, s[20:23], s51 offen offset:1024 sc1\n"
    "s_waitcnt vmcnt(13) lgkmcnt(7)\n"
    "v_mfma_f32_32x32x16_bf16 v[216:231], v[32:35], v[112:115], v[216:231]\n"
    "ds_read_b128 v[56:59], v26 offset:31200\n"
    "ds_read_b128 v[212:215], v14 offset:2048\n"
    "ds_read_b128 v[252:255], v14 offset:4352\n"
    "s_waitcnt lgkmcnt(9)\n"
    "v_mfma_f32_32x32x16_bf16 v[232:247], v[36:39], v[112:115], v[232:247]\n"
    "ds_read_b128 v[60:63], v26 offset:31712\n"
    "v_cvt_pk_bf16_f32 v186, v188, v189\n"
    "v_cvt_pk_bf16_f32 v187, v190, v191\n"
    "buffer_load_dwordx4 v[168:171], v22, s[20:23], s51 offen offset:2048 sc1\n"
    "s_waitcnt vmcnt(13) lgkmcnt(9)\n"
    "v_mfma_f32_32x32x16_bf16 v[216:231], v[40:43], v[116:119], v[216:231]\n"
    "ds_read_b128 v[32:35], v26 offset:33280\n"
    "ds_write_b128 v25, v[184:187] offset:16640\n"
    "s_waitcnt lgkmcnt(9)\n"
    "v_fma_f32 v192, v192, v208, v248\n"
    "s_waitcnt lgkmcnt(8)\n"
    "v_mfma_f32_32x32x16_bf16 v[232:247], v[44:47], v[116:119], v[232:247]\n"
    "ds_read_b128 v[36:39], v26 offset:33792\n"
    "v_fma_f32 v193, v193, v209, v249\n"
    "v_fma_f32 v194, v194, v210, v250\n"
    "buffer_load_dwordx4 v[172:175], v22, s[20:23], s51 offen offset:3072 sc1\n"
    "s_waitcnt vmcnt(13) lgkmcnt(8)\n"
    "v_mfma_f32_32x32x16_bf16 v[216:231], v[48:51], v[120:123], v[216:231]\n"
    "ds_read_b128 v[40:43], v26 offset:35360\n"
    "v_fma_f32 v195, v195, v211, v251\n"
    "ds_read_b128 v[208:211], v14 offset:1024\n"
    "s_waitcnt lgkmcnt(9)\n"
    "v_mfma_f32_32x32x16_bf16 v[232:247], v[52:55], v[120:123], v[232:247]\n"
    "ds_read_b128 v[44:47], v26 offset:35872\n"
    "ds_read_b128 v[248:251], v14 offset:3328\n"
    "v_cvt_pk_bf16_f32 v192, v192, v193\n"
    "s_add_u32 s51, s51, 4096\n"
    "buffer_load_dwordx4 v[176:179], v22, s[20:23], s51 offen sc1\n"
    "s_waitcnt vmcnt(13) lgkmcnt(10)\n"
    "v_mfma_f32_32x32x16_bf16 v[216:231], v[56:59], v[124:127], v[216:231]\n"
    "ds_read_b128 v[48:51], v27\n"
    "v_cvt_pk_bf16_f32 v193, v194, v195\n"
    "s_waitcnt lgkmcnt(9)\n"
    "v_fma_f32 v196, v196, v212, v252\n"
    "s_waitcnt lgkmcnt(8)\n"
    "v_mfma_f32_32x32x16_bf16 v[232:247], v[60:63], v[124:127], v[232:247]\n"
    "ds_read_b128 v[52:55], v27 offset:512\n"
    "v_fma_f32 v197, v197, v213, v253\n"
    "v_fma_f32 v198, v198, v214, v254\n"
    "buffer_load_dwordx4 v[180:183], v22, s[20:23], s51 offen offset:1024 sc1\n"
    "s_waitcnt vmcnt(13) lgkmcnt(8)\n"
    "v_mfma_f32_32x32x16_bf16 v[216:231], v[32:35], v[128:131], v[216:231]\n"
    "ds_read_b128 v[56:59], v27 offset:2080\n"
    "v_fma_f32 v199, v199, v215, v255\n"
    "ds_read_b128 v[212:215], v14 offset:2176\n"
    "s_waitcnt lgkmcnt(8)\n"
    "v_mfma_f32_32x32x16_bf16 v[232:247], v[36:39], v[128:131], v[232:247]\n"
    "ds_read_b128 v[60:63], v27 offset:2592\n"
    "ds_read_b128 v[252:255], v14 offset:4480\n"
    "v_cvt_pk_bf16_f32 v194, v196, v197\n"
    "buffer_load_dwordx4 v[184:187], v22, s[20:23], s51 offen offset:2048 sc1\n"
    "s_waitcnt vmcnt(13) lgkmcnt(9)\n"
    "v_mfma_f32_32x32x16_bf16 v[216:231], v[40:43], v[132:135], v[216:231]\n"
    "ds_read_b128 v[32:35], v27 offset:4160\n"
    "v_cvt_pk_bf16_f32 v195, v198, v199\n"
    "ds_write_b128 v25, v[192:195] offset:24960\n"
    "s_waitcnt lgkmcnt(9)\n"
    "v_mfma_f32_32x32x16_bf16 v[232:247], v[44:47], v[132:135], v[232:247]\n"
    "ds_read_b128 v[36:39], v27 offset:4672\n"
    "s_waitcnt lgkmcnt(9)\n"
    "v_fma_f32 v200, v200, v208, v248\n"
    "v_fma_f32 v201, v201, v209, v249\n"
    "buffer_load_dwordx4 v[188:191], v22, s[20:23], s51 offen offset:3072 sc1\n"
    "s_waitcnt vmcnt(13) lgkmcnt(8)\n"
    "v_mfma_f32_32x32x16_bf16 v[216:231], v[48:51], v[136:139], v[216:231]\n"
    "ds_read_b128 v[40:43], v27 offset:6240\n"
    "v_fma_f32 v202, v202, v210, v250\n"
    "v_fma_f32 v203, v203, v211, v251\n"
    "s_waitcnt lgkmcnt(8)\n"
    "v_mfma_f32_32x32x16_bf16 v[232:247], v[52:55], v[136:139], v[232:247]\n"
    "ds_read_b128 v[44:47], v27 offset:6752\n"
    "v_cvt_pk_bf16_f32 v200, v200, v201\n"
    "v_cvt_pk_bf16_f32 v201, v202, v203\n"
    "s_add_u32 s51, s51, 4096\n"
    "buffer_load_dwordx4 v[192:195], v22, s[20:23], s51 offen sc1\n"
    "s_waitcnt vmcnt(13) lgkmcnt(8)\n"
    "v_mfma_f32_32x32x16_bf16 v[216:231], v[56:59], v[140:143], v[216:231]\n"
    "ds_read_b128 v[48:51], v27 offset:8320\n"
    "s_waitcnt lgkmcnt(6)\n"
    "v_fma_f32 v204, v204, v212, v252\n"
    "v_fma_f32 v205, v205, v213, v253\n"
    "v_mfma_f32_32x32x16_bf16 v[232:247], v[60:63], v[140:143], v[232:247]\n"
    "ds_read_b128 v[52:55], v27 offset:8832\n"
    "v_fma_f32 v206, v206, v214, v254\n"
    "v_fma_f32 v207, v207, v215, v255\n"
    "buffer_load_dwordx4 v[196:199], v22, s[20:23], s51 offen offset:1024 sc1\n"
    "s_mov_b32 s30, 5308416\n"
    "s_mov_b32 s31, 0x20000\n"
    "s_mov_b64 s[28:29], s[10:11]\n"
    "s_mul_i32 s47, s38, 162\n"
    "s_mul_i32 s48, s36, 21\n"
    "s_add_u32 s48, s48, s47\n"
    "v_add_u32 v30, s48, v2\n"
    "s_add_u32 s47, s47, 162\n"
    "s_add_u32 s48, s48, 21\n"
    "s_min_u32 s47, s47, s48\n"
    "v_mul_u32_u24 v28, 7282, v30\n"
    "v_lshrrev_b32 v28, 16, v28\n"
    "v_mad_i32_i24 v29, v28, -9, v30\n"
    "v_mul_u32_u24 v28, 9216, v28\n"
    "v_lshl_add_u32 v28, v29, 7, v28\n"
    "s_mul_i32 s61, s37, 1152\n"
    "v_cmp_gt_u32 vcc, s47, v30\n"
    "s_nop 1\n"
    "v_cndmask_b32 v28, v11, v28, vcc\n"
    "s_waitcnt vmcnt(13) lgkmcnt(6)\n"
    "v_mfma_f32_32x32x16_bf16 v[216:231], v[32:35], v[144:147], v[216:231]\n"
    "ds_read_b128 v[56:59], v27 offset:10400\n"
    "v_cvt_pk_bf16_f32 v202, v204, v205\n"
    "v_cvt_pk_bf16_f32 v203, v206, v207\n"
    "s_waitcnt lgkmcnt(5)\n"
    "v_mfma_f32_32x32x16_bf16 v[232:247], v[36:39], v[144:147], v[232:247]\n"
    "ds_read_b128 v[60:63], v27 offset:10912\n"
    "ds_write_b128 v25, v[200:203] offset:33280\n"
    "buffer_load_dwordx4 v[200:203], v22, s[20:23], s51 offen offset:2048 sc1\n"
    "s_and_b32 s53, s39, 1\n"
    "s_lshl_b32 s53, s53, 1\n"
    "v_lshrrev_b32 v13, 2, v8\n"
    "v_mov_b32 v14, s53\n"
    "v_cmp_eq_u32 vcc, 4, v13\n"
    "s_nop 1\n"
    "v_cndmask_b32 v14, 0, v14, vcc\n"
    "v_xor_b32 v14, v14, v8\n"
    "v_lshl_add_u32 v20, v14, 4, v10\n"
    "v_cmp_gt_u32 vcc, 20, v14\n"
    "s_nop 1\n"
    "v_cndmask_b32 v20, v11, v20, vcc\n"
    "v_lshlrev_b32 v21, 9, v9\n"
    "v_lshl_add_u32 v21, v14, 4, v21\n"
    "v_add_u32 v21, s43, v21\n"
    "v_mov_b32 v16, 0x3eae1a93\n"
    "v_xor_b32 v13, s53, v8\n"
    "v_lshrrev_b32 v13, 1, v13\n"
    "v_cmp_eq_u32 vcc, 10, v13\n"
    "s_nop 1\n"
    "v_cndmask_b32 v16, 1.0, v16, vcc\n"
    "v_cmp_eq_u32 vcc, 18, v14\n"
    "s_nop 1\n"
    "v_cndmask_b32 v16, v16, 0, vcc\n"
    "v_cndmask_b32 v18, 0, 1.0, vcc\n"
    "v_mov_b32 v17, v16\n"
    "v_mov_b32 v19, v18\n"
    "s_waitcnt lgkmcnt(0)\n"
    "s_barrier\n"
    "s_waitcnt vmcnt(13)\n"
    "v_mfma_f32_32x32x16_bf16 v[216:231], v[40:43], v[148:151], v[216:231]\n"
    "ds_read_b128 v[32:35], v27 offset:12480\n"
    "v_mfma_f32_32x32x16_bf16 v[232:247], v[44:47], v[148:151], v[232:247]\n"
    "ds_read_b128 v[36:39], v27 offset:12992\n"
    "buffer_load_dwordx4 v[204:207], v22, s[20:23], s51 offen offset:3072 sc1\n"
    "buffer_load_dword v31, v28, s[28:31], s61 offen\n"
    "s_waitcnt vmcnt(14)\n"
    "v_mfma_f32_32x32x16_bf16 v[216:231], v[48:51], v[152:155], v[216:231]\n"
    "ds_read_b128 v[40:43], v27 offset:14560\n"
    "v_mfma_f32_32x32x16_bf16 v[232:247], v[52:55], v[152:155], v[232:247]\n"
    "ds_read_b128 v[44:47], v27 offset:15072\n"
    "v_subrev_u32 v13, 10, v13\n"
    "v_lshlrev_b32 v13, 8, v13\n"
    "v_and_b32 v14, 1, v8\n"
    "v_lshl_add_u32 v13, v14, 1, v13\n"
    "v_lshl_add_u32 v13, v9, 4, v13\n"
    "v_add_u32 v13, s43, v13\n"
    "v_lshrrev_b32 v14, 3, v2\n"
    "v_mul_u32_u24 v14, 77824, v14\n"
    "v_and_b32 v15, 7, v2\n"
    "v_lshl_add_u32 v14, v15, 4, v14\n"
    "v_cmp_gt_u32 vcc, 16, v2\n"
    "s_nop 1\n"
    "v_cndmask_b32 v14, v11, v14, vcc\n"
    "s_waitcnt vmcnt(13)\n"
    "v_mfma_f32_32x32x16_bf16 v[216:231], v[56:59], v[156:159], v[216:231]\n"
    "ds_read_b128 v[48:51], v27 offset:16640\n"
    "v_mfma_f32_32x32x16_bf16 v[232:247], v[60:63], v[156:159], v[232:247]\n"
    "ds_read_b128 v[52:55], v27 offset:17152\n"
    "s_waitcnt vmcnt(12) lgkmcnt(5)\n"
    "v_mfma_f32_32x32x16_bf16 v[216:231], v[32:35], v[160:163], v[216:231]\n"
    "ds_read_b128 v[56:59], v27 offset:18720\n"
    "s_waitcnt lgkmcnt(5)\n"
    "v_mfma_f32_32x32x16_bf16 v[232:247], v[36:39], v[160:163], v[232:247]\n"
    "ds_read_b128 v[60:63], v27 offset:19232\n"
    "s_waitcnt vmcnt(11) lgkmcnt(5)\n"
    "v_mfma_f32_32x32x16_bf16 v[216:231], v[40:43], v[164:167], v[216:231]\n"
    "ds_read_b128 v[32:35], v27 offset:20800\n"
    "s_waitcnt lgkmcnt(5)\n"
    "v_mfma_f32_32x32x16_bf16 v[232:247], v[44:47], v[164:167], v[232:247]\n"
    "ds_read_b128 v[36:39], v27 offset:21312\n"
    "s_waitcnt vmcnt(10) lgkmcnt(5)\n"
    "v_mfma_f32_32x32x16_bf16 v[216:231], v[48:51], v[168:171], v[216:231]\n"
    "ds_read_b128 v[40:43], v27 offset:22880\n"
    "s_waitcnt lgkmcnt(5)\n"
    "v_mfma_f32_32x32x16_bf16 v[232:247], v[52:55], v[168:171], v[232:247]\n"
    "ds_read_b128 v[44:47], v27 offset:23392\n"
    "s_cmp_lt_u32 s44, 7\n"
    "s_cbranch_scc1 .Lk_wo_26\n"
    "s_cmp_gt_u32 s36, 4\n"
    "s_cbranch_scc1 .Lk_wo_26\n"
    "ds_read_b64_tr_b16 v[64:65], v23 offset:0\n"
    "ds_read_b64_tr_b16 v[66:67], v23 offset:576\n"
    "s_mul_i32 s47, s37, 18\n"
    "s_add_u32 s47, s47, s46\n"
    "s_mul_i32 s47, s47, 5\n"
    "s_add_u32 s47, s47, s36\n"
    "s_mul_i32 s47, s47, 1024\n"
    "s_add_u32 s47, s47, 9957376\n"
    "s_cmp_lt_u32 s36, 4\n"
    "s_cselect_b32 s48, 16, 8\n"
    "v_and_b32 v68, 15, v2\n"
    "v_cmp_gt_u32 vcc, s48, v68\n"
    "s_waitcnt lgkmcnt(1)\n"
    "s_nop 0\n"
    "v_cndmask_b32 v64, 0, v64, vcc\n"
    "v_cndmask_b32 v65, 0, v65, vcc\n"
    "s_waitcnt lgkmcnt(0)\n"
    "v_cndmask_b32 v66, 0, v66, vcc\n"
    "v_cndmask_b32 v67, 0, v67, vcc\n"
    "buffer_store_dwordx4 v[64:67], v1, s[20:23], s47 offen sc1\n"
    ".Lk_wo_26:\n"
    "s_waitcnt vmcnt(9) lgkmcnt(5)\n"
    "v_mfma_f32_32x32x16_bf16 v[216:231], v[56:59], v[172:175], v[216:231]\n"
    "ds_read_b128 v[48:51], v27 offset:24960\n"
    "s_waitcnt lgkmcnt(5)\n"
    "v_mfma_f32_32x32x16_bf16 v[232:247], v[60:63], v[172:175], v[232:247]\n"
    "ds_read_b128 v[52:55], v27 offset:25472\n"
    "s_waitcnt vmcnt(8) lgkmcnt(5)\n"
    "v_mfma_f32_32x32x16_bf16 v[216:231], v[32:35], v[176:179], v[216:231]\n"
    "ds_read_b128 v[56:59], v27 offset:27040\n"
    "s_waitcnt lgkmcnt(5)\n"
    "v_mfma_f32_32x32x16_bf16 v[232:247], v[36:39], v[176:179], v[232:247]\n"
    "ds_read_b128 v[60:63], v27 offset:27552\n"
    "s_waitcnt vmcnt(7) lgkmcnt(5)\n"
    "v_mfma_f32_32x32x16_bf16 v[216:231], v[40:43], v[180:183], v[216:231]\n"
    "ds_read_b128 v[32:35], v27 offset:29120\n"
    "s_waitcnt lgkmcnt(5)\n"
    "v_mfma_f32_32x32x16_bf16 v[232:247], v[44:47], v[180:183], v[232:247]\n"
    "ds_read_b128 v[36:39], v27 offset:29632\n"
    "s_waitcnt vmcnt(6) lgkmcnt(5)\n"
    "v_mfma_f32_32x32x16_bf16 v[216:231], v[48:51], v[184:187], v[216:231]\n"
    "ds_read_b128 v[40:43], v27 offset:31200\n"
    "s_waitcnt lgkmcnt(5)\n"
    "v_mfma_f32_32x32x16_bf16 v[232:247], v[52:55], v[184:187], v[232:247]\n"
    "ds_read_b128 v[44:47], v27 offset:31712\n"
    "s_waitcnt vmcnt(5) lgkmcnt(5)\n"
    "v_mfma_f32_32x32x16_bf16 v[216:231], v[56:59], v[188:191], v[216:231]\n"
    "ds_read_b128 v[48:51], v27 offset:33280\n"
    "s_waitcnt lgkmcnt(5)\n"
    "v_mfma_f32_32x32x16_bf16 v[232:247], v[60:63], v[188:191], v[232:247]\n"
    "ds_read_b128 v[52:55], v27 offset:33792\n"
    "s_mov_b64 s[28:29], s[12:13]\n"
    "s_mul_i32 s48, s36, 21\n"
    "v_add_u32 v30, s48, v2\n"
    "s_add_u32 s48, s48, 21\n"
    "s_min_u32 s48, s48, 162\n"
    "v_lshlrev_b32 v29, 7, v30\n"
    "v_cmp_gt_u32 vcc, s48, v30\n"
    "s_nop 1\n"
    "v_cndmask_b32 v29, v11, v29, vcc\n"
    "s_mul_i32 s48, s38, 20736\n"
    "s_mul_i32 s49, s37, 663552\n"
    "s_add_u32 s49, s49, s48\n"
    "buffer_load_dword v30, v29, s[28:31], s49 offen\n"
    "s_waitcnt vmcnt(5) lgkmcnt(5)\n"
    "v_mfma_f32_32x32x16_bf16 v[216:231], v[32:35], v[192:195], v[216:231]\n"
    "ds_read_b128 v[56:59], v27 offset:35360\n"
    "s_waitcnt lgkmcnt(5)\n"
    "v_mfma_f32_32x32x16_bf16 v[232:247], v[36:39], v[192:195], v[232:247]\n"
    "ds_read_b128 v[60:63], v27 offset:35872\n"
    "s_waitcnt vmcnt(4) lgkmcnt(5)\n"
    "v_mfma_f32_32x32x16_bf16 v[216:231], v[40:43], v[196:199], v[216:231]\n"
    "s_waitcnt lgkmcnt(4)\n"
    "v_mfma_f32_32x32x16_bf16 v[232:247], v[44:47], v[196:199], v[232:247]\n"
    "s_waitcnt vmcnt(3) lgkmcnt(3)\n"
    "v_mfma_f32_32x32x16_bf16 v[216:231], v[48:51], v[200:203], v[216:231]\n"
    "s_waitcnt lgkmcnt(2)\n"
    "v_mfma_f32_32x32x16_bf16 v[232:247], v[52:55], v[200:203], v[232:247]\n"
    "s_waitcnt vmcnt(2) lgkmcnt(1)\n"
    "v_mfma_f32_32x32x16_bf16 v[216:231], v[56:59], v[204:207], v[216:231]\n"
    "s_waitcnt lgkmcnt(0)\n"
    "v_mfma_f32_32x32x16_bf16 v[232:247], v[60:63], v[204:207], v[232:247]\n"
    "s_setprio 0\n"
    "s_nop 8\n"
    "v_pk_fma_f32 v[216:217], v[216:217], v[16:17], v[18:19]\n"
    "v_pk_fma_f32 v[218:219], v[218:219], v[16:17], v[18:19]\n"
    "v_pk_fma_f32 v[220:221], v[220:221], v[16:17], v[18:19]\n"
    "v_pk_fma_f32 v[222:223], v[222:223], v[16:17], v[18:19]\n"
    "v_pk_fma_f32 v[224:225], v[224:225], v[16:17], v[18:19]\n"
    "v_pk_fma_f32 v[226:227], v[226:227], v[16:17], v[18:19]\n"
    "v_pk_fma_f32 v[228:229], v[228:229], v[16:17], v[18:19]\n"
    "v_pk_fma_f32 v[230:231], v[230:231], v[16:17], v[18:19]\n"
    "v_cvt_pk_bf16_f32 v64, v216, v217\n"
    "v_cvt_pk_bf16_f32 v65, v218, v219\n"
    "v_cvt_pk_bf16_f32 v66, v220, v221\n"
    "v_cvt_pk_bf16_f32 v67, v222, v223\n"
    "v_cvt_pk_bf16_f32 v68, v224, v225\n"
    "v_cvt_pk_bf16_f32 v69, v226, v227\n"
    "v_cvt_pk_bf16_f32 v70, v228, v229\n"
    "v_cvt_pk_bf16_f32 v71, v230, v231\n"
    "buffer_store_dwordx4 v[64:67], v20, s[20:23], s41 offen offset:1024 sc1\n"
    "buffer_store_dwordx4 v[68:71], v20, s[20:23], s41 offen offset:1664 sc1\n"
    "v_pk_fma_f32 v[232:233], v[232:233], v[16:17], v[18:19]\n"
    "v_pk_fma_f32 v[234:235], v[234:235], v[16:17], v[18:19]\n"
    "v_pk_fma_f32 v[236:237], v[236:237], v[16:17], v[18:19]\n"
    "v_pk_fma_f32 v[238:239], v[238:239], v[16:17], v[18:19]\n"
    "v_pk_fma_f32 v[240:241], v[240:241], v[16:17], v[18:19]\n"
    "v_pk_fma_f32 v[242:243], v[242:243], v[16:17], v[18:19]\n"
    "v_pk_fma_f32 v[244:245], v[244:245], v[16:17], v[18:19]\n"
    "v_pk_fma_f32 v[246:247], v[246:247], v[16:17], v[18:19]\n"
    "v_cvt_pk_bf16_f32 v72, v232, v233\n"
    "v_cvt_pk_bf16_f32 v73, v234, v235\n"
    "v_cvt_pk_bf16_f32 v74, v236, v237\n"
    "v_cvt_pk_bf16_f32 v75, v238, v239\n"
    "v_cvt_pk_bf16_f32 v76, v240, v241\n"
    "v_cvt_pk_bf16_f32 v77, v242, v243\n"
    "v_cvt_pk_bf16_f32 v78, v244, v245\n"
    "v_cvt_pk_bf16_f32 v79, v246, v247\n"
    "buffer_store_dwordx4 v[72:75], v20, s[20:23], s42 offen offset:1024 sc1\n"
    "buffer_store_dwordx4 v[76:79], v20, s[20:23], s42 offen offset:1664 sc1\n"
    "s_mov_b32 exec_lo, 0xf00000\n"
    "s_mov_b32 exec_hi, exec_lo\n"
    "ds_write_b16 v13, v64 offset:8192\n"
    "ds_write_b16_d16_hi v13, v64 offset:8196\n"
    "ds_write_b16 v13, v65 offset:8200\n"
    "ds_write_b16_d16_hi v13, v65 offset:8204\n"
    "ds_write_b16 v13, v66 offset:8224\n"
    "ds_write_b16_d16_hi v13, v66 offset:8228\n"
    "ds_write_b16 v13, v67 offset:8232\n"
    "ds_write_b16_d16_hi v13, v67 offset:8236\n"
    "ds_write_b16 v13, v68 offset:8256\n"
    "ds_write_b16_d16_hi v13, v68 offset:8260\n"
    "ds_write_b16 v13, v69 offset:8264\n"
    "ds_write_b16_d16_hi v13, v69 offset:8268\n"
    "ds_write_b16 v13, v70 offset:8288\n"
    "ds_write_b16_d16_hi v13, v70 offset:8292\n"
    "ds_write_b16 v13, v71 offset:8296\n"
    "ds_write_b16_d16_hi v13, v71 offset:8300\n"
    "ds_write_b16 v13, v72 offset:8320\n"
    "ds_write_b16_d16_hi v13, v72 offset:8324\n"
    "ds_write_b16 v13, v73 offset:8328\n"
    "ds_write_b16_d16_hi v13, v73 offset:8332\n"
    "ds_write_b16 v13, v74 offset:8352\n"
    "ds_write_b16_d16_hi v13, v74 offset:8356\n"
    "ds_write_b16 v13, v75 offset:8360\n"
    "ds_write_b16_d16_hi v13, v75 offset:8364\n"
    "ds_write_b16 v13, v76 offset:8384\n"
    "ds_write_b16_d16_hi v13, v76 offset:8388\n"
    "ds_write_b16 v13, v77 offset:8392\n"
    "ds_write_b16_d16_hi v13, v77 offset:8396\n"
    "ds_write_b16 v13, v78 offset:8416\n"
    "ds_write_b16_d16_hi v13, v78 offset:8420\n"
    "ds_write_b16 v13, v79 offset:8424\n"
    "ds_write_b16_d16_hi v13, v79 offset:8428\n"
    "s_mov_b64 exec, -1\n"
    "ds_read_b128 v[80:83], v12 offset:8448\n"
    "ds_write_b128 v21, v[64:67] offset:4096\n"
    "ds_write_b128 v21, v[68:71] offset:5120\n"
    "ds_write_b128 v21, v[72:75] offset:6144\n"
    "ds_write_b128 v21, v[76:79] offset:7168\n"
    "s_waitcnt lgkmcnt(4)\n"
    "buffer_store_dwordx4 v[80:83], v14, s[20:23], s41 offen offset:2304 sc1\n"
    ".Lk_aj_18:\n"
    "s_waitcnt lgkmcnt(0)\n"
    "s_barrier\n"
    "s_lshl_b32 s44, s40, 10\n"
    "v_add_u32 v13, s44, v12\n"
    "s_xor_b32 s45, s44, 1024\n"
    "v_add_u32 v14, s45, v12\n"
    "v_add_u32 v15, s44, v13\n"
    "s_lshl_b32 s44, s40, 7\n"
    "v_xor_b32 v16, s44, v6\n"
    "v_add_u32 v16, s43, v16\n"
    "v_add_u32 v17, s43, v4\n"
    "v_add_u32 v18, s44, v17\n"
    "ds_read_b32 v184, v16 offset:8192\n"
    "ds_read_b32 v36, v17 offset:8448\n"
    "ds_read_b128 v[32:35], v12 offset:2048\n"
    "ds_read_b128 v[176:179], v13\n"
    "ds_read_b128 v[180:183], v14\n"
    "ds_read_b32 v200, v18 offset:8448\n"
    "ds_read_b128 v[188:191], v13 offset:2048\n"
    "ds_read_b128 v[208:211], v15 offset:4096\n"
    "ds_read_b128 v[212:215], v15 offset:5120\n"
    "v_mov_b32 v185, 0\n"
    "v_mov_b32 v37, 0x3f803f80\n"
    "v_mov_b32 v201, 0x3f803f80\n"
    "v_mov_b32 v203, 0x3f803f80\n"
    "v_mov_b32 v205, 0x3f803f80\n"
    "v_mov_b32 v9, s95\n"
    "s_lshl_b32 s46, s37, 12\n"
    "s_lshl_b32 s98, s38, 2\n"
    "s_add_u32 s98, s98, s46\n"
    "s_lshl_b32 s99, s38, 7\n"
    "s_add_u32 s99, s99, s46\n"
    "v_and_b32 v10, 0x7c, v6\n"
    "v_lshlrev_b32 v19, 7, v2\n"
    "v_cmp_gt_u32 vcc, 32, v2\n"
    "s_nop 1\n"
    "v_cndmask_b32 v5, v11, v19, vcc\n"
    "s_mul_i32 s58, s40, 77824\n"
    "s_add_u32 s58, s58, s41\n"
    "s_sub_u32 s59, 32, s38\n"
    "s_mul_i32 s59, s59, 2432\n"
    "s_add_u32 s59, s59, s58\n"
    "v_mov_b32 v20, 0\n"
    "v_mov_b32 v24, 162816\n"
    "ds_write_b32 v24, v20\n"
    "s_waitcnt lgkmcnt(0)\n"
    "s_waitcnt vmcnt(0)\n"
    "s_barrier\n"
    "s_cmp_lg_u32 s36, 7\n"
    "s_cbranch_scc1 .Lk_pub_27\n"
    "buffer_store_dword v9, v5, s[20:23], s98 offen sc1\n"
    "buffer_load_dword v8, v10, s[20:23], s99 offen sc1\n"
    ".Lk_pub_27:\n"
    "v_mfma_f32_32x32x4_2b_bf16 v[64:95], v[36:37], v[184:185], 0\n"
    "s_nop 15\n"
    "s_nop 1\n"
    "v_mfma_f32_32x32x16_bf16 v[64:79], v[32:35], v[176:179], v[64:79]\n"
    "v_mfma_f32_32x32x16_bf16 v[80:95], v[32:35], v[180:183], v[80:95]\n"
    "s_nop 10\n"
    "v_max3_f32 v20, v64, v65, v66\n"
    "v_max3_f32 v20, v20, v67, v68\n"
    "v_max3_f32 v20, v20, v69, v70\n"
    "v_max3_f32 v20, v20, v71, v72\n"
    "v_max3_f32 v20, v20, v73, v74\n"
    "v_max3_f32 v20, v20, v75, v76\n"
    "v_max3_f32 v20, v20, v77, v78\n"
    "v_max_f32 v20, v20, v79\n"
    "v_max3_f32 v21, v80, v81, v82\n"
    "v_max3_f32 v21, v21, v83, v84\n"
    "v_max3_f32 v21, v21, v85, v86\n"
    "v_max3_f32 v21, v21, v87, v88\n"
    "v_max3_f32 v21, v21, v89, v90\n"
    "v_max3_f32 v21, v21, v91, v92\n"
    "v_max3_f32 v21, v21, v93, v94\n"
    "v_max_f32 v21, v21, v95\n"
    "s_nop 1\n"
    "v_permlane32_swap_b32 v20, v21\n"
    "v_max_f32 v20, v20, v21\n"
    "v_cvt_pk_bf16_f32 v21, v20, v20\n"
    "v_lshlrev_b32 v21, 16, v21\n"
    "v_mov_b32 v23, 0x49800000\n"
    "v_cmp_nle_f32 s[56:57], abs(v20), v23\n"
    "v_sub_f32 v22, v21, v20\n"
    "v_cvt_pk_bf16_f32 v185, -v21, v22\n"
    "s_add_u32 s46, s98, 32768\n"
    "s_add_u32 s44, s99, 32768\n"
    "v_mov_b32 v16, v5\n"
    "v_mov_b32 v17, v10\n"
    "s_xor_b32 s48, s40, 1\n"
    "s_lshl_b32 s48, s48, 7\n"
    "s_lshl_b32 s61, s38, 2\n"
    "s_add_u32 s61, s61, s39\n"
    "s_add_u32 s61, s61, s48\n"
    "v_lshrrev_b32 v20, 3, v2\n"
    "v_and_b32 v21, 7, v2\n"
    "s_mul_i32 s48, s39, 12288\n"
    "s_add_u32 s48, s48, 92160\n"
    "v_and_b32 v25, 3, v2\n"
    "v_lshl_add_u32 v25, v25, 3, s48\n"
    "v_bfe_u32 v26, v2, 2, 2\n"
    "v_lshl_add_u32 v25, v26, 7, v25\n"
    "v_lshrrev_b32 v26, 4, v2\n"
    "v_lshl_add_u32 v12, v26, 10, v25\n"
    "v_lshl_add_u32 v13, v26, 9, v25\n"
    "s_and_b32 s14, %[bid], 0\n"
    "s_lshl_b32 s14, s14, 3\n"
    "s_bfe_u32 s41, %[bid], 0x30003\n"
    "s_or_b32 s14, s14, s41\n"
    "s_bfe_u32 s15, %[bid], 0x30000\n"
    "s_lshl_b32 s15, s15, 2\n"
    "s_lshr_b32 s41, %[bid], 6\n"
    "s_or_b32 s15, s15, s41\n"
    "s_cmp_gt_u32 s36, 4\n"
    "s_cbranch_scc1 .Lk_lt_29\n"
    "v_min_u32 v113, 35, v2\n"
    "s_cmp_eq_u32 s36, 4\n"
    "s_cbranch_scc1 .Lk_lt_28\n"
    "s_mul_i32 s53, s36, 36\n"
    "v_add_u32 v113, s53, v113\n"
    "v_lshlrev_b32 v112, 4, v113\n"
    "s_and_b32 s53, %[bid], 3\n"
    "s_mul_i32 s53, s53, 13824\n"
    "s_add_u32 s53, s53, 203520\n"
    "buffer_load_dwordx4 v[96:99], v112, s[20:23], s53 offen sc1\n"
    "buffer_load_dwordx4 v[100:103], v112, s[20:23], s53 offen offset:2304 sc1\n"
    "s_branch .Lk_lt_29\n"
    ".Lk_lt_28:\n"
    "s_sub_u32 s53, s37, s14\n"
    "s_mul_i32 s53, s53, 288\n"
    "s_add_u32 s53, s53, 6624\n"
    "v_cmp_gt_u32 vcc, 18, v113\n"
    "v_mov_b32 v112, s53\n"
    "s_nop 0\n"
    "v_cndmask_b32 v112, v112, 0, vcc\n"
    "v_lshl_add_u32 v112, v113, 4, v112\n"
    "s_and_b32 s53, %[bid], 3\n"
    "s_mul_i32 s53, s53, 13824\n"
    "s_add_u32 s53, s53, 201216\n"
    "s_mul_i32 s54, s14, 288\n"
    "s_add_u32 s53, s53, s54\n"
    "buffer_load_dwordx4 v[96:99], v112, s[20:23], s53 offen sc1\n"
    ".Lk_lt_29:\n"
    "s_cmp_lg_u32 s36, 5\n"
    "s_cbranch_scc1 .Lk_tl_30\n"
    "s_lshl_b32 s48, s38, 3\n"
    "s_add_u32 s48, s48, s37\n"
    "s_mul_i32 s48, s48, 128\n"
    "s_add_u32 s48, s48, 65536\n"
    "v_lshlrev_b32 v250, 15, v2\n"
    "v_add_u32 v250, s48, v250\n"
    "v_cmp_gt_u32 vcc, 3, v2\n"
    "s_nop 1\n"
    "v_cndmask_b32 v250, v11, v250, vcc\n"
    "v_bfe_u32 v28, v2, 1, 2\n"
    "s_lshl_b32 s48, s38, 2\n"
    "v_add_u32 v28, s48, v28\n"
    "v_and_b32 v29, 1, v2\n"
    "v_lshlrev_b32 v29, 7, v29\n"
    "s_lshl_b32 s48, s37, 8\n"
    "v_add_u32 v29, s48, v29\n"
    "v_lshrrev_b32 v251, 3, v2\n"
    "v_mul_u32_u24 v30, 2048, v28\n"
    "v_add_u32 v30, v30, v29\n"
    "v_add_u32 v30, 13053952, v30\n"
    "v_cmp_eq_u32 vcc, 1, v251\n"
    "s_nop 1\n"
    "v_cndmask_b32 v250, v250, v30, vcc\n"
    "v_mul_u32_u24 v30, 18432, v28\n"
    "v_add_u32 v30, v30, v29\n"
    "v_add_u32 v30, 7614464, v30\n"
    "v_cmp_eq_u32 vcc, 2, v251\n"
    "s_nop 1\n"
    "v_cndmask_b32 v250, v250, v30, vcc\n"
    "buffer_load_dword v251, v250, s[20:23], 0 offen sc1\n"
    ".Lk_tl_30:\n"
    "v_mfma_f32_32x32x4_2b_bf16 v[64:95], v[200:201], v[184:185], 0\n"
    "s_nop 15\n"
    "s_nop 1\n"
    "v_mfma_f32_32x32x16_bf16 v[64:79], v[188:191], v[176:179], v[64:79]\n"
    "v_mfma_f32_32x32x16_bf16 v[80:95], v[188:191], v[180:183], v[80:95]\n"
    "s_nop 10\n"
    "v_exp_f32 v64, v64\n"
    "v_exp_f32 v65, v65\n"
    "v_exp_f32 v66, v66\n"
    "v_cvt_pk_bf16_f32 v160, v64, v65\n"
    "v_exp_f32 v67, v67\n"
    "v_exp_f32 v68, v68\n"
    "v_cvt_pk_bf16_f32 v161, v66, v67\n"
    "v_exp_f32 v69, v69\n"
    "v_exp_f32 v70, v70\n"
    "v_cvt_pk_bf16_f32 v162, v68, v69\n"
    "v_exp_f32 v71, v71\n"
    "v_exp_f32 v72, v72\n"
    "v_cvt_pk_bf16_f32 v163, v70, v71\n"
    "v_exp_f32 v73, v73\n"
    "s_nop 0\n"
    "v_mfma_f32_32x32x16_bf16 v[128:143], v[208:211], v[160:163], 0\n"
    "v_exp_f32 v74, v74\n"
    "v_cvt_pk_bf16_f32 v164, v72, v73\n"
    "v_exp_f32 v75, v75\n"
    "v_exp_f32 v76, v76\n"
    "v_cvt_pk_bf16_f32 v165, v74, v75\n"
    "v_exp_f32 v77, v77\n"
    "v_exp_f32 v78, v78\n"
    "v_cvt_pk_bf16_f32 v166, v76, v77\n"
    "v_exp_f32 v79, v79\n"
    "v_exp_f32 v80, v80\n"
    "v_cvt_pk_bf16_f32 v167, v78, v79\n"
    "v_exp_f32 v81, v81\n"
    "v_exp_f32 v82, v82\n"
    "v_cvt_pk_bf16_f32 v168, v80, v81\n"
    "v_mfma_f32_32x32x16_bf16 v[128:143], v[212:215], v[164:167], v[128:143]\n"
    "v_exp_f32 v83, v83\n"
    "v_exp_f32 v84, v84\n"
    "v_cvt_pk_bf16_f32 v169, v82, v83\n"
    "v_exp_f32 v85, v85\n"
    "v_exp_f32 v86, v86\n"
    "v_cvt_pk_bf16_f32 v170, v84, v85\n"
    "v_exp_f32 v87, v87\n"
    "v_exp_f32 v88, v88\n"
    "v_cvt_pk_bf16_f32 v171, v86, v87\n"
    "v_exp_f32 v89, v89\n"
    "v_exp_f32 v90, v90\n"
    "v_cvt_pk_bf16_f32 v172, v88, v89\n"
    "v_mfma_f32_32x32x16_bf16 v[144:159], v[208:211], v[168:171], 0\n"
    "v_exp_f32 v91, v91\n"
    "v_exp_f32 v92, v92\n"
    "v_cvt_pk_bf16_f32 v173, v90, v91\n"
    "v_exp_f32 v93, v93\n"
    "v_exp_f32 v94, v94\n"
    "v_cvt_pk_bf16_f32 v174, v92, v93\n"
    "v_exp_f32 v95, v95\n"
    "s_nop 0\n"
    "v_cvt_pk_bf16_f32 v175, v94, v95\n"
    "s_nop 1\n"
    "v_mfma_f32_32x32x16_bf16 v[144:159], v[212:215], v[172:175], v[144:159]\n"
    "s_cmp_gt_u32 s36, 4\n"
    "s_cbranch_scc1 .Lk_lt_32\n"
    "s_cmp_eq_u32 s36, 4\n"
    "s_cbranch_scc1 .Lk_lt_31\n"
    "v_lshlrev_b32 v113, 5, v113\n"
    "v_add_u32 v113, 156672, v113\n"
    "s_mov_b32 s60, 0x8000\n"
    ".Lk_sp_33:\n"
    "s_waitcnt vmcnt(0)\n"
    "v_cmp_ne_u32 vcc, s95, v96\n"
    "v_cmp_ne_u32 s[54:55], s95, v98\n"
    "s_or_b64 vcc, vcc, s[54:55]\n"
    "v_cmp_ne_u32 s[54:55], s95, v100\n"
    "s_or_b64 vcc, vcc, s[54:55]\n"
    "v_cmp_ne_u32 s[54:55], s95, v102\n"
    "s_or_b64 vcc, vcc, s[54:55]\n"
    "s_cbranch_scc0 .Lk_spd_34\n"
    "s_sub_u32 s60, s60, 1\n"
    "s_cbranch_scc1 .Lk_spd_34\n"
    "s_sleep 1\n"
    "buffer_load_dwordx4 v[96:99], v112, s[20:23], s53 offen sc1\n"
    "buffer_load_dwordx4 v[100:103], v112, s[20:23], s53 offen offset:2304 sc1\n"
    "s_branch .Lk_sp_33\n"
    ".Lk_spd_34:\n"
    "v_lshlrev_b32 v108, 16, v101\n"
    "v_and_b32 v109, 0xffff0000, v101\n"
    "v_lshlrev_b32 v110, 16, v103\n"
    "v_and_b32 v111, 0xffff0000, v103\n"
    "v_add_f32 v108, 1.0, v108\n"
    "v_add_f32 v109, 1.0, v109\n"
    "v_add_f32 v110, 1.0, v110\n"
    "v_add_f32 v111, 1.0, v111\n"
    "v_cvt_pk_bf16_f32 v101, v108, v109\n"
    "v_cvt_pk_bf16_f32 v103, v110, v111\n"
    "v_lshlrev_b32 v104, 16, v97\n"
    "v_and_b32 v105, 0xffff0000, v97\n"
    "v_lshlrev_b32 v106, 16, v99\n"
    "v_and_b32 v107, 0xffff0000, v99\n"
    "v_lshlrev_b32 v108, 16, v101\n"
    "v_and_b32 v109, 0xffff0000, v101\n"
    "v_lshlrev_b32 v110, 16, v103\n"
    "v_and_b32 v111, 0xffff0000, v103\n"
    "v_mul_f32 v104, 0xbfb8aa3b, v104\n"
    "v_mul_f32 v105, 0xbfb8aa3b, v105\n"
    "v_mul_f32 v106, 0xbfb8aa3b, v106\n"
    "v_mul_f32 v107, 0xbfb8aa3b, v107\n"
    "v_mul_f32 v108, 0xbfb8aa3b, v108\n"
    "v_mul_f32 v109, 0xbfb8aa3b, v109\n"
    "v_mul_f32 v110, 0xbfb8aa3b, v110\n"
    "v_mul_f32 v111, 0xbfb8aa3b, v111\n"
    "ds_write_b128 v113, v[108:111]\n"
    "ds_write_b128 v113, v[104:107] offset:16\n"
    "s_branch .Lk_lt_32\n"
    ".Lk_lt_31:\n"
    "v_cmp_gt_u32 vcc, 18, v113\n"
    "v_mov_b32 v114, 0xbf317218\n"
    "s_nop 0\n"
    "v_cndmask_b32 v114, v114, 1.0, vcc\n"
    "v_lshlrev_b32 v113, 4, v113\n"
    "v_add_u32 v113, 161792, v113\n"
    "s_mov_b32 s60, 0x8000\n"
    ".Lk_sp_35:\n"
    "s_waitcnt vmcnt(0)\n"
    "v_cmp_ne_u32 vcc, s95, v96\n"
    "v_cmp_ne_u32 s[54:55], s95, v98\n"
    "s_or_b64 vcc, vcc, s[54:55]\n"
    "s_cbranch_scc0 .Lk_spd_36\n"
    "s_sub_u32 s60, s60, 1\n"
    "s_cbranch_scc1 .Lk_spd_36\n"
    "s_sleep 1\n"
    "buffer_load_dwordx4 v[96:99], v112, s[20:23], s53 offen sc1\n"
    "s_branch .Lk_sp_35\n"
    ".Lk_spd_36:\n"
    "v_lshlrev_b32 v104, 16, v97\n"
    "v_and_b32 v105, 0xffff0000, v97\n"
    "v_lshlrev_b32 v106, 16, v99\n"
    "v_and_b32 v107, 0xffff0000, v99\n"
    "v_mul_f32 v104, v114, v104\n"
    "v_mul_f32 v105, v114, v105\n"
    "v_mul_f32 v106, v114, v106\n"
    "v_mul_f32 v107, v114, v107\n"
    "ds_write_b128 v113, v[104:107]\n"
    ".Lk_lt_32:\n"
    "s_mul_i32 s48, s36, 11520\n"
    "s_min_u32 s48, s48, 79872\n"
    "s_lshl_b32 s51, s14, 5\n"
    "s_add_u32 s51, s51, s38\n"
    "s_mul_i32 s51, s51, 128\n"
    "s_add_u32 s51, s51, 0\n"
    "s_mul_i32 s97, s14, 92160\n"
    "s_add_u32 s97, s97, 9957376\n"
    "s_add_u32 s97, s97, s48\n"
    "s_mul_i32 s49, s37, 1327104\n"
    "s_add_u32 s49, s49, 13316096\n"
    "s_mul_i32 s96, s36, 73728\n"
    "s_add_u32 s96, s96, s49\n"
    "v_add_u32 v252, 18432, v1\n"
    "v_add_u32 v253, 36864, v1\n"
    "v_add_u32 v254, 55296, v1\n"
    "s_lshl_b32 s48, s15, 2\n"
    "s_add_u32 s48, s48, s39\n"
    "s_mul_i32 s48, s48, 18432\n"
    "s_mul_i32 s49, s14, 144\n"
    "s_add_u32 s48, s48, s49\n"
    "s_lshl_b32 s49, s40, 6\n"
    "s_add_u32 s52, s48, s49\n"
    "v_and_b32 v25, 15, v2\n"
    "v_lshrrev_b32 v26, 4, v2\n"
    "v_mul_u32_u24 v25, 1152, v25\n"
    "v_lshl_add_u32 v246, v26, 4, v25\n"
    "v_lshl_add_u32 v25, v26, 3, v25\n"
    "s_cmp_eq_u32 s40, 0\n"
    "s_cselect_b32 s48, 32, 0\n"
    "v_cmp_gt_u32 vcc, s48, v2\n"
    "s_nop 1\n"
    "v_cndmask_b32 v247, v11, v25, vcc\n"
    "s_cmp_lg_u32 s36, 7\n"
    "s_cbranch_scc1 .Lk_polld_38\n"
    "s_mov_b32 s60, 0x8000\n"
    ".Lk_poll_37:\n"
    "s_waitcnt vmcnt(0)\n"
    "v_cmp_ne_u32 vcc, s95, v8\n"
    "s_cbranch_vccz .Lk_polld_38\n"
    "s_sub_u32 s60, s60, 1\n"
    "s_cbranch_scc1 .Lk_polld_38\n"
    "s_sleep 1\n"
    "buffer_load_dword v8, v10, s[20:23], s99 offen sc1\n"
    "s_branch .Lk_poll_37\n"
    ".Lk_polld_38:\n"
    "s_barrier\n"
    "s_lshl_b32 s48, s38, 10\n"
    "s_add_u32 s48, s48, 65536\n"
    "s_lshl_b32 s98, s37, 2\n"
    "s_add_u32 s98, s98, s48\n"
    "s_lshl_b32 s48, s15, 10\n"
    "s_add_u32 s48, s48, 65536\n"
    "s_lshl_b32 s99, s14, 7\n"
    "s_add_u32 s99, s99, s48\n"
    "v_and_b32 v10, 0x1c, v6\n"
    "v_cmp_gt_u32 vcc, 8, v2\n"
    "s_nop 1\n"
    "v_cndmask_b32 v5, v11, v5, vcc\n"
    "s_add_u32 s58, s58, 2432\n"
    "s_cmp_eq_u32 s58, s59\n"
    "s_cselect_b32 s41, 77824, 0\n"
    "s_sub_u32 s58, s58, s41\n"
    "buffer_load_dword v202, v4, s[20:23], s58 offen offset:2304 sc1\n"
    "buffer_load_dwordx4 v[192:195], v1, s[20:23], s58 offen sc1\n"
    "buffer_load_dwordx4 v[216:219], v3, s[20:23], s58 offen offset:1024 sc1\n"
    "buffer_load_dwordx4 v[220:223], v3, s[20:23], s58 offen offset:1664 sc1\n"
    "s_add_u32 s58, s58, 2432\n"
    "s_cmp_eq_u32 s58, s59\n"
    "s_cselect_b32 s41, 77824, 0\n"
    "s_sub_u32 s58, s58, s41\n"
    "buffer_load_dword v204, v4, s[20:23], s58 offen offset:2304 sc1\n"
    "buffer_load_dwordx4 v[196:199], v1, s[20:23], s58 offen sc1\n"
    "buffer_load_dwordx4 v[224:227], v3, s[20:23], s58 offen offset:1024 sc1\n"
    "buffer_load_dwordx4 v[228:231], v3, s[20:23], s58 offen offset:1664 sc1\n"
    "s_add_u32 s58, s58, 2432\n"
    "s_cmp_eq_u32 s58, s59\n"
    "s_cselect_b32 s41, 77824, 0\n"
    "s_sub_u32 s58, s58, s41\n"
    "buffer_load_dword v200, v4, s[20:23], s58 offen offset:2304 sc1\n"
    "buffer_load_dwordx4 v[188:191], v1, s[20:23], s58 offen sc1\n"
    "buffer_load_dwordx4 v[232:235], v3, s[20:23], s58 offen offset:1024 sc1\n"
    "buffer_load_dwordx4 v[236:239], v3, s[20:23], s58 offen offset:1664 sc1\n"
    "s_waitcnt vmcnt(11)\n"
    "v_mfma_f32_32x32x4_2b_bf16 v[96:127], v[202:203], v[184:185], 0\n"
    "s_add_u32 s58, s58, 2432\n"
    "s_cmp_eq_u32 s58, s59\n"
    "s_cselect_b32 s41, 77824, 0\n"
    "s_sub_u32 s58, s58, s41\n"
    "buffer_load_dword v202, v4, s[20:23], s58 offen offset:2304 sc1\n"
    "s_waitcnt vmcnt(11)\n"
    "s_nop 11\n"
    "v_mfma_f32_32x32x16_bf16 v[96:111], v[192:195], v[176:179], v[96:111]\n"
    "v_mfma_f32_32x32x16_bf16 v[112:127], v[192:195], v[180:183], v[112:127]\n"
    "buffer_load_dwordx4 v[192:195], v1, s[20:23], s58 offen sc1\n"
    "s_waitcnt vmcnt(9)\n"
    "v_mfma_f32_32x32x4_2b_bf16 v[64:95], v[204:205], v[184:185], 0\n"
    "s_nop 7\n"
    "v_exp_f32 v96, v96\n"
    "v_exp_f32 v97, v97\n"
    "s_cmp_eq_u32 s40, 0\n"
    "s_cbranch_scc1 .Lk_pr_40\n"
    "s_setprio 1\n"
    ".Lk_pr_40:\n"
    "v_exp_f32 v98, v98\n"
    "v_cvt_pk_bf16_f32 v160, v96, v97\n"
    "v_exp_f32 v99, v99\n"
    "v_exp_f32 v100, v100\n"
    "v_cvt_pk_bf16_f32 v161, v98, v99\n"
    "v_exp_f32 v101, v101\n"
    "v_exp_f32 v102, v102\n"
    "v_cvt_pk_bf16_f32 v162, v100, v101\n"
    "v_exp_f32 v103, v103\n"
    "v_exp_f32 v104, v104\n"
    "v_cvt_pk_bf16_f32 v163, v102, v103\n"
    "v_exp_f32 v105, v105\n"
    "v_exp_f32 v106, v106\n"
    "buffer_load_dwordx4 v[208:211], v3, s[20:23], s58 offen offset:1024 sc1\n"
    "buffer_load_dwordx4 v[212:215], v3, s[20:23], s58 offen offset:1664 sc1\n"
    "v_cvt_pk_bf16_f32 v164, v104, v105\n"
    "v_exp_f32 v107, v107\n"
    "v_exp_f32 v108, v108\n"
    "v_cvt_pk_bf16_f32 v165, v106, v107\n"
    "v_exp_f32 v109, v109\n"
    "s_waitcnt vmcnt(10)\n"
    "v_mfma_f32_32x32x16_bf16 v[64:79], v[196:199], v[176:179], v[64:79]\n"
    "v_exp_f32 v110, v110\n"
    "v_cvt_pk_bf16_f32 v166, v108, v109\n"
    "s_add_u32 s58, s58, 2432\n"
    "s_cmp_eq_u32 s58, s59\n"
    "s_cselect_b32 s41, 77824, 0\n"
    "s_sub_u32 s58, s58, s41\n"
    "buffer_load_dword v204, v4, s[20:23], s58 offen offset:2304 sc1\n"
    "v_exp_f32 v111, v111\n"
    "v_exp_f32 v112, v112\n"
    "v_cvt_pk_bf16_f32 v167, v110, v111\n"
    "v_exp_f32 v113, v113\n"
    "v_exp_f32 v114, v114\n"
    "v_mfma_f32_32x32x16_bf16 v[128:143], v[216:219], v[160:163], v[128:143]\n"
    "v_cvt_pk_bf16_f32 v168, v112, v113\n"
    "v_exp_f32 v115, v115\n"
    "v_exp_f32 v116, v116\n"
    "v_cvt_pk_bf16_f32 v169, v114, v115\n"
    "v_exp_f32 v117, v117\n"
    "v_exp_f32 v118, v118\n"
    "v_cvt_pk_bf16_f32 v170, v116, v117\n"
    "v_mfma_f32_32x32x16_bf16 v[80:95], v[196:199], v[180:183], v[80:95]\n"
    "v_exp_f32 v119, v119\n"
    "v_exp_f32 v120, v120\n"
    "buffer_load_dwordx4 v[196:199], v1, s[20:23], s58 offen sc1\n"
    "v_cvt_pk_bf16_f32 v171, v118, v119\n"
    "v_exp_f32 v121, v121\n"
    "v_exp_f32 v122, v122\n"
    "v_cvt_pk_bf16_f32 v172, v120, v121\n"
    "v_exp_f32 v123, v123\n"
    "v_mfma_f32_32x32x16_bf16 v[128:143], v[220:223], v[164:167], v[128:143]\n"
    "v_exp_f32 v124, v124\n"
    "v_cvt_pk_bf16_f32 v173, v122, v123\n"
    "s_cmp_lt_u32 s61, 128\n"
    "s_cbranch_scc0 .Lk_j_39\n"
    "s_mov_b32 s60, 0\n"
    "s_mov_b32 s48, s61\n"
    "s_mul_i32 s49, s48, 7282\n"
    "s_lshr_b32 s49, s49, 16\n"
    "s_mul_i32 s45, s49, 9\n"
    "s_sub_u32 s45, s48, s45\n"
    "s_mul_i32 s48, s45, 144\n"
    "s_mov_b32 s43, 144\n"
    "s_cmp_lt_u32 s45, 8\n"
    "s_cselect_b32 s48, s48, 128\n"
    "s_cselect_b32 s43, 16, s43\n"
    "s_cmp_eq_u32 s60, 0\n"
    "s_cselect_b32 s43, 16, s43\n"
    "v_mul_u32_u24 v19, s43, v21\n"
    "s_mul_i32 s43, s37, 18\n"
    "s_add_u32 s43, s43, s49\n"
    "s_mul_i32 s43, s43, 36864\n"
    "s_add_u32 s48, s48, s43\n"
    "s_mul_i32 s42, s37, 9\n"
    "s_add_u32 s42, s42, s45\n"
    "s_lshl_b32 s42, s42, 7\n"
    "s_mul_i32 s43, s49, 294912\n"
    "s_add_u32 s42, s42, s43\n"
    "s_mov_b32 s43, 9216\n"
    "s_cmp_eq_u32 s60, 0\n"
    "s_cselect_b32 s42, s42, s48\n"
    "s_cselect_b32 s43, s43, 1152\n"
    "s_cselect_b64 s[28:29], s[10:11], s[12:13]\n"
    "v_mad_u32_u24 v19, v20, s43, v19\n"
    "s_lshl_b32 s43, s43, 3\n"
    "s_mul_i32 s45, s45, 72\n"
    "s_add_u32 s45, s45, s49\n"
    "s_mul_i32 s60, s60, 648\n"
    "s_add_u32 s45, s45, s60\n"
    "s_mul_i32 s60, s37, 1296\n"
    "s_add_u32 s45, s45, s60\n"
    "s_lshl_b32 s45, s45, 10\n"
    "s_add_u32 s45, s45, 13316096\n"
    "s_mul_i32 s48, s39, 12288\n"
    "s_add_u32 m0, s48, 92160\n"
    "s_mov_b32 s30, 5308416\n"
    "s_mov_b32 s31, 0x20000\n"
    "buffer_load_dwordx4 v19, s[28:31], s42 offen lds\n"
    "s_add_u32 m0, m0, 1024\n"
    "s_add_u32 s42, s42, s43\n"
    "buffer_load_dwordx4 v19, s[28:31], s42 offen lds\n"
    ".Lk_j_39:\n"
    "v_exp_f32 v125, v125\n"
    "v_exp_f32 v126, v126\n"
    "v_cvt_pk_bf16_f32 v174, v124, v125\n"
    "v_exp_f32 v127, v127\n"
    "v_exp_f32 v64, v64\n"
    "v_cvt_pk_bf16_f32 v175, v126, v127\n"
    "s_waitcnt vmcnt(9)\n"
    "v_mfma_f32_32x32x4_2b_bf16 v[96:127], v[200:201], v[184:185], 0\n"
    "v_exp_f32 v65, v65\n"
    "v_exp_f32 v66, v66\n"
    "v_cvt_pk_bf16_f32 v160, v64, v65\n"
    "v_exp_f32 v67, v67\n"
    "v_exp_f32 v68, v68\n"
    "v_cvt_pk_bf16_f32 v161, v66, v67\n"
    "v_mfma_f32_32x32x16_bf16 v[144:159], v[216:219], v[168:171], v[144:159]\n"
    "v_exp_f32 v69, v69\n"
    "v_exp_f32 v70, v70\n"
    "v_cvt_pk_bf16_f32 v162, v68, v69\n"
    "v_exp_f32 v71, v71\n"
    "v_exp_f32 v72, v72\n"
    "v_cvt_pk_bf16_f32 v163, v70, v71\n"
    "v_exp_f32 v73, v73\n"
    "v_mfma_f32_32x32x16_bf16 v[144:159], v[220:223], v[172:175], v[144:159]\n"
    "v_exp_f32 v74, v74\n"
    "v_cvt_pk_bf16_f32 v164, v72, v73\n"
    "buffer_load_dwordx4 v[216:219], v3, s[20:23], s58 offen offset:1024 sc1\n"
    "buffer_load_dwordx4 v[220:223], v3, s[20:23], s58 offen offset:1664 sc1\n"
    "v_exp_f32 v75, v75\n"
    "v_exp_f32 v76, v76\n"
    "v_cvt_pk_bf16_f32 v165, v74, v75\n"
    "v_exp_f32 v77, v77\n"
    "v_exp_f32 v78, v78\n"
    "s_waitcnt vmcnt(10)\n"
    "v_mfma_f32_32x32x16_bf16 v[96:111], v[188:191], v[176:179], v[96:111]\n"
    "v_cvt_pk_bf16_f32 v166, v76, v77\n"
    "v_exp_f32 v79, v79\n"
    "s_add_u32 s58, s58, 2432\n"
    "s_cmp_eq_u32 s58, s59\n"
    "s_cselect_b32 s41, 77824, 0\n"
    "s_sub_u32 s58, s58, s41\n"
    "buffer_load_dword v200, v4, s[20:23], s58 offen offset:2304 sc1\n"
    "v_exp_f32 v80, v80\n"
    "v_cvt_pk_bf16_f32 v167, v78, v79\n"
    "v_exp_f32 v81, v81\n"
    "v_exp_f32 v82, v82\n"
    "v_cvt_pk_bf16_f32 v168, v80, v81\n"
    "v_mfma_f32_32x32x16_bf16 v[128:143], v[224:227], v[160:163], v[128:143]\n"
    "v_exp_f32 v83, v83\n"
    "v_exp_f32 v84, v84\n"
    "v_cvt_pk_bf16_f32 v169, v82, v83\n"
    "v_exp_f32 v85, v85\n"
    "v_exp_f32 v86, v86\n"
    "v_cvt_pk_bf16_f32 v170, v84, v85\n"
    "v_exp_f32 v87, v87\n"
    "v_mfma_f32_32x32x16_bf16 v[112:127], v[188:191], v[180:183], v[112:127]\n"
    "v_exp_f32 v88, v88\n"
    "v_cvt_pk_bf16_f32 v171, v86, v87\n"
    "buffer_load_dwordx4 v[188:191], v1, s[20:23], s58 offen sc1\n"
    "v_exp_f32 v89, v89\n"
    "v_exp_f32 v90, v90\n"
    "v_cvt_pk_bf16_f32 v172, v88, v89\n"
    "v_exp_f32 v91, v91\n"
    "v_exp_f32 v92, v92\n"
    "v_mfma_f32_32x32x16_bf16 v[128:143], v[228:231], v[164:167], v[128:143]\n"
    "v_cvt_pk_bf16_f32 v173, v90, v91\n"
    "v_exp_f32 v93, v93\n"
    "s_cmp_lt_u32 s61, 128\n"
    "s_cbranch_scc0 .Lk_j_41\n"
    "s_add_u32 m0, m0, 1024\n"
    "s_add_u32 s42, s42, s43\n"
    "buffer_load_dwordx4 v19, s[28:31], s42 offen lds\n"
    "s_add_u32 m0, m0, 1024\n"
    "s_add_u32 s42, s42, s43\n"
    "buffer_load_dwordx4 v19, s[28:31], s42 offen lds\n"
    ".Lk_j_41:\n"
    "v_exp_f32 v94, v94\n"
    "v_cvt_pk_bf16_f32 v174, v92, v93\n"
    "v_exp_f32 v95, v95\n"
    "v_exp_f32 v96, v96\n"
    "v_cvt_pk_bf16_f32 v175, v94, v95\n"
    "s_waitcnt vmcnt(9)\n"
    "v_mfma_f32_32x32x4_2b_bf16 v[64:95], v[202:203], v[184:185], 0\n"
    "v_exp_f32 v97, v97\n"
    "v_exp_f32 v98, v98\n"
    "v_cvt_pk_bf16_f32 v160, v96, v97\n"
    "v_exp_f32 v99, v99\n"
    "v_exp_f32 v100, v100\n"
    "v_cvt_pk_bf16_f32 v161, v98, v99\n"
    "v_mfma_f32_32x32x16_bf16 v[144:159], v[224:227], v[168:171], v[144:159]\n"
    "v_exp_f32 v101, v101\n"
    "v_exp_f32 v102, v102\n"
    "v_cvt_pk_bf16_f32 v162, v100, v101\n"
    "v_exp_f32 v103, v103\n"
    "v_exp_f32 v104, v104\n"
    "v_cvt_pk_bf16_f32 v163, v102, v103\n"
    "v_exp_f32 v105, v105\n"
    "v_mfma_f32_32x32x16_bf16 v[144:159], v[228:231], v[172:175], v[144:159]\n"
    "v_exp_f32 v106, v106\n"
    "v_cvt_pk_bf16_f32 v164, v104, v105\n"
    "buffer_load_dwordx4 v[224:227], v3, s[20:23], s58 offen offset:1024 sc1\n"
    "buffer_load_dwordx4 v[228:231], v3, s[20:23], s58 offen offset:1664 sc1\n"
    "v_exp_f32 v107, v107\n"
    "v_exp_f32 v108, v108\n"
    "v_cvt_pk_bf16_f32 v165, v106, v107\n"
    "v_exp_f32 v109, v109\n"
    "v_exp_f32 v110, v110\n"
    "s_waitcnt vmcnt(10)\n"
    "v_mfma_f32_32x32x16_bf16 v[64:79], v[192:195], v[176:179], v[64:79]\n"
    "v_cvt_pk_bf16_f32 v166, v108, v109\n"
    "v_exp_f32 v111, v111\n"
    "s_add_u32 s58, s58, 2432\n"
    "s_cmp_eq_u32 s58, s59\n"
    "s_cselect_b32 s41, 77824, 0\n"
    "s_sub_u32 s58, s58, s41\n"
    "buffer_load_dword v202, v4, s[20:23], s58 offen offset:2304 sc1\n"
    "v_exp_f32 v112, v112\n"
    "v_cvt_pk_bf16_f32 v167, v110, v111\n"
    "v_exp_f32 v113, v113\n"
    "v_exp_f32 v114, v114\n"
    "v_cvt_pk_bf16_f32 v168, v112, v113\n"
    "v_mfma_f32_32x32x16_bf16 v[128:143], v[232:235], v[160:163], v[128:143]\n"
    "v_exp_f32 v115, v115\n"
    "v_exp_f32 v116, v116\n"
    "v_cvt_pk_bf16_f32 v169, v114, v115\n"
    "v_exp_f32 v117, v117\n"
    "v_exp_f32 v118, v118\n"
    "v_cvt_pk_bf16_f32 v170, v116, v117\n"
    "v_exp_f32 v119, v119\n"
    "v_mfma_f32_32x32x16_bf16 v[80:95], v[192:195], v[180:183], v[80:95]\n"
    "v_exp_f32 v120, v120\n"
    "v_cvt_pk_bf16_f32 v171, v118, v119\n"
    "buffer_load_dwordx4 v[192:195], v1, s[20:23], s58 offen sc1\n"
    "v_exp_f32 v121, v121\n"
    "v_exp_f32 v122, v122\n"
    "v_cvt_pk_bf16_f32 v172, v120, v121\n"
    "v_exp_f32 v123, v123\n"
    "v_exp_f32 v124, v124\n"
    "v_mfma_f32_32x32x16_bf16 v[128:143], v[236:239], v[164:167], v[128:143]\n"
    "v_cvt_pk_bf16_f32 v173, v122, v123\n"
    "v_exp_f32 v125, v125\n"
    "s_cmp_lt_u32 s61, 128\n"
    "s_cbranch_scc0 .Lk_j_42\n"
    "s_mov_b32 s60, 1\n"
    "s_mov_b32 s48, s61\n"
    "s_mul_i32 s49, s48, 7282\n"
    "s_lshr_b32 s49, s49, 16\n"
    "s_mul_i32 s47, s49, 9\n"
    "s_sub_u32 s47, s48, s47\n"
    "s_mul_i32 s48, s47, 144\n"
    "s_mov_b32 s43, 144\n"
    "s_cmp_lt_u32 s47, 8\n"
    "s_cselect_b32 s48, s48, 128\n"
    "s_cselect_b32 s43, 16, s43\n"
    "s_cmp_eq_u32 s60, 0\n"
    "s_cselect_b32 s43, 16, s43\n"
    "v_mul_u32_u24 v19, s43, v21\n"
    "s_mul_i32 s43, s37, 18\n"
    "s_add_u32 s43, s43, s49\n"
    "s_mul_i32 s43, s43, 36864\n"
    "s_add_u32 s48, s48, s43\n"
    "s_mul_i32 s42, s37, 9\n"
    "s_add_u32 s42, s42, s47\n"
    "s_lshl_b32 s42, s42, 7\n"
    "s_mul_i32 s43, s49, 294912\n"
    "s_add_u32 s42, s42, s43\n"
    "s_mov_b32 s43, 9216\n"
    "s_cmp_eq_u32 s60, 0\n"
    "s_cselect_b32 s42, s42, s48\n"
    "s_cselect_b32 s43, s43, 1152\n"
    "s_cselect_b64 s[28:29], s[10:11], s[12:13]\n"
    "v_mad_u32_u24 v19, v20, s43, v19\n"
    "s_lshl_b32 s43, s43, 3\n"
    "s_mul_i32 s47, s47, 72\n"
    "s_add_u32 s47, s47, s49\n"
    "s_mul_i32 s60, s60, 648\n"
    "s_add_u32 s47, s47, s60\n"
    "s_mul_i32 s60, s37, 1296\n"
    "s_add_u32 s47, s47, s60\n"
    "s_lshl_b32 s47, s47, 10\n"
    "s_add_u32 s47, s47, 13316096\n"
    "s_mul_i32 s48, s39, 12288\n"
    "s_add_u32 m0, s48, 96256\n"
    "s_nop 0\n"
    "buffer_load_dwordx4 v19, s[28:31], s42 offen lds\n"
    "s_add_u32 m0, m0, 1024\n"
    "s_add_u32 s42, s42, s43\n"
    "buffer_load_dwordx4 v19, s[28:31], s42 offen lds\n"
    ".Lk_j_42:\n"
    "v_exp_f32 v126, v126\n"
    "v_cvt_pk_bf16_f32 v174, v124, v125\n"
    "v_exp_f32 v127, v127\n"
    "v_exp_f32 v64, v64\n"
    "v_cvt_pk_bf16_f32 v175, v126, v127\n"
    "s_waitcnt vmcnt(9)\n"
    "v_mfma_f32_32x32x4_2b_bf16 v[96:127], v[204:205], v[184:185], 0\n"
    "v_exp_f32 v65, v65\n"
    "v_exp_f32 v66, v66\n"
    "v_cvt_pk_bf16_f32 v160, v64, v65\n"
    "v_exp_f32 v67, v67\n"
    "v_exp_f32 v68, v68\n"
    "v_cvt_pk_bf16_f32 v161, v66, v67\n"
    "v_mfma_f32_32x32x16_bf16 v[144:159], v[232:235], v[168:171], v[144:159]\n"
    "v_exp_f32 v69, v69\n"
    "v_exp_f32 v70, v70\n"
    "buffer_load_dword v18, v17, s[20:23], s51 offen sc1\n"
    "v_cvt_pk_bf16_f32 v162, v68, v69\n"
    "v_exp_f32 v71, v71\n"
    "v_exp_f32 v72, v72\n"
    "v_cvt_pk_bf16_f32 v163, v70, v71\n"
    "v_exp_f32 v73, v73\n"
    "v_mfma_f32_32x32x16_bf16 v[144:159], v[236:239], v[172:175], v[144:159]\n"
    "v_exp_f32 v74, v74\n"
    "v_cvt_pk_bf16_f32 v164, v72, v73\n"
    "buffer_load_dwordx4 v[232:235], v3, s[20:23], s58 offen offset:1024 sc1\n"
    "buffer_load_dwordx4 v[236:239], v3, s[20:23], s58 offen offset:1664 sc1\n"
    "v_exp_f32 v75, v75\n"
    "v_exp_f32 v76, v76\n"
    "v_cvt_pk_bf16_f32 v165, v74, v75\n"
    "v_exp_f32 v77, v77\n"
    "v_exp_f32 v78, v78\n"
    "s_waitcnt vmcnt(11)\n"
    "v_mfma_f32_32x32x16_bf16 v[96:111], v[196:199], v[176:179], v[96:111]\n"
    "v_cvt_pk_bf16_f32 v166, v76, v77\n"
    "v_exp_f32 v79, v79\n"
    "s_add_u32 s58, s58, 2432\n"
    "s_cmp_eq_u32 s58, s59\n"
    "s_cselect_b32 s41, 77824, 0\n"
    "s_sub_u32 s58, s58, s41\n"
    "buffer_load_dword v204, v4, s[20:23], s58 offen offset:2304 sc1\n"
    "v_exp_f32 v80, v80\n"
    "v_cvt_pk_bf16_f32 v167, v78, v79\n"
    "v_exp_f32 v81, v81\n"
    "v_exp_f32 v82, v82\n"
    "v_cvt_pk_bf16_f32 v168, v80, v81\n"
    "v_mfma_f32_32x32x16_bf16 v[128:143], v[208:211], v[160:163], v[128:143]\n"
    "v_exp_f32 v83, v83\n"
    "v_exp_f32 v84, v84\n"
    "v_cvt_pk_bf16_f32 v169, v82, v83\n"
    "v_exp_f32 v85, v85\n"
    "v_exp_f32 v86, v86\n"
    "v_cvt_pk_bf16_f32 v170, v84, v85\n"
    "v_exp_f32 v87, v87\n"
    "v_mfma_f32_32x32x16_bf16 v[112:127], v[196:199], v[180:183], v[112:127]\n"
    "v_exp_f32 v88, v88\n"
    "v_cvt_pk_bf16_f32 v171, v86, v87\n"
    "buffer_load_dwordx4 v[196:199], v1, s[20:23], s58 offen sc1\n"
    "v_exp_f32 v89, v89\n"
    "v_exp_f32 v90, v90\n"
    "v_cvt_pk_bf16_f32 v172, v88, v89\n"
    "v_exp_f32 v91, v91\n"
    "v_exp_f32 v92, v92\n"
    "v_mfma_f32_32x32x16_bf16 v[128:143], v[212:215], v[164:167], v[128:143]\n"
    "v_cvt_pk_bf16_f32 v173, v90, v91\n"
    "v_exp_f32 v93, v93\n"
    "s_cmp_lt_u32 s61, 128\n"
    "s_cbranch_scc0 .Lk_j_43\n"
    "s_add_u32 m0, m0, 1024\n"
    "s_add_u32 s42, s42, s43\n"
    "buffer_load_dwordx4 v19, s[28:31], s42 offen lds\n"
    "s_add_u32 m0, m0, 1024\n"
    "s_add_u32 s42, s42, s43\n"
    "buffer_load_dwordx4 v19, s[28:31], s42 offen lds\n"
    ".Lk_j_43:\n"
    "v_exp_f32 v94, v94\n"
    "v_cvt_pk_bf16_f32 v174, v92, v93\n"
    "v_exp_f32 v95, v95\n"
    "v_exp_f32 v96, v96\n"
    "v_cvt_pk_bf16_f32 v175, v94, v95\n"
    "s_waitcnt vmcnt(10)\n"
    "v_mfma_f32_32x32x4_2b_bf16 v[64:95], v[200:201], v[184:185], 0\n"
    "v_exp_f32 v97, v97\n"
    "v_exp_f32 v98, v98\n"
    "s_cmp_lt_u32 s61, 128\n"
    "s_cbranch_scc0 .Lk_j_44\n"
    "s_waitcnt vmcnt(9)\n"
    "ds_read_b64_tr_b16 v[28:29], v12 offset:0\n"
    "ds_read_b64_tr_b16 v[30:31], v12 offset:512\n"
    "ds_read_b64_tr_b16 v[32:33], v12 offset:32\n"
    "ds_read_b64_tr_b16 v[34:35], v12 offset:544\n"
    "ds_read_b64_tr_b16 v[36:37], v12 offset:64\n"
    "ds_read_b64_tr_b16 v[38:39], v12 offset:576\n"
    "ds_read_b64_tr_b16 v[40:41], v12 offset:96\n"
    "ds_read_b64_tr_b16 v[42:43], v12 offset:608\n"
    ".Lk_j_44:\n"
    "v_cvt_pk_bf16_f32 v160, v96, v97\n"
    "v_exp_f32 v99, v99\n"
    "v_exp_f32 v100, v100\n"
    "v_cvt_pk_bf16_f32 v161, v98, v99\n"
    "v_mfma_f32_32x32x16_bf16 v[144:159], v[208:211], v[168:171], v[144:159]\n"
    "v_exp_f32 v101, v101\n"
    "v_exp_f32 v102, v102\n"
    "v_cvt_pk_bf16_f32 v162, v100, v101\n"
    "v_exp_f32 v103, v103\n"
    "v_exp_f32 v104, v104\n"
    "v_cvt_pk_bf16_f32 v163, v102, v103\n"
    "v_exp_f32 v105, v105\n"
    "v_mfma_f32_32x32x16_bf16 v[144:159], v[212:215], v[172:175], v[144:159]\n"
    "v_exp_f32 v106, v106\n"
    "v_cvt_pk_bf16_f32 v164, v104, v105\n"
    "buffer_load_dwordx4 v[208:211], v3, s[20:23], s58 offen offset:1024 sc1\n"
    "buffer_load_dwordx4 v[212:215], v3, s[20:23], s58 offen offset:1664 sc1\n"
    "v_exp_f32 v107, v107\n"
    "v_exp_f32 v108, v108\n"
    "v_cvt_pk_bf16_f32 v165, v106, v107\n"
    "v_exp_f32 v109, v109\n"
    "v_exp_f32 v110, v110\n"
    "s_waitcnt vmcnt(11)\n"
    "v_mfma_f32_32x32x16_bf16 v[64:79], v[188:191], v[176:179], v[64:79]\n"
    "v_cvt_pk_bf16_f32 v166, v108, v109\n"
    "v_exp_f32 v111, v111\n"
    "s_add_u32 s58, s58, 2432\n"
    "s_cmp_eq_u32 s58, s59\n"
    "s_cselect_b32 s41, 77824, 0\n"
    "s_sub_u32 s58, s58, s41\n"
    "buffer_load_dword v200, v4, s[20:23], s58 offen offset:2304 sc1\n"
    "v_exp_f32 v112, v112\n"
    "v_cvt_pk_bf16_f32 v167, v110, v111\n"
    "v_exp_f32 v113, v113\n"
    "v_exp_f32 v114, v114\n"
    "v_cvt_pk_bf16_f32 v168, v112, v113\n"
    "v_mfma_f32_32x32x16_bf16 v[128:143], v[216:219], v[160:163], v[128:143]\n"
    "v_exp_f32 v115, v115\n"
    "v_exp_f32 v116, v116\n"
    "v_cvt_pk_bf16_f32 v169, v114, v115\n"
    "v_exp_f32 v117, v117\n"
    "v_exp_f32 v118, v118\n"
    "v_cvt_pk_bf16_f32 v170, v116, v117\n"
    "v_exp_f32 v119, v119\n"
    "v_mfma_f32_32x32x16_bf16 v[80:95], v[188:191], v[180:183], v[80:95]\n"
    "v_exp_f32 v120, v120\n"
    "v_cvt_pk_bf16_f32 v171, v118, v119\n"
    "buffer_load_dwordx4 v[188:191], v1, s[20:23], s58 offen sc1\n"
    "v_exp_f32 v121, v121\n"
    "v_exp_f32 v122, v122\n"
    "v_cvt_pk_bf16_f32 v172, v120, v121\n"
    "v_exp_f32 v123, v123\n"
    "v_exp_f32 v124, v124\n"
    "v_mfma_f32_32x32x16_bf16 v[128:143], v[220:223], v[164:167], v[128:143]\n"
    "v_cvt_pk_bf16_f32 v173, v122, v123\n"
    "v_exp_f32 v125, v125\n"
    "s_cmp_lt_u32 s61, 68\n"
    "s_cbranch_scc0 .Lk_j_45\n"
    "s_cmp_ge_u32 s61, 34\n"
    "s_cselect_b32 s60, 1, 0\n"
    "s_cselect_b32 s48, 0, 34\n"
    "s_add_u32 s48, s48, 94\n"
    "s_add_u32 s48, s48, s61\n"
    "s_mul_i32 s49, s48, 7282\n"
    "s_lshr_b32 s49, s49, 16\n"
    "s_mul_i32 s50, s49, 9\n"
    "s_sub_u32 s50, s48, s50\n"
    "s_mul_i32 s48, s50, 144\n"
    "s_mov_b32 s43, 144\n"
    "s_cmp_lt_u32 s50, 8\n"
    "s_cselect_b32 s48, s48, 128\n"
    "s_cselect_b32 s43, 16, s43\n"
    "s_cmp_eq_u32 s60, 0\n"
    "s_cselect_b32 s43, 16, s43\n"
    "v_mul_u32_u24 v19, s43, v21\n"
    "s_mul_i32 s43, s37, 18\n"
    "s_add_u32 s43, s43, s49\n"
    "s_mul_i32 s43, s43, 36864\n"
    "s_add_u32 s48, s48, s43\n"
    "s_mul_i32 s42, s37, 9\n"
    "s_add_u32 s42, s42, s50\n"
    "s_lshl_b32 s42, s42, 7\n"
    "s_mul_i32 s43, s49, 294912\n"
    "s_add_u32 s42, s42, s43\n"
    "s_mov_b32 s43, 9216\n"
    "s_cmp_eq_u32 s60, 0\n"
    "s_cselect_b32 s42, s42, s48\n"
    "s_cselect_b32 s43, s43, 1152\n"
    "s_cselect_b64 s[28:29], s[10:11], s[12:13]\n"
    "s_cselect_b64 vcc, -1, 0\n"
    "s_cselect_b32 s48, 512, 0\n"
    "s_cselect_b32 s60, 0, 2048\n"
    "s_or_b32 s48, s48, s60\n"
    "v_cndmask_b32 v14, v13, v12, vcc\n"
    "v_add_u32 v15, s48, v14\n"
    "s_lshr_b32 s60, s60, 11\n"
    "v_mad_u32_u24 v19, v20, s43, v19\n"
    "s_lshl_b32 s43, s43, 3\n"
    "s_mul_i32 s50, s50, 72\n"
    "s_add_u32 s50, s50, s49\n"
    "s_mul_i32 s60, s60, 648\n"
    "s_add_u32 s50, s50, s60\n"
    "s_mul_i32 s60, s37, 1296\n"
    "s_add_u32 s50, s50, s60\n"
    "s_lshl_b32 s50, s50, 10\n"
    "s_add_u32 s50, s50, 13316096\n"
    "s_mul_i32 s48, s39, 12288\n"
    "s_add_u32 m0, s48, 100352\n"
    "s_nop 0\n"
    "buffer_load_dwordx4 v19, s[28:31], s42 offen lds\n"
    "s_add_u32 m0, m0, 1024\n"
    "s_add_u32 s42, s42, s43\n"
    "buffer_load_dwordx4 v19, s[28:31], s42 offen lds\n"
    ".Lk_j_45:\n"
    "v_exp_f32 v126, v126\n"
    "v_cvt_pk_bf16_f32 v174, v124, v125\n"
    "v_exp_f32 v127, v127\n"
    "v_exp_f32 v64, v64\n"
    "v_cvt_pk_bf16_f32 v175, v126, v127\n"
    "s_waitcnt vmcnt(10)\n"
    "v_mfma_f32_32x32x4_2b_bf16 v[96:127], v[202:203], v[184:185], 0\n"
    "v_exp_f32 v65, v65\n"
    "v_exp_f32 v66, v66\n"
    "v_cvt_pk_bf16_f32 v160, v64, v65\n"
    "v_exp_f32 v67, v67\n"
    "v_exp_f32 v68, v68\n"
    "v_cvt_pk_bf16_f32 v161, v66, v67\n"
    "v_mfma_f32_32x32x16_bf16 v[144:159], v[216:219], v[168:171], v[144:159]\n"
    "v_exp_f32 v69, v69\n"
    "v_exp_f32 v70, v70\n"
    "v_cvt_pk_bf16_f32 v162, v68, v69\n"
    "v_exp_f32 v71, v71\n"
    "v_exp_f32 v72, v72\n"
    "v_cvt_pk_bf16_f32 v163, v70, v71\n"
    "v_exp_f32 v73, v73\n"
    "v_mfma_f32_32x32x16_bf16 v[144:159], v[220:223], v[172:175], v[144:159]\n"
    "v_exp_f32 v74, v74\n"
    "v_cvt_pk_bf16_f32 v164, v72, v73\n"
    "buffer_load_dwordx4 v[216:219], v3, s[20:23], s58 offen offset:1024 sc1\n"
    "buffer_load_dwordx4 v[220:223], v3, s[20:23], s58 offen offset:1664 sc1\n"
    "v_exp_f32 v75, v75\n"
    "v_exp_f32 v76, v76\n"
    "v_cvt_pk_bf16_f32 v165, v74, v75\n"
    "v_exp_f32 v77, v77\n"
    "v_exp_f32 v78, v78\n"
    "s_waitcnt vmcnt(11)\n"
    "v_mfma_f32_32x32x16_bf16 v[96:111], v[192:195], v[176:179], v[96:111]\n"
    "v_cvt_pk_bf16_f32 v166, v76, v77\n"
    "v_exp_f32 v79, v79\n"
    "s_add_u32 s58, s58, 2432\n"
    "s_cmp_eq_u32 s58, s59\n"
    "s_cselect_b32 s41, 77824, 0\n"
    "s_sub_u32 s58, s58, s41\n"
    "buffer_load_dword v202, v4, s[20:23], s58 offen offset:2304 sc1\n"
    "v_exp_f32 v80, v80\n"
    "v_cvt_pk_bf16_f32 v167, v78, v79\n"
    "v_exp_f32 v81, v81\n"
    "v_exp_f32 v82, v82\n"
    "v_cvt_pk_bf16_f32 v168, v80, v81\n"
    "v_mfma_f32_32x32x16_bf16 v[128:143], v[224:227], v[160:163], v[128:143]\n"
    "v_exp_f32 v83, v83\n"
    "v_exp_f32 v84, v84\n"
    "s_cmp_lt_u32 s61, 128\n"
    "s_cbranch_scc0 .Lk_j_46\n"
    "s_waitcnt lgkmcnt(6)\n"
    "buffer_store_dwordx4 v[28:31], v1, s[20:23], s45 offen sc1\n"
    "s_waitcnt lgkmcnt(4)\n"
    "buffer_store_dwordx4 v[32:35], v252, s[20:23], s45 offen sc1\n"
    ".Lk_j_46:\n"
    "v_cvt_pk_bf16_f32 v169, v82, v83\n"
    "v_exp_f32 v85, v85\n"
    "v_exp_f32 v86, v86\n"
    "v_cvt_pk_bf16_f32 v170, v84, v85\n"
    "v_exp_f32 v87, v87\n"
    "v_mfma_f32_32x32x16_bf16 v[112:127], v[192:195], v[180:183], v[112:127]\n"
    "v_exp_f32 v88, v88\n"
    "v_cvt_pk_bf16_f32 v171, v86, v87\n"
    "buffer_load_dwordx4 v[192:195], v1, s[20:23], s58 offen sc1\n"
    "v_exp_f32 v89, v89\n"
    "v_exp_f32 v90, v90\n"
    "v_cvt_pk_bf16_f32 v172, v88, v89\n"
    "v_exp_f32 v91, v91\n"
    "v_exp_f32 v92, v92\n"
    "v_mfma_f32_32x32x16_bf16 v[128:143], v[228:231], v[164:167], v[128:143]\n"
    "v_cvt_pk_bf16_f32 v173, v90, v91\n"
    "v_exp_f32 v93, v93\n"
    "s_cmp_lt_u32 s61, 68\n"
    "s_cbranch_scc0 .Lk_j_47\n"
    "s_add_u32 m0, m0, 1024\n"
    "s_add_u32 s42, s42, s43\n"
    "buffer_load_dwordx4 v19, s[28:31], s42 offen lds\n"
    "s_add_u32 m0, m0, 1024\n"
    "s_add_u32 s42, s42, s43\n"
    "buffer_load_dwordx4 v19, s[28:31], s42 offen lds\n"
    ".Lk_j_47:\n"
    "v_exp_f32 v94, v94\n"
    "v_cvt_pk_bf16_f32 v174, v92, v93\n"
    "v_exp_f32 v95, v95\n"
    "v_exp_f32 v96, v96\n"
    "v_cvt_pk_bf16_f32 v175, v94, v95\n"
    "s_waitcnt vmcnt(9)\n"
    "v_mfma_f32_32x32x4_2b_bf16 v[64:95], v[204:205], v[184:185], 0\n"
    "v_exp_f32 v97, v97\n"
    "v_exp_f32 v98, v98\n"
    "s_cmp_lt_u32 s61, 128\n"
    "s_cbranch_scc0 .Lk_j_49\n"
    "s_waitcnt vmcnt(8)\n"
    "ds_read_b64_tr_b16 v[44:45], v13 offset:4096\n"
    "ds_read_b64_tr_b16 v[46:47], v13 offset:6144\n"
    "ds_read_b64_tr_b16 v[48:49], v13 offset:4128\n"
    "ds_read_b64_tr_b16 v[50:51], v13 offset:6176\n"
    "ds_read_b64_tr_b16 v[52:53], v13 offset:4160\n"
    "ds_read_b64_tr_b16 v[54:55], v13 offset:6208\n"
    "ds_read_b64_tr_b16 v[56:57], v13 offset:4192\n"
    "ds_read_b64_tr_b16 v[58:59], v13 offset:6240\n"
    ".Lk_j_49:\n"
    "v_cvt_pk_bf16_f32 v160, v96, v97\n"
    "v_exp_f32 v99, v99\n"
    "v_exp_f32 v100, v100\n"
    "v_cvt_pk_bf16_f32 v161, v98, v99\n"
    "v_mfma_f32_32x32x16_bf16 v[144:159], v[224:227], v[168:171], v[144:159]\n"
    "v_exp_f32 v101, v101\n"
    "v_exp_f32 v102, v102\n"
    "v_cmp_ne_u32 vcc, s95, v18\n"
    "s_cbranch_vccz .Lk_wpd_51\n"
    "s_mov_b32 s60, 0x8000\n"
    ".Lk_wp_50:\n"
    "s_sleep 4\n"
    "buffer_load_dword v18, v17, s[20:23], s51 offen sc1\n"
    "s_waitcnt vmcnt(0)\n"
    "v_cmp_ne_u32 vcc, s95, v18\n"
    "s_cbranch_vccz .Lk_wpd_51\n"
    "s_sub_u32 s60, s60, 1\n"
    "s_cbranch_scc0 .Lk_wp_50\n"
    ".Lk_wpd_51:\n"
    "v_cvt_pk_bf16_f32 v162, v100, v101\n"
    "v_exp_f32 v103, v103\n"
    "v_exp_f32 v104, v104\n"
    "v_cvt_pk_bf16_f32 v163, v102, v103\n"
    "v_exp_f32 v105, v105\n"
    "v_mfma_f32_32x32x16_bf16 v[144:159], v[228:231], v[172:175], v[144:159]\n"
    "v_exp_f32 v106, v106\n"
    "v_cvt_pk_bf16_f32 v164, v104, v105\n"
    "buffer_load_dwordx4 v[224:227], v3, s[20:23], s58 offen offset:1024 sc1\n"
    "buffer_load_dwordx4 v[228:231], v3, s[20:23], s58 offen offset:1664 sc1\n"
    "v_exp_f32 v107, v107\n"
    "v_exp_f32 v108, v108\n"
    "v_cvt_pk_bf16_f32 v165, v106, v107\n"
    "v_exp_f32 v109, v109\n"
    "v_exp_f32 v110, v110\n"
    "s_waitcnt vmcnt(10)\n"
    "v_mfma_f32_32x32x16_bf16 v[64:79], v[196:199], v[176:179], v[64:79]\n"
    "v_cvt_pk_bf16_f32 v166, v108, v109\n"
    "v_exp_f32 v111, v111\n"
    "s_add_u32 s58, s58, 2432\n"
    "s_cmp_eq_u32 s58, s59\n"
    "s_cselect_b32 s41, 77824, 0\n"
    "s_sub_u32 s58, s58, s41\n"
    "buffer_load_dword v204, v4, s[20:23], s58 offen offset:2304 sc1\n"
    "v_exp_f32 v112, v112\n"
    "v_cvt_pk_bf16_f32 v167, v110, v111\n"
    "v_exp_f32 v113, v113\n"
    "v_exp_f32 v114, v114\n"
    "v_cvt_pk_bf16_f32 v168, v112, v113\n"
    "v_mfma_f32_32x32x16_bf16 v[128:143], v[232:235], v[160:163], v[128:143]\n"
    "v_exp_f32 v115, v115\n"
    "v_exp_f32 v116, v116\n"
    "s_cmp_lt_u32 s61, 128\n"
    "s_cbranch_scc0 .Lk_j_48\n"
    "s_waitcnt lgkmcnt(2)\n"
    "buffer_store_dwordx4 v[36:39], v253, s[20:23], s45 offen sc1\n"
    "s_waitcnt lgkmcnt(0)\n"
    "buffer_store_dwordx4 v[40:43], v254, s[20:23], s45 offen sc1\n"
    ".Lk_j_48:\n"
    "v_cvt_pk_bf16_f32 v169, v114, v115\n"
    "v_exp_f32 v117, v117\n"
    "v_exp_f32 v118, v118\n"
    "v_cvt_pk_bf16_f32 v170, v116, v117\n"
    "v_exp_f32 v119, v119\n"
    "v_mfma_f32_32x32x16_bf16 v[80:95], v[196:199], v[180:183], v[80:95]\n"
    "v_exp_f32 v120, v120\n"
    "v_cvt_pk_bf16_f32 v171, v118, v119\n"
    "buffer_load_dwordx4 v[196:199], v1, s[20:23], s58 offen sc1\n"
    "v_exp_f32 v121, v121\n"
    "v_exp_f32 v122, v122\n"
    "v_cvt_pk_bf16_f32 v172, v120, v121\n"
    "v_exp_f32 v123, v123\n"
    "v_exp_f32 v124, v124\n"
    "v_mfma_f32_32x32x16_bf16 v[128:143], v[236:239], v[164:167], v[128:143]\n"
    "v_cvt_pk_bf16_f32 v173, v122, v123\n"
    "v_exp_f32 v125, v125\n"
    "s_mul_i32 s48, s36, 11520\n"
    "s_min_u32 m0, s48, 79872\n"
    "s_nop 0\n"
    "buffer_load_dwordx4 v1, s[20:23], s97 offen sc1 lds\n"
    "v_exp_f32 v126, v126\n"
    "v_cvt_pk_bf16_f32 v174, v124, v125\n"
    "v_exp_f32 v127, v127\n"
    "v_exp_f32 v64, v64\n"
    "v_cvt_pk_bf16_f32 v175, v126, v127\n"
    "s_waitcnt vmcnt(10)\n"
    "v_mfma_f32_32x32x4_2b_bf16 v[96:127], v[200:201], v[184:185], 0\n"
    "v_exp_f32 v65, v65\n"
    "v_exp_f32 v66, v66\n"
    "v_cvt_pk_bf16_f32 v160, v64, v65\n"
    "v_exp_f32 v67, v67\n"
    "v_exp_f32 v68, v68\n"
    "v_cvt_pk_bf16_f32 v161, v66, v67\n"
    "v_mfma_f32_32x32x16_bf16 v[144:159], v[232:235], v[168:171], v[144:159]\n"
    "v_exp_f32 v69, v69\n"
    "v_exp_f32 v70, v70\n"
    "v_cvt_pk_bf16_f32 v162, v68, v69\n"
    "v_exp_f32 v71, v71\n"
    "v_exp_f32 v72, v72\n"
    "v_cvt_pk_bf16_f32 v163, v70, v71\n"
    "v_exp_f32 v73, v73\n"
    "v_mfma_f32_32x32x16_bf16 v[144:159], v[236:239], v[172:175], v[144:159]\n"
    "v_exp_f32 v74, v74\n"
    "v_cvt_pk_bf16_f32 v164, v72, v73\n"
    "buffer_load_dwordx4 v[232:235], v3, s[20:23], s58 offen offset:1024 sc1\n"
    "buffer_load_dwordx4 v[236:239], v3, s[20:23], s58 offen offset:1664 sc1\n"
    "v_exp_f32 v75, v75\n"
    "v_exp_f32 v76, v76\n"
    "v_cvt_pk_bf16_f32 v165, v74, v75\n"
    "v_exp_f32 v77, v77\n"
    "v_exp_f32 v78, v78\n"
    "s_waitcnt vmcnt(11)\n"
    "v_mfma_f32_32x32x16_bf16 v[96:111], v[188:191], v[176:179], v[96:111]\n"
    "v_cvt_pk_bf16_f32 v166, v76, v77\n"
    "v_exp_f32 v79, v79\n"
    "s_add_u32 s58, s58, 2432\n"
    "s_cmp_eq_u32 s58, s59\n"
    "s_cselect_b32 s41, 77824, 0\n"
    "s_sub_u32 s58, s58, s41\n"
    "buffer_load_dword v200, v4, s[20:23], s58 offen offset:2304 sc1\n"
    "v_exp_f32 v80, v80\n"
    "v_cvt_pk_bf16_f32 v167, v78, v79\n"
    "v_exp_f32 v81, v81\n"
    "v_exp_f32 v82, v82\n"
    "v_cvt_pk_bf16_f32 v168, v80, v81\n"
    "v_mfma_f32_32x32x16_bf16 v[128:143], v[208:211], v[160:163], v[128:143]\n"
    "v_exp_f32 v83, v83\n"
    "v_exp_f32 v84, v84\n"
    "s_cmp_lt_u32 s61, 128\n"
    "s_cbranch_scc0 .Lk_j_52\n"
    "s_waitcnt lgkmcnt(6)\n"
    "buffer_store_dwordx4 v[44:47], v1, s[20:23], s47 offen sc1\n"
    "s_waitcnt lgkmcnt(4)\n"
    "buffer_store_dwordx4 v[48:51], v252, s[20:23], s47 offen sc1\n"
    ".Lk_j_52:\n"
    "v_cvt_pk_bf16_f32 v169, v82, v83\n"
    "v_exp_f32 v85, v85\n"
    "v_exp_f32 v86, v86\n"
    "v_cvt_pk_bf16_f32 v170, v84, v85\n"
    "v_exp_f32 v87, v87\n"
    "v_mfma_f32_32x32x16_bf16 v[112:127], v[188:191], v[180:183], v[112:127]\n"
    "v_exp_f32 v88, v88\n"
    "v_cvt_pk_bf16_f32 v171, v86, v87\n"
    "buffer_load_dwordx4 v[188:191], v1, s[20:23], s58 offen sc1\n"
    "v_exp_f32 v89, v89\n"
    "v_exp_f32 v90, v90\n"
    "v_cvt_pk_bf16_f32 v172, v88, v89\n"
    "v_exp_f32 v91, v91\n"
    "v_exp_f32 v92, v92\n"
    "v_mfma_f32_32x32x16_bf16 v[128:143], v[212:215], v[164:167], v[128:143]\n"
    "v_cvt_pk_bf16_f32 v173, v90, v91\n"
    "v_exp_f32 v93, v93\n"
    "buffer_load_dwordx4 v1, s[20:23], s97 offen offset:1024 sc1 lds\n"
    "v_exp_f32 v94, v94\n"
    "v_cvt_pk_bf16_f32 v174, v92, v93\n"
    "v_exp_f32 v95, v95\n"
    "v_exp_f32 v96, v96\n"
    "v_cvt_pk_bf16_f32 v175, v94, v95\n"
    "s_waitcnt vmcnt(11)\n"
    "v_mfma_f32_32x32x4_2b_bf16 v[64:95], v[202:203], v[184:185], 0\n"
    "v_exp_f32 v97, v97\n"
    "v_exp_f32 v98, v98\n"
    "s_cmp_lt_u32 s61, 68\n"
    "s_cbranch_scc0 .Lk_j_54\n"
    "s_waitcnt vmcnt(10)\n"
    "ds_read_b64_tr_b16 v[28:29], v14 offset:8192\n"
    "ds_read_b64_tr_b16 v[30:31], v15 offset:8192\n"
    "ds_read_b64_tr_b16 v[32:33], v14 offset:8224\n"
    "ds_read_b64_tr_b16 v[34:35], v15 offset:8224\n"
    "ds_read_b64_tr_b16 v[36:37], v14 offset:8256\n"
    "ds_read_b64_tr_b16 v[38:39], v15 offset:8256\n"
    "ds_read_b64_tr_b16 v[40:41], v14 offset:8288\n"
    "ds_read_b64_tr_b16 v[42:43], v15 offset:8288\n"
    ".Lk_j_54:\n"
    "v_cvt_pk_bf16_f32 v160, v96, v97\n"
    "v_exp_f32 v99, v99\n"
    "v_exp_f32 v100, v100\n"
    "v_cvt_pk_bf16_f32 v161, v98, v99\n"
    "v_mfma_f32_32x32x16_bf16 v[144:159], v[208:211], v[168:171], v[144:159]\n"
    "v_exp_f32 v101, v101\n"
    "v_exp_f32 v102, v102\n"
    "v_cvt_pk_bf16_f32 v162, v100, v101\n"
    "v_exp_f32 v103, v103\n"
    "v_exp_f32 v104, v104\n"
    "v_cvt_pk_bf16_f32 v163, v102, v103\n"
    "v_exp_f32 v105, v105\n"
    "v_mfma_f32_32x32x16_bf16 v[144:159], v[212:215], v[172:175], v[144:159]\n"
    "v_exp_f32 v106, v106\n"
    "v_cvt_pk_bf16_f32 v164, v104, v105\n"
    "buffer_load_dwordx4 v[208:211], v3, s[20:23], s58 offen offset:1024 sc1\n"
    "buffer_load_dwordx4 v[212:215], v3, s[20:23], s58 offen offset:1664 sc1\n"
    "v_exp_f32 v107, v107\n"
    "v_exp_f32 v108, v108\n"
    "v_cvt_pk_bf16_f32 v165, v106, v107\n"
    "v_exp_f32 v109, v109\n"
    "v_exp_f32 v110, v110\n"
    "s_waitcnt vmcnt(12)\n"
    "v_mfma_f32_32x32x16_bf16 v[64:79], v[192:195], v[176:179], v[64:79]\n"
    "v_cvt_pk_bf16_f32 v166, v108, v109\n"
    "v_exp_f32 v111, v111\n"
    "s_add_u32 s58, s58, 2432\n"
    "s_cmp_eq_u32 s58, s59\n"
    "s_cselect_b32 s41, 77824, 0\n"
    "s_sub_u32 s58, s58, s41\n"
    "buffer_load_dword v202, v4, s[20:23], s58 offen offset:2304 sc1\n"
    "v_exp_f32 v112, v112\n"
    "v_cvt_pk_bf16_f32 v167, v110, v111\n"
    "v_exp_f32 v113, v113\n"
    "v_exp_f32 v114, v114\n"
    "v_cvt_pk_bf16_f32 v168, v112, v113\n"
    "v_mfma_f32_32x32x16_bf16 v[128:143], v[216:219], v[160:163], v[128:143]\n"
    "v_exp_f32 v115, v115\n"
    "v_exp_f32 v116, v116\n"
    "s_cmp_lt_u32 s61, 128\n"
    "s_cbranch_scc0 .Lk_j_53\n"
    "s_waitcnt lgkmcnt(2)\n"
    "buffer_store_dwordx4 v[52:55], v253, s[20:23], s47 offen sc1\n"
    "s_waitcnt lgkmcnt(0)\n"
    "buffer_store_dwordx4 v[56:59], v254, s[20:23], s47 offen sc1\n"
    ".Lk_j_53:\n"
    "v_cvt_pk_bf16_f32 v169, v114, v115\n"
    "v_exp_f32 v117, v117\n"
    "v_exp_f32 v118, v118\n"
    "v_cvt_pk_bf16_f32 v170, v116, v117\n"
    "v_exp_f32 v119, v119\n"
    "v_mfma_f32_32x32x16_bf16 v[80:95], v[192:195], v[180:183], v[80:95]\n"
    "v_exp_f32 v120, v120\n"
    "v_cvt_pk_bf16_f32 v171, v118, v119\n"
    "buffer_load_dwordx4 v[192:195], v1, s[20:23], s58 offen sc1\n"
    "v_exp_f32 v121, v121\n"
    "v_exp_f32 v122, v122\n"
    "v_cvt_pk_bf16_f32 v172, v120, v121\n"
    "v_exp_f32 v123, v123\n"
    "v_exp_f32 v124, v124\n"
    "v_mfma_f32_32x32x16_bf16 v[128:143], v[220:223], v[164:167], v[128:143]\n"
    "v_cvt_pk_bf16_f32 v173, v122, v123\n"
    "v_exp_f32 v125, v125\n"
    "buffer_load_dwordx4 v1, s[20:23], s97 offen offset:2048 sc1 lds\n"
    "v_exp_f32 v126, v126\n"
    "v_cvt_pk_bf16_f32 v174, v124, v125\n"
    "v_exp_f32 v127, v127\n"
    "v_exp_f32 v64, v64\n"
    "v_cvt_pk_bf16_f32 v175, v126, v127\n"
    "s_waitcnt vmcnt(12)\n"
    "v_mfma_f32_32x32x4_2b_bf16 v[96:127], v[204:205], v[184:185], 0\n"
    "v_exp_f32 v65, v65\n"
    "v_exp_f32 v66, v66\n"
    "v_cvt_pk_bf16_f32 v160, v64, v65\n"
    "v_exp_f32 v67, v67\n"
    "v_exp_f32 v68, v68\n"
    "v_cvt_pk_bf16_f32 v161, v66, v67\n"
    "v_mfma_f32_32x32x16_bf16 v[144:159], v[216:219], v[168:171], v[144:159]\n"
    "v_exp_f32 v69, v69\n"
    "v_exp_f32 v70, v70\n"
    "v_cvt_pk_bf16_f32 v162, v68, v69\n"
    "v_exp_f32 v71, v71\n"
    "v_exp_f32 v72, v72\n"
    "v_cvt_pk_bf16_f32 v163, v70, v71\n"
    "v_exp_f32 v73, v73\n"
    "v_mfma_f32_32x32x16_bf16 v[144:159], v[220:223], v[172:175], v[144:159]\n"
    "v_exp_f32 v74, v74\n"
    "v_cvt_pk_bf16_f32 v164, v72, v73\n"
    "buffer_load_dwordx4 v[216:219], v3, s[20:23], s58 offen offset:1024 sc1\n"
    "buffer_load_dwordx4 v[220:223], v3, s[20:23], s58 offen offset:1664 sc1\n"
    "v_exp_f32 v75, v75\n"
    "v_exp_f32 v76, v76\n"
    "v_cvt_pk_bf16_f32 v165, v74, v75\n"
    "v_exp_f32 v77, v77\n"
    "v_exp_f32 v78, v78\n"
    "s_waitcnt vmcnt(13)\n"
    "v_mfma_f32_32x32x16_bf16 v[96:111], v[196:199], v[176:179], v[96:111]\n"
    "v_cvt_pk_bf16_f32 v166, v76, v77\n"
    "v_exp_f32 v79, v79\n"
    "s_add_u32 s58, s58, 2432\n"
    "s_cmp_eq_u32 s58, s59\n"
    "s_cselect_b32 s41, 77824, 0\n"
    "s_sub_u32 s58, s58, s41\n"
    "buffer_load_dword v204, v4, s[20:23], s58 offen offset:2304 sc1\n"
    "v_exp_f32 v80, v80\n"
    "v_cvt_pk_bf16_f32 v167, v78, v79\n"
    "v_exp_f32 v81, v81\n"
    "v_exp_f32 v82, v82\n"
    "v_cvt_pk_bf16_f32 v168, v80, v81\n"
    "v_mfma_f32_32x32x16_bf16 v[128:143], v[224:227], v[160:163], v[128:143]\n"
    "v_exp_f32 v83, v83\n"
    "v_exp_f32 v84, v84\n"
    "s_cmp_lt_u32 s61, 68\n"
    "s_cbranch_scc0 .Lk_j_55\n"
    "s_waitcnt lgkmcnt(6)\n"
    "buffer_store_dwordx4 v[28:31], v1, s[20:23], s50 offen sc1\n"
    "s_waitcnt lgkmcnt(4)\n"
    "buffer_store_dwordx4 v[32:35], v252, s[20:23], s50 offen sc1\n"
    ".Lk_j_55:\n"
    "v_cvt_pk_bf16_f32 v169, v82, v83\n"
    "v_exp_f32 v85, v85\n"
    "v_exp_f32 v86, v86\n"
    "v_cvt_pk_bf16_f32 v170, v84, v85\n"
    "v_exp_f32 v87, v87\n"
    "v_mfma_f32_32x32x16_bf16 v[112:127], v[196:199], v[180:183], v[112:127]\n"
    "v_exp_f32 v88, v88\n"
    "v_cvt_pk_bf16_f32 v171, v86, v87\n"
    "buffer_load_dwordx4 v[196:199], v1, s[20:23], s58 offen sc1\n"
    "v_exp_f32 v89, v89\n"
    "v_exp_f32 v90, v90\n"
    "v_cvt_pk_bf16_f32 v172, v88, v89\n"
    "v_exp_f32 v91, v91\n"
    "v_exp_f32 v92, v92\n"
    "v_mfma_f32_32x32x16_bf16 v[128:143], v[228:231], v[164:167], v[128:143]\n"
    "v_cvt_pk_bf16_f32 v173, v90, v91\n"
    "v_exp_f32 v93, v93\n"
    "buffer_load_dwordx4 v1, s[20:23], s97 offen offset:3072 sc1 lds\n"
    "v_exp_f32 v94, v94\n"
    "v_cvt_pk_bf16_f32 v174, v92, v93\n"
    "v_exp_f32 v95, v95\n"
    "v_exp_f32 v96, v96\n"
    "v_cvt_pk_bf16_f32 v175, v94, v95\n"
    "s_waitcnt vmcnt(12)\n"
    "v_mfma_f32_32x32x4_2b_bf16 v[64:95], v[200:201], v[184:185], 0\n"
    "v_exp_f32 v97, v97\n"
    "v_exp_f32 v98, v98\n"
    "v_cvt_pk_bf16_f32 v160, v96, v97\n"
    "v_exp_f32 v99, v99\n"
    "v_exp_f32 v100, v100\n"
    "v_cvt_pk_bf16_f32 v161, v98, v99\n"
    "v_mfma_f32_32x32x16_bf16 v[144:159], v[224:227], v[168:171], v[144:159]\n"
    "v_exp_f32 v101, v101\n"
    "v_exp_f32 v102, v102\n"
    "v_cvt_pk_bf16_f32 v162, v100, v101\n"
    "v_exp_f32 v103, v103\n"
    "v_exp_f32 v104, v104\n"
    "v_cvt_pk_bf16_f32 v163, v102, v103\n"
    "v_exp_f32 v105, v105\n"
    "v_mfma_f32_32x32x16_bf16 v[144:159], v[228:231], v[172:175], v[144:159]\n"
    "v_exp_f32 v106, v106\n"
    "v_cvt_pk_bf16_f32 v164, v104, v105\n"
    "buffer_load_dwordx4 v[224:227], v3, s[20:23], s58 offen offset:1024 sc1\n"
    "buffer_load_dwordx4 v[228:231], v3, s[20:23], s58 offen offset:1664 sc1\n"
    "v_exp_f32 v107, v107\n"
    "v_exp_f32 v108, v108\n"
    "v_cvt_pk_bf16_f32 v165, v106, v107\n"
    "v_exp_f32 v109, v109\n"
    "v_exp_f32 v110, v110\n"
    "s_waitcnt vmcnt(13)\n"
    "v_mfma_f32_32x32x16_bf16 v[64:79], v[188:191], v[176:179], v[64:79]\n"
    "v_cvt_pk_bf16_f32 v166, v108, v109\n"
    "v_exp_f32 v111, v111\n"
    "s_add_u32 s58, s58, 2432\n"
    "s_cmp_eq_u32 s58, s59\n"
    "s_cselect_b32 s41, 77824, 0\n"
    "s_sub_u32 s58, s58, s41\n"
    "buffer_load_dword v200, v4, s[20:23], s58 offen offset:2304 sc1\n"
    "v_exp_f32 v112, v112\n"
    "v_cvt_pk_bf16_f32 v167, v110, v111\n"
    "v_exp_f32 v113, v113\n"
    "v_exp_f32 v114, v114\n"
    "v_cvt_pk_bf16_f32 v168, v112, v113\n"
    "v_mfma_f32_32x32x16_bf16 v[128:143], v[232:235], v[160:163], v[128:143]\n"
    "v_exp_f32 v115, v115\n"
    "v_exp_f32 v116, v116\n"
    "s_cmp_lt_u32 s61, 68\n"
    "s_cbranch_scc0 .Lk_j_56\n"
    "s_waitcnt lgkmcnt(2)\n"
    "buffer_store_dwordx4 v[36:39], v253, s[20:23], s50 offen sc1\n"
    "s_waitcnt lgkmcnt(0)\n"
    "buffer_store_dwordx4 v[40:43], v254, s[20:23], s50 offen sc1\n"
    ".Lk_j_56:\n"
    "v_cvt_pk_bf16_f32 v169, v114, v115\n"
    "v_exp_f32 v117, v117\n"
    "v_exp_f32 v118, v118\n"
    "v_cvt_pk_bf16_f32 v170, v116, v117\n"
    "v_exp_f32 v119, v119\n"
    "v_mfma_f32_32x32x16_bf16 v[80:95], v[188:191], v[180:183], v[80:95]\n"
    "v_exp_f32 v120, v120\n"
    "v_cvt_pk_bf16_f32 v171, v118, v119\n"
    "buffer_load_dwordx4 v[188:191], v1, s[20:23], s58 offen sc1\n"
    "v_exp_f32 v121, v121\n"
    "v_exp_f32 v122, v122\n"
    "v_cvt_pk_bf16_f32 v172, v120, v121\n"
    "v_exp_f32 v123, v123\n"
    "v_exp_f32 v124, v124\n"
    "v_mfma_f32_32x32x16_bf16 v[128:143], v[236:239], v[164:167], v[128:143]\n"
    "v_cvt_pk_bf16_f32 v173, v122, v123\n"
    "v_exp_f32 v125, v125\n"
    "s_add_u32 m0, m0, 4096\n"
    "s_add_u32 s97, s97, 4096\n"
    "buffer_load_dwordx4 v1, s[20:23], s97 offen sc1 lds\n"
    "v_exp_f32 v126, v126\n"
    "v_cvt_pk_bf16_f32 v174, v124, v125\n"
    "v_exp_f32 v127, v127\n"
    "v_exp_f32 v64, v64\n"
    "v_cvt_pk_bf16_f32 v175, v126, v127\n"
    "s_waitcnt vmcnt(12)\n"
    "v_mfma_f32_32x32x4_2b_bf16 v[96:127], v[202:203], v[184:185], 0\n"
    "v_exp_f32 v65, v65\n"
    "v_exp_f32 v66, v66\n"
    "v_cvt_pk_bf16_f32 v160, v64, v65\n"
    "v_exp_f32 v67, v67\n"
    "v_exp_f32 v68, v68\n"
    "v_cvt_pk_bf16_f32 v161, v66, v67\n"
    "v_mfma_f32_32x32x16_bf16 v[144:159], v[232:235], v[168:171], v[144:159]\n"
    "v_exp_f32 v69, v69\n"
    "v_exp_f32 v70, v70\n"
    "v_cvt_pk_bf16_f32 v162, v68, v69\n"
    "v_exp_f32 v71, v71\n"
    "v_exp_f32 v72, v72\n"
    "v_cvt_pk_bf16_f32 v163, v70, v71\n"
    "v_exp_f32 v73, v73\n"
    "v_mfma_f32_32x32x16_bf16 v[144:159], v[236:239], v[172:175], v[144:159]\n"
    "v_exp_f32 v74, v74\n"
    "v_cvt_pk_bf16_f32 v164, v72, v73\n"
    "buffer_load_dwordx4 v[232:235], v3, s[20:23], s58 offen offset:1024 sc1\n"
    "buffer_load_dwordx4 v[236:239], v3, s[20:23], s58 offen offset:1664 sc1\n"
    "v_exp_f32 v75, v75\n"
    "v_exp_f32 v76, v76\n"
    "v_cvt_pk_bf16_f32 v165, v74, v75\n"
    "v_exp_f32 v77, v77\n"
    "v_exp_f32 v78, v78\n"
    "s_waitcnt vmcnt(13)\n"
    "v_mfma_f32_32x32x16_bf16 v[96:111], v[192:195], v[176:179], v[96:111]\n"
    "v_cvt_pk_bf16_f32 v166, v76, v77\n"
    "v_exp_f32 v79, v79\n"
    "s_add_u32 s58, s58, 2432\n"
    "s_cmp_eq_u32 s58, s59\n"
    "s_cselect_b32 s41, 77824, 0\n"
    "s_sub_u32 s58, s58, s41\n"
    "buffer_load_dword v202, v4, s[20:23], s58 offen offset:2304 sc1\n"
    "v_exp_f32 v80, v80\n"
    "v_cvt_pk_bf16_f32 v167, v78, v79\n"
    "v_exp_f32 v81, v81\n"
    "v_exp_f32 v82, v82\n"
    "v_cvt_pk_bf16_f32 v168, v80, v81\n"
    "v_mfma_f32_32x32x16_bf16 v[128:143], v[208:211], v[160:163], v[128:143]\n"
    "v_exp_f32 v83, v83\n"
    "v_exp_f32 v84, v84\n"
    "v_cvt_pk_bf16_f32 v169, v82, v83\n"
    "v_exp_f32 v85, v85\n"
    "v_exp_f32 v86, v86\n"
    "v_cvt_pk_bf16_f32 v170, v84, v85\n"
    "v_exp_f32 v87, v87\n"
    "v_mfma_f32_32x32x16_bf16 v[112:127], v[192:195], v[180:183], v[112:127]\n"
    "v_exp_f32 v88, v88\n"
    "v_cvt_pk_bf16_f32 v171, v86, v87\n"
    "buffer_load_dwordx4 v[192:195], v1, s[20:23], s58 offen sc1\n"
    "v_exp_f32 v89, v89\n"
    "v_exp_f32 v90, v90\n"
    "v_cvt_pk_bf16_f32 v172, v88, v89\n"
    "v_exp_f32 v91, v91\n"
    "v_exp_f32 v92, v92\n"
    "v_mfma_f32_32x32x16_bf16 v[128:143], v[212:215], v[164:167], v[128:143]\n"
    "v_cvt_pk_bf16_f32 v173, v90, v91\n"
    "v_exp_f32 v93, v93\n"
    "buffer_load_dwordx4 v1, s[20:23], s97 offen offset:1024 sc1 lds\n"
    "v_exp_f32 v94, v94\n"
    "v_cvt_pk_bf16_f32 v174, v92, v93\n"
    "v_exp_f32 v95, v95\n"
    "v_exp_f32 v96, v96\n"
    "v_cvt_pk_bf16_f32 v175, v94, v95\n"
    "s_waitcnt vmcnt(12)\n"
    "v_mfma_f32_32x32x4_2b_bf16 v[64:95], v[204:205], v[184:185], 0\n"
    "v_exp_f32 v97, v97\n"
    "v_exp_f32 v98, v98\n"
    "v_cvt_pk_bf16_f32 v160, v96, v97\n"
    "v_exp_f32 v99, v99\n"
    "v_exp_f32 v100, v100\n"
    "v_cvt_pk_bf16_f32 v161, v98, v99\n"
    "v_mfma_f32_32x32x16_bf16 v[144:159], v[208:211], v[168:171], v[144:159]\n"
    "v_exp_f32 v101, v101\n"
    "v_exp_f32 v102, v102\n"
    "s_cmp_lt_u32 s61, 128\n"
    "s_cbranch_scc0 .Lk_j_58\n"
    "s_waitcnt vmcnt(0)\n"
    "v_mov_b32 v18, 1\n"
    "s_mov_b64 exec, 1\n"
    "ds_add_rtn_u32 v18, v24, v18\n"
    "s_mov_b64 exec, -1\n"
    "s_waitcnt lgkmcnt(0)\n"
    "v_readfirstlane_b32 s48, v18\n"
    "s_cmp_eq_u32 s48, 3\n"
    "s_cbranch_scc0 .Lk_jd_57\n"
    "buffer_store_dword v9, v16, s[20:23], s46 offen sc1\n"
    ".Lk_jd_57:\n"
    ".Lk_j_58:\n"
    "v_cvt_pk_bf16_f32 v162, v100, v101\n"
    "v_exp_f32 v103, v103\n"
    "v_exp_f32 v104, v104\n"
    "v_cvt_pk_bf16_f32 v163, v102, v103\n"
    "v_exp_f32 v105, v105\n"
    "v_mfma_f32_32x32x16_bf16 v[144:159], v[212:215], v[172:175], v[144:159]\n"
    "v_exp_f32 v106, v106\n"
    "v_cvt_pk_bf16_f32 v164, v104, v105\n"
    "buffer_load_dwordx4 v[208:211], v3, s[20:23], s58 offen offset:1024 sc1\n"
    "buffer_load_dwordx4 v[212:215], v3, s[20:23], s58 offen offset:1664 sc1\n"
    "v_exp_f32 v107, v107\n"
    "v_exp_f32 v108, v108\n"
    "v_cvt_pk_bf16_f32 v165, v106, v107\n"
    "v_exp_f32 v109, v109\n"
    "v_exp_f32 v110, v110\n"
    "s_waitcnt vmcnt(13)\n"
    "v_mfma_f32_32x32x16_bf16 v[64:79], v[196:199], v[176:179], v[64:79]\n"
    "v_cvt_pk_bf16_f32 v166, v108, v109\n"
    "v_exp_f32 v111, v111\n"
    "s_add_u32 s58, s58, 2432\n"
    "s_cmp_eq_u32 s58, s59\n"
    "s_cselect_b32 s41, 77824, 0\n"
    "s_sub_u32 s58, s58, s41\n"
    "buffer_load_dword v204, v4, s[20:23], s58 offen offset:2304 sc1\n"
    "v_exp_f32 v112, v112\n"
    "v_cvt_pk_bf16_f32 v167, v110, v111\n"
    "v_exp_f32 v113, v113\n"
    "v_exp_f32 v114, v114\n"
    "v_cvt_pk_bf16_f32 v168, v112, v113\n"
    "v_mfma_f32_32x32x16_bf16 v[128:143], v[216:219], v[160:163], v[128:143]\n"
    "v_exp_f32 v115, v115\n"
    "v_exp_f32 v116, v116\n"
    "v_cvt_pk_bf16_f32 v169, v114, v115\n"
    "v_exp_f32 v117, v117\n"
    "v_exp_f32 v118, v118\n"
    "v_cvt_pk_bf16_f32 v170, v116, v117\n"
    "v_exp_f32 v119, v119\n"
    "v_mfma_f32_32x32x16_bf16 v[80:95], v[196:199], v[180:183], v[80:95]\n"
    "v_exp_f32 v120, v120\n"
    "v_cvt_pk_bf16_f32 v171, v118, v119\n"
    "buffer_load_dwordx4 v[196:199], v1, s[20:23], s58 offen sc1\n"
    "v_exp_f32 v121, v121\n"
    "v_exp_f32 v122, v122\n"
    "v_cvt_pk_bf16_f32 v172, v120, v121\n"
    "v_exp_f32 v123, v123\n"
    "v_exp_f32 v124, v124\n"
    "v_mfma_f32_32x32x16_bf16 v[128:143], v[220:223], v[164:167], v[128:143]\n"
    "v_cvt_pk_bf16_f32 v173, v122, v123\n"
    "v_exp_f32 v125, v125\n"
    "buffer_load_dwordx4 v1, s[20:23], s97 offen offset:2048 sc1 lds\n"
    "v_exp_f32 v126, v126\n"
    "v_cvt_pk_bf16_f32 v174, v124, v125\n"
    "v_exp_f32 v127, v127\n"
    "v_exp_f32 v64, v64\n"
    "v_cvt_pk_bf16_f32 v175, v126, v127\n"
    "s_waitcnt vmcnt(12)\n"
    "v_mfma_f32_32x32x4_2b_bf16 v[96:127], v[200:201], v[184:185], 0\n"
    "v_exp_f32 v65, v65\n"
    "v_exp_f32 v66, v66\n"
    "v_cvt_pk_bf16_f32 v160, v64, v65\n"
    "v_exp_f32 v67, v67\n"
    "v_exp_f32 v68, v68\n"
    "v_cvt_pk_bf16_f32 v161, v66, v67\n"
    "v_mfma_f32_32x32x16_bf16 v[144:159], v[216:219], v[168:171], v[144:159]\n"
    "v_exp_f32 v69, v69\n"
    "v_exp_f32 v70, v70\n"
    "v_cvt_pk_bf16_f32 v162, v68, v69\n"
    "v_exp_f32 v71, v71\n"
    "v_exp_f32 v72, v72\n"
    "v_cvt_pk_bf16_f32 v163, v70, v71\n"
    "v_exp_f32 v73, v73\n"
    "v_mfma_f32_32x32x16_bf16 v[144:159], v[220:223], v[172:175], v[144:159]\n"
    "v_exp_f32 v74, v74\n"
    "v_cvt_pk_bf16_f32 v164, v72, v73\n"
    "buffer_load_dwordx4 v[216:219], v3, s[20:23], s58 offen offset:1024 sc1\n"
    "buffer_load_dwordx4 v[220:223], v3, s[20:23], s58 offen offset:1664 sc1\n"
    "v_exp_f32 v75, v75\n"
    "v_exp_f32 v76, v76\n"
    "v_cvt_pk_bf16_f32 v165, v74, v75\n"
    "v_exp_f32 v77, v77\n"
    "v_exp_f32 v78, v78\n"
    "s_waitcnt vmcnt(13)\n"
    "v_mfma_f32_32x32x16_bf16 v[96:111], v[188:191], v[176:179], v[96:111]\n"
    "v_cvt_pk_bf16_f32 v166, v76, v77\n"
    "v_exp_f32 v79, v79\n"
    "s_add_u32 s58, s58, 2432\n"
    "s_cmp_eq_u32 s58, s59\n"
    "s_cselect_b32 s41, 77824, 0\n"
    "s_sub_u32 s58, s58, s41\n"
    "buffer_load_dword v200, v4, s[20:23], s58 offen offset:2304 sc1\n"
    "v_exp_f32 v80, v80\n"
    "v_cvt_pk_bf16_f32 v167, v78, v79\n"
    "v_exp_f32 v81, v81\n"
    "v_exp_f32 v82, v82\n"
    "v_cvt_pk_bf16_f32 v168, v80, v81\n"
    "v_mfma_f32_32x32x16_bf16 v[128:143], v[224:227], v[160:163], v[128:143]\n"
    "v_exp_f32 v83, v83\n"
    "v_exp_f32 v84, v84\n"
    "v_cvt_pk_bf16_f32 v169, v82, v83\n"
    "v_exp_f32 v85, v85\n"
    "v_exp_f32 v86, v86\n"
    "v_cvt_pk_bf16_f32 v170, v84, v85\n"
    "v_exp_f32 v87, v87\n"
    "v_mfma_f32_32x32x16_bf16 v[112:127], v[188:191], v[180:183], v[112:127]\n"
    "v_exp_f32 v88, v88\n"
    "v_cvt_pk_bf16_f32 v171, v86, v87\n"
    "buffer_load_dwordx4 v[188:191], v1, s[20:23], s58 offen sc1\n"
    "v_exp_f32 v89, v89\n"
    "v_exp_f32 v90, v90\n"
    "v_cvt_pk_bf16_f32 v172, v88, v89\n"
    "v_exp_f32 v91, v91\n"
    "v_exp_f32 v92, v92\n"
    "v_mfma_f32_32x32x16_bf16 v[128:143], v[228:231], v[164:167], v[128:143]\n"
    "v_cvt_pk_bf16_f32 v173, v90, v91\n"
    "v_exp_f32 v93, v93\n"
    "buffer_load_dwordx4 v1, s[20:23], s97 offen offset:3072 sc1 lds\n"
    "v_exp_f32 v94, v94\n"
    "v_cvt_pk_bf16_f32 v174, v92, v93\n"
    "v_exp_f32 v95, v95\n"
    "v_exp_f32 v96, v96\n"
    "v_cvt_pk_bf16_f32 v175, v94, v95\n"
    "s_waitcnt vmcnt(12)\n"
    "v_mfma_f32_32x32x4_2b_bf16 v[64:95], v[202:203], v[184:185], 0\n"
    "v_exp_f32 v97, v97\n"
    "v_exp_f32 v98, v98\n"
    "v_cvt_pk_bf16_f32 v160, v96, v97\n"
    "v_exp_f32 v99, v99\n"
    "v_exp_f32 v100, v100\n"
    "v_cvt_pk_bf16_f32 v161, v98, v99\n"
    "v_mfma_f32_32x32x16_bf16 v[144:159], v[224:227], v[168:171], v[144:159]\n"
    "v_exp_f32 v101, v101\n"
    "v_exp_f32 v102, v102\n"
    "v_cvt_pk_bf16_f32 v162, v100, v101\n"
    "v_exp_f32 v103, v103\n"
    "v_exp_f32 v104, v104\n"
    "v_cvt_pk_bf16_f32 v163, v102, v103\n"
    "v_exp_f32 v105, v105\n"
    "v_mfma_f32_32x32x16_bf16 v[144:159], v[228:231], v[172:175], v[144:159]\n"
    "v_exp_f32 v106, v106\n"
    "v_cvt_pk_bf16_f32 v164, v104, v105\n"
    "buffer_load_dwordx4 v[224:227], v3, s[20:23], s58 offen offset:1024 sc1\n"
    "buffer_load_dwordx4 v[228:231], v3, s[20:23], s58 offen offset:1664 sc1\n"
    "v_exp_f32 v107, v107\n"
    "v_exp_f32 v108, v108\n"
    "v_cvt_pk_bf16_f32 v165, v106, v107\n"
    "v_exp_f32 v109, v109\n"
    "v_exp_f32 v110, v110\n"
    "s_waitcnt vmcnt(13)\n"
    "v_mfma_f32_32x32x16_bf16 v[64:79], v[192:195], v[176:179], v[64:79]\n"
    "v_cvt_pk_bf16_f32 v166, v108, v109\n"
    "v_exp_f32 v111, v111\n"
    "s_add_u32 s58, s58, 2432\n"
    "s_cmp_eq_u32 s58, s59\n"
    "s_cselect_b32 s41, 77824, 0\n"
    "s_sub_u32 s58, s58, s41\n"
    "buffer_load_dword v202, v4, s[20:23], s58 offen offset:2304 sc1\n"
    "v_exp_f32 v112, v112\n"
    "v_cvt_pk_bf16_f32 v167, v110, v111\n"
    "v_exp_f32 v113, v113\n"
    "v_exp_f32 v114, v114\n"
    "v_cvt_pk_bf16_f32 v168, v112, v113\n"
    "v_mfma_f32_32x32x16_bf16 v[128:143], v[232:235], v[160:163], v[128:143]\n"
    "v_exp_f32 v115, v115\n"
    "v_exp_f32 v116, v116\n"
    "v_cvt_pk_bf16_f32 v169, v114, v115\n"
    "v_exp_f32 v117, v117\n"
    "v_exp_f32 v118, v118\n"
    "v_cvt_pk_bf16_f32 v170, v116, v117\n"
    "v_exp_f32 v119, v119\n"
    "v_mfma_f32_32x32x16_bf16 v[80:95], v[192:195], v[180:183], v[80:95]\n"
    "v_exp_f32 v120, v120\n"
    "v_cvt_pk_bf16_f32 v171, v118, v119\n"
    "buffer_load_dwordx4 v[192:195], v1, s[20:23], s58 offen sc1\n"
    "v_exp_f32 v121, v121\n"
    "v_exp_f32 v122, v122\n"
    "v_cvt_pk_bf16_f32 v172, v120, v121\n"
    "v_exp_f32 v123, v123\n"
    "v_exp_f32 v124, v124\n"
    "v_mfma_f32_32x32x16_bf16 v[128:143], v[236:239], v[164:167], v[128:143]\n"
    "v_cvt_pk_bf16_f32 v173, v122, v123\n"
    "v_exp_f32 v125, v125\n"
    "s_add_u32 m0, m0, 4096\n"
    "s_add_u32 s97, s97, 4096\n"
    "buffer_load_dwordx4 v1, s[20:23], s97 offen sc1 lds\n"
    "v_exp_f32 v126, v126\n"
    "v_cvt_pk_bf16_f32 v174, v124, v125\n"
    "v_exp_f32 v127, v127\n"
    "v_exp_f32 v64, v64\n"
    "v_cvt_pk_bf16_f32 v175, v126, v127\n"
    "s_waitcnt vmcnt(12)\n"
    "v_mfma_f32_32x32x4_2b_bf16 v[96:127], v[204:205], v[184:185], 0\n"
    "v_exp_f32 v65, v65\n"
    "v_exp_f32 v66, v66\n"
    "v_cvt_pk_bf16_f32 v160, v64, v65\n"
    "v_exp_f32 v67, v67\n"
    "v_exp_f32 v68, v68\n"
    "v_cvt_pk_bf16_f32 v161, v66, v67\n"
    "v_mfma_f32_32x32x16_bf16 v[144:159], v[232:235], v[168:171], v[144:159]\n"
    "v_exp_f32 v69, v69\n"
    "v_exp_f32 v70, v70\n"
    "v_cvt_pk_bf16_f32 v162, v68, v69\n"
    "v_exp_f32 v71, v71\n"
    "v_exp_f32 v72, v72\n"
    "v_cvt_pk_bf16_f32 v163, v70, v71\n"
    "v_exp_f32 v73, v73\n"
    "v_mfma_f32_32x32x16_bf16 v[144:159], v[236:239], v[172:175], v[144:159]\n"
    "v_exp_f32 v74, v74\n"
    "v_cvt_pk_bf16_f32 v164, v72, v73\n"
    "buffer_load_dwordx4 v[232:235], v3, s[20:23], s58 offen offset:1024 sc1\n"
    "buffer_load_dwordx4 v[236:239], v3, s[20:23], s58 offen offset:1664 sc1\n"
    "v_exp_f32 v75, v75\n"
    "v_exp_f32 v76, v76\n"
    "v_cvt_pk_bf16_f32 v165, v74, v75\n"
    "v_exp_f32 v77, v77\n"
    "v_exp_f32 v78, v78\n"
    "s_waitcnt vmcnt(13)\n"
    "v_mfma_f32_32x32x16_bf16 v[96:111], v[196:199], v[176:179], v[96:111]\n"
    "v_cvt_pk_bf16_f32 v166, v76, v77\n"
    "v_exp_f32 v79, v79\n"
    "s_add_u32 s58, s58, 2432\n"
    "s_cmp_eq_u32 s58, s59\n"
    "s_cselect_b32 s41, 77824, 0\n"
    "s_sub_u32 s58, s58, s41\n"
    "buffer_load_dword v204, v4, s[20:23], s58 offen offset:2304 sc1\n"
    "v_exp_f32 v80, v80\n"
    "v_cvt_pk_bf16_f32 v167, v78, v79\n"
    "v_exp_f32 v81, v81\n"
    "v_exp_f32 v82, v82\n"
    "v_cvt_pk_bf16_f32 v168, v80, v81\n"
    "v_mfma_f32_32x32x16_bf16 v[128:143], v[208:211], v[160:163], v[128:143]\n"
    "v_exp_f32 v83, v83\n"
    "v_exp_f32 v84, v84\n"
    "v_cvt_pk_bf16_f32 v169, v82, v83\n"
    "v_exp_f32 v85, v85\n"
    "v_exp_f32 v86, v86\n"
    "v_cvt_pk_bf16_f32 v170, v84, v85\n"
    "v_exp_f32 v87, v87\n"
    "v_mfma_f32_32x32x16_bf16 v[112:127], v[196:199], v[180:183], v[112:127]\n"
    "v_exp_f32 v88, v88\n"
    "v_cvt_pk_bf16_f32 v171, v86, v87\n"
    "buffer_load_dwordx4 v[196:199], v1, s[20:23], s58 offen sc1\n"
    "v_exp_f32 v89, v89\n"
    "v_exp_f32 v90, v90\n"
    "v_cvt_pk_bf16_f32 v172, v88, v89\n"
    "v_exp_f32 v91, v91\n"
    "v_exp_f32 v92, v92\n"
    "v_mfma_f32_32x32x16_bf16 v[128:143], v[212:215], v[164:167], v[128:143]\n"
    "v_cvt_pk_bf16_f32 v173, v90, v91\n"
    "v_exp_f32 v93, v93\n"
    "buffer_load_dwordx4 v1, s[20:23], s97 offen offset:1024 sc1 lds\n"
    "v_exp_f32 v94, v94\n"
    "v_cvt_pk_bf16_f32 v174, v92, v93\n"
    "v_exp_f32 v95, v95\n"
    "v_exp_f32 v96, v96\n"
    "v_cvt_pk_bf16_f32 v175, v94, v95\n"
    "s_waitcnt vmcnt(12)\n"
    "v_mfma_f32_32x32x4_2b_bf16 v[64:95], v[200:201], v[184:185], 0\n"
    "v_exp_f32 v97, v97\n"
    "v_exp_f32 v98, v98\n"
    "v_cvt_pk_bf16_f32 v160, v96, v97\n"
    "v_exp_f32 v99, v99\n"
    "v_exp_f32 v100, v100\n"
    "v_cvt_pk_bf16_f32 v161, v98, v99\n"
    "v_mfma_f32_32x32x16_bf16 v[144:159], v[208:211], v[168:171], v[144:159]\n"
    "v_exp_f32 v101, v101\n"
    "v_exp_f32 v102, v102\n"
    "v_cvt_pk_bf16_f32 v162, v100, v101\n"
    "v_exp_f32 v103, v103\n"
    "v_exp_f32 v104, v104\n"
    "v_cvt_pk_bf16_f32 v163, v102, v103\n"
    "v_exp_f32 v105, v105\n"
    "v_mfma_f32_32x32x16_bf16 v[144:159], v[212:215], v[172:175], v[144:159]\n"
    "v_exp_f32 v106, v106\n"
    "v_cvt_pk_bf16_f32 v164, v104, v105\n"
    "buffer_load_dwordx4 v[208:211], v3, s[20:23], s58 offen offset:1024 sc1\n"
    "buffer_load_dwordx4 v[212:215], v3, s[20:23], s58 offen offset:1664 sc1\n"
    "v_exp_f32 v107, v107\n"
    "v_exp_f32 v108, v108\n"
    "v_cvt_pk_bf16_f32 v165, v106, v107\n"
    "v_exp_f32 v109, v109\n"
    "v_exp_f32 v110, v110\n"
    "s_waitcnt vmcnt(13)\n"
    "v_mfma_f32_32x32x16_bf16 v[64:79], v[188:191], v[176:179], v[64:79]\n"
    "v_cvt_pk_bf16_f32 v166, v108, v109\n"
    "v_exp_f32 v111, v111\n"
    "s_add_u32 s58, s58, 2432\n"
    "s_cmp_eq_u32 s58, s59\n"
    "s_cselect_b32 s41, 77824, 0\n"
    "s_sub_u32 s58, s58, s41\n"
    "buffer_load_dword v200, v4, s[20:23], s58 offen offset:2304 sc1\n"
    "v_exp_f32 v112, v112\n"
    "v_cvt_pk_bf16_f32 v167, v110, v111\n"
    "v_exp_f32 v113, v113\n"
    "v_exp_f32 v114, v114\n"
    "v_cvt_pk_bf16_f32 v168, v112, v113\n"
    "v_mfma_f32_32x32x16_bf16 v[128:143], v[216:219], v[160:163], v[128:143]\n"
    "v_exp_f32 v115, v115\n"
    "v_exp_f32 v116, v116\n"
    "v_cvt_pk_bf16_f32 v169, v114, v115\n"
    "v_exp_f32 v117, v117\n"
    "v_exp_f32 v118, v118\n"
    "v_cvt_pk_bf16_f32 v170, v116, v117\n"
    "v_exp_f32 v119, v119\n"
    "v_mfma_f32_32x32x16_bf16 v[80:95], v[188:191], v[180:183], v[80:95]\n"
    "v_exp_f32 v120, v120\n"
    "v_cvt_pk_bf16_f32 v171, v118, v119\n"
    "buffer_load_dwordx4 v[188:191], v1, s[20:23], s58 offen sc1\n"
    "v_exp_f32 v121, v121\n"
    "v_exp_f32 v122, v122\n"
    "v_cvt_pk_bf16_f32 v172, v120, v121\n"
    "v_exp_f32 v123, v123\n"
    "v_exp_f32 v124, v124\n"
    "v_mfma_f32_32x32x16_bf16 v[128:143], v[220:223], v[164:167], v[128:143]\n"
    "v_cvt_pk_bf16_f32 v173, v122, v123\n"
    "v_exp_f32 v125, v125\n"
    "buffer_load_dwordx4 v1, s[20:23], s97 offen offset:2048 sc1 lds\n"
    "v_exp_f32 v126, v126\n"
    "v_cvt_pk_bf16_f32 v174, v124, v125\n"
    "v_exp_f32 v127, v127\n"
    "v_exp_f32 v64, v64\n"
    "v_cvt_pk_bf16_f32 v175, v126, v127\n"
    "s_waitcnt vmcnt(12)\n"
    "v_mfma_f32_32x32x4_2b_bf16 v[96:127], v[202:203], v[184:185], 0\n"
    "v_exp_f32 v65, v65\n"
    "v_exp_f32 v66, v66\n"
    "v_cvt_pk_bf16_f32 v160, v64, v65\n"
    "v_exp_f32 v67, v67\n"
    "v_exp_f32 v68, v68\n"
    "v_cvt_pk_bf16_f32 v161, v66, v67\n"
    "v_mfma_f32_32x32x16_bf16 v[144:159], v[216:219], v[168:171], v[144:159]\n"
    "v_exp_f32 v69, v69\n"
    "v_exp_f32 v70, v70\n"
    "buffer_load_dword v18, v17, s[20:23], s44 offen sc1\n"
    "v_cvt_pk_bf16_f32 v162, v68, v69\n"
    "v_exp_f32 v71, v71\n"
    "v_exp_f32 v72, v72\n"
    "v_cvt_pk_bf16_f32 v163, v70, v71\n"
    "v_exp_f32 v73, v73\n"
    "v_mfma_f32_32x32x16_bf16 v[144:159], v[220:223], v[172:175], v[144:159]\n"
    "v_exp_f32 v74, v74\n"
    "v_cvt_pk_bf16_f32 v164, v72, v73\n"
    "buffer_load_dwordx4 v[216:219], v3, s[20:23], s58 offen offset:1024 sc1\n"
    "buffer_load_dwordx4 v[220:223], v3, s[20:23], s58 offen offset:1664 sc1\n"
    "v_exp_f32 v75, v75\n"
    "v_exp_f32 v76, v76\n"
    "v_cvt_pk_bf16_f32 v165, v74, v75\n"
    "v_exp_f32 v77, v77\n"
    "v_exp_f32 v78, v78\n"
    "s_waitcnt vmcnt(14)\n"
    "v_mfma_f32_32x32x16_bf16 v[96:111], v[192:195], v[176:179], v[96:111]\n"
    "v_cvt_pk_bf16_f32 v166, v76, v77\n"
    "v_exp_f32 v79, v79\n"
    "s_add_u32 s58, s58, 2432\n"
    "s_cmp_eq_u32 s58, s59\n"
    "s_cselect_b32 s41, 77824, 0\n"
    "s_sub_u32 s58, s58, s41\n"
    "buffer_load_dword v202, v4, s[20:23], s58 offen offset:2304 sc1\n"
    "v_exp_f32 v80, v80\n"
    "v_cvt_pk_bf16_f32 v167, v78, v79\n"
    "v_exp_f32 v81, v81\n"
    "v_exp_f32 v82, v82\n"
    "v_cvt_pk_bf16_f32 v168, v80, v81\n"
    "v_mfma_f32_32x32x16_bf16 v[128:143], v[224:227], v[160:163], v[128:143]\n"
    "v_exp_f32 v83, v83\n"
    "v_exp_f32 v84, v84\n"
    "v_cvt_pk_bf16_f32 v169, v82, v83\n"
    "v_exp_f32 v85, v85\n"
    "v_exp_f32 v86, v86\n"
    "v_cvt_pk_bf16_f32 v170, v84, v85\n"
    "v_exp_f32 v87, v87\n"
    "v_mfma_f32_32x32x16_bf16 v[112:127], v[192:195], v[180:183], v[112:127]\n"
    "v_exp_f32 v88, v88\n"
    "v_cvt_pk_bf16_f32 v171, v86, v87\n"
    "buffer_load_dwordx4 v[192:195], v1, s[20:23], s58 offen sc1\n"
    "v_exp_f32 v89, v89\n"
    "v_exp_f32 v90, v90\n"
    "v_cvt_pk_bf16_f32 v172, v88, v89\n"
    "v_exp_f32 v91, v91\n"
    "v_exp_f32 v92, v92\n"
    "v_mfma_f32_32x32x16_bf16 v[128:143], v[228:231], v[164:167], v[128:143]\n"
    "v_cvt_pk_bf16_f32 v173, v90, v91\n"
    "v_exp_f32 v93, v93\n"
    "buffer_load_dwordx4 v1, s[20:23], s97 offen offset:3072 sc1 lds\n"
    "v_exp_f32 v94, v94\n"
    "v_cvt_pk_bf16_f32 v174, v92, v93\n"
    "v_exp_f32 v95, v95\n"
    "v_exp_f32 v96, v96\n"
    "v_cvt_pk_bf16_f32 v175, v94, v95\n"
    "s_waitcnt vmcnt(13)\n"
    "v_mfma_f32_32x32x4_2b_bf16 v[64:95], v[204:205], v[184:185], 0\n"
    "v_exp_f32 v97, v97\n"
    "v_exp_f32 v98, v98\n"
    "v_cvt_pk_bf16_f32 v160, v96, v97\n"
    "v_exp_f32 v99, v99\n"
    "v_exp_f32 v100, v100\n"
    "v_cvt_pk_bf16_f32 v161, v98, v99\n"
    "v_mfma_f32_32x32x16_bf16 v[144:159], v[224:227], v[168:171], v[144:159]\n"
    "v_exp_f32 v101, v101\n"
    "v_exp_f32 v102, v102\n"
    "v_cvt_pk_bf16_f32 v162, v100, v101\n"
    "v_exp_f32 v103, v103\n"
    "v_exp_f32 v104, v104\n"
    "v_cvt_pk_bf16_f32 v163, v102, v103\n"
    "v_exp_f32 v105, v105\n"
    "v_mfma_f32_32x32x16_bf16 v[144:159], v[228:231], v[172:175], v[144:159]\n"
    "v_exp_f32 v106, v106\n"
    "v_cvt_pk_bf16_f32 v164, v104, v105\n"
    "buffer_load_dwordx4 v[224:227], v3, s[20:23], s58 offen offset:1024 sc1\n"
    "buffer_load_dwordx4 v[228:231], v3, s[20:23], s58 offen offset:1664 sc1\n"
    "v_exp_f32 v107, v107\n"
    "v_exp_f32 v108, v108\n"
    "v_cvt_pk_bf16_f32 v165, v106, v107\n"
    "v_exp_f32 v109, v109\n"
    "v_exp_f32 v110, v110\n"
    "s_waitcnt vmcnt(14)\n"
    "v_mfma_f32_32x32x16_bf16 v[64:79], v[196:199], v[176:179], v[64:79]\n"
    "v_cvt_pk_bf16_f32 v166, v108, v109\n"
    "v_exp_f32 v111, v111\n"
    "s_add_u32 s58, s58, 2432\n"
    "s_cmp_eq_u32 s58, s59\n"
    "s_cselect_b32 s41, 77824, 0\n"
    "s_sub_u32 s58, s58, s41\n"
    "buffer_load_dword v204, v4, s[20:23], s58 offen offset:2304 sc1\n"
    "v_exp_f32 v112, v112\n"
    "v_cvt_pk_bf16_f32 v167, v110, v111\n"
    "v_exp_f32 v113, v113\n"
    "v_exp_f32 v114, v114\n"
    "v_cvt_pk_bf16_f32 v168, v112, v113\n"
    "v_mfma_f32_32x32x16_bf16 v[128:143], v[232:235], v[160:163], v[128:143]\n"
    "v_exp_f32 v115, v115\n"
    "v_exp_f32 v116, v116\n"
    "v_cvt_pk_bf16_f32 v169, v114, v115\n"
    "v_exp_f32 v117, v117\n"
    "v_exp_f32 v118, v118\n"
    "v_cvt_pk_bf16_f32 v170, v116, v117\n"
    "v_exp_f32 v119, v119\n"
    "v_mfma_f32_32x32x16_bf16 v[80:95], v[196:199], v[180:183], v[80:95]\n"
    "v_exp_f32 v120, v120\n"
    "v_cvt_pk_bf16_f32 v171, v118, v119\n"
    "buffer_load_dwordx4 v[196:199], v1, s[20:23], s58 offen sc1\n"
    "v_exp_f32 v121, v121\n"
    "v_exp_f32 v122, v122\n"
    "v_cvt_pk_bf16_f32 v172, v120, v121\n"
    "v_exp_f32 v123, v123\n"
    "v_exp_f32 v124, v124\n"
    "v_mfma_f32_32x32x16_bf16 v[128:143], v[236:239], v[164:167], v[128:143]\n"
    "v_cvt_pk_bf16_f32 v173, v122, v123\n"
    "v_exp_f32 v125, v125\n"
    "v_exp_f32 v126, v126\n"
    "v_cvt_pk_bf16_f32 v174, v124, v125\n"
    "v_exp_f32 v127, v127\n"
    "v_exp_f32 v64, v64\n"
    "v_cvt_pk_bf16_f32 v175, v126, v127\n"
    "s_waitcnt vmcnt(12)\n"
    "v_mfma_f32_32x32x4_2b_bf16 v[96:127], v[200:201], v[184:185], 0\n"
    "v_exp_f32 v65, v65\n"
    "v_exp_f32 v66, v66\n"
    "v_cvt_pk_bf16_f32 v160, v64, v65\n"
    "v_exp_f32 v67, v67\n"
    "v_exp_f32 v68, v68\n"
    "v_cvt_pk_bf16_f32 v161, v66, v67\n"
    "v_mfma_f32_32x32x16_bf16 v[144:159], v[232:235], v[168:171], v[144:159]\n"
    "v_exp_f32 v69, v69\n"
    "v_exp_f32 v70, v70\n"
    "s_waitcnt vmcnt(9)\n"
    "v_cmp_ne_u32 vcc, s95, v18\n"
    "s_cbranch_vccz .Lk_jpd_61\n"
    "s_mov_b32 s60, 0x8000\n"
    ".Lk_jp_59:\n"
    "s_sleep 4\n"
    "buffer_load_dword v18, v17, s[20:23], s44 offen sc1\n"
    "s_waitcnt vmcnt(0)\n"
    "v_cmp_ne_u32 vcc, s95, v18\n"
    "s_cbranch_vccz .Lk_jpd_61\n"
    "s_sub_u32 s60, s60, 1\n"
    "s_cbranch_scc0 .Lk_jp_59\n"
    ".Lk_jpd_61:\n"
    "v_cvt_pk_bf16_f32 v162, v68, v69\n"
    "v_exp_f32 v71, v71\n"
    "v_exp_f32 v72, v72\n"
    "v_cvt_pk_bf16_f32 v163, v70, v71\n"
    "v_exp_f32 v73, v73\n"
    "v_mfma_f32_32x32x16_bf16 v[144:159], v[236:239], v[172:175], v[144:159]\n"
    "v_exp_f32 v74, v74\n"
    "v_cvt_pk_bf16_f32 v164, v72, v73\n"
    "buffer_load_dwordx4 v[232:235], v3, s[20:23], s58 offen offset:1024 sc1\n"
    "buffer_load_dwordx4 v[236:239], v3, s[20:23], s58 offen offset:1664 sc1\n"
    "v_exp_f32 v75, v75\n"
    "v_exp_f32 v76, v76\n"
    "v_cvt_pk_bf16_f32 v165, v74, v75\n"
    "v_exp_f32 v77, v77\n"
    "v_exp_f32 v78, v78\n"
    "v_mfma_f32_32x32x16_bf16 v[96:111], v[188:191], v[176:179], v[96:111]\n"
    "v_cvt_pk_bf16_f32 v166, v76, v77\n"
    "v_exp_f32 v79, v79\n"
    "s_add_u32 s58, s58, 2432\n"
    "s_cmp_eq_u32 s58, s59\n"
    "s_cselect_b32 s41, 77824, 0\n"
    "s_sub_u32 s58, s58, s41\n"
    "buffer_load_dword v200, v4, s[20:23], s58 offen offset:2304 sc1\n"
    "v_exp_f32 v80, v80\n"
    "v_cvt_pk_bf16_f32 v167, v78, v79\n"
    "v_exp_f32 v81, v81\n"
    "v_exp_f32 v82, v82\n"
    "v_cvt_pk_bf16_f32 v168, v80, v81\n"
    "v_mfma_f32_32x32x16_bf16 v[128:143], v[208:211], v[160:163], v[128:143]\n"
    "v_exp_f32 v83, v83\n"
    "v_exp_f32 v84, v84\n"
    "v_cvt_pk_bf16_f32 v169, v82, v83\n"
    "v_exp_f32 v85, v85\n"
    "v_exp_f32 v86, v86\n"
    "v_cvt_pk_bf16_f32 v170, v84, v85\n"
    "v_exp_f32 v87, v87\n"
    "v_mfma_f32_32x32x16_bf16 v[112:127], v[188:191], v[180:183], v[112:127]\n"
    "v_exp_f32 v88, v88\n"
    "v_cvt_pk_bf16_f32 v171, v86, v87\n"
    "buffer_load_dwordx4 v[188:191], v1, s[20:23], s58 offen sc1\n"
    "v_exp_f32 v89, v89\n"
    "v_exp_f32 v90, v90\n"
    "v_cvt_pk_bf16_f32 v172, v88, v89\n"
    "v_exp_f32 v91, v91\n"
    "v_exp_f32 v92, v92\n"
    "v_mfma_f32_32x32x16_bf16 v[128:143], v[212:215], v[164:167], v[128:143]\n"
    "v_cvt_pk_bf16_f32 v173, v90, v91\n"
    "v_exp_f32 v93, v93\n"
    "buffer_load_dwordx4 v[240:243], v246, s[24:27], s52 offen\n"
    "buffer_load_dwordx2 v[244:245], v247, s[24:27], s52 offen offset:128\n"
    "v_exp_f32 v94, v94\n"
    "v_cvt_pk_bf16_f32 v174, v92, v93\n"
    "v_exp_f32 v95, v95\n"
    "v_exp_f32 v96, v96\n"
    "v_cvt_pk_bf16_f32 v175, v94, v95\n"
    "s_waitcnt vmcnt(12)\n"
    "v_mfma_f32_32x32x4_2b_bf16 v[64:95], v[202:203], v[184:185], 0\n"
    "v_exp_f32 v97, v97\n"
    "v_exp_f32 v98, v98\n"
    "v_cvt_pk_bf16_f32 v160, v96, v97\n"
    "v_exp_f32 v99, v99\n"
    "v_exp_f32 v100, v100\n"
    "v_cvt_pk_bf16_f32 v161, v98, v99\n"
    "v_mfma_f32_32x32x16_bf16 v[144:159], v[208:211], v[168:171], v[144:159]\n"
    "v_exp_f32 v101, v101\n"
    "v_exp_f32 v102, v102\n"
    "v_cvt_pk_bf16_f32 v162, v100, v101\n"
    "v_exp_f32 v103, v103\n"
    "v_exp_f32 v104, v104\n"
    "v_cvt_pk_bf16_f32 v163, v102, v103\n"
    "v_exp_f32 v105, v105\n"
    "v_mfma_f32_32x32x16_bf16 v[144:159], v[212:215], v[172:175], v[144:159]\n"
    "v_exp_f32 v106, v106\n"
    "v_cvt_pk_bf16_f32 v164, v104, v105\n"
    "buffer_load_dwordx4 v[208:211], v3, s[20:23], s58 offen offset:1024 sc1\n"
    "buffer_load_dwordx4 v[212:215], v3, s[20:23], s58 offen offset:1664 sc1\n"
    "v_exp_f32 v107, v107\n"
    "v_exp_f32 v108, v108\n"
    "v_cvt_pk_bf16_f32 v165, v106, v107\n"
    "v_exp_f32 v109, v109\n"
    "v_exp_f32 v110, v110\n"
    "s_waitcnt vmcnt(13)\n"
    "v_mfma_f32_32x32x16_bf16 v[64:79], v[192:195], v[176:179], v[64:79]\n"
    "v_cvt_pk_bf16_f32 v166, v108, v109\n"
    "v_exp_f32 v111, v111\n"
    "s_add_u32 s58, s58, 2432\n"
    "s_cmp_eq_u32 s58, s59\n"
    "s_cselect_b32 s41, 77824, 0\n"
    "s_sub_u32 s58, s58, s41\n"
    "buffer_load_dword v202, v4, s[20:23], s58 offen offset:2304 sc1\n"
    "v_exp_f32 v112, v112\n"
    "v_cvt_pk_bf16_f32 v167, v110, v111\n"
    "v_exp_f32 v113, v113\n"
    "v_exp_f32 v114, v114\n"
    "v_cvt_pk_bf16_f32 v168, v112, v113\n"
    "v_mfma_f32_32x32x16_bf16 v[128:143], v[216:219], v[160:163], v[128:143]\n"
    "v_exp_f32 v115, v115\n"
    "v_exp_f32 v116, v116\n"
    "v_cvt_pk_bf16_f32 v169, v114, v115\n"
    "v_exp_f32 v117, v117\n"
    "v_exp_f32 v118, v118\n"
    "v_cvt_pk_bf16_f32 v170, v116, v117\n"
    "v_exp_f32 v119, v119\n"
    "v_mfma_f32_32x32x16_bf16 v[80:95], v[192:195], v[180:183], v[80:95]\n"
    "v_exp_f32 v120, v120\n"
    "v_cvt_pk_bf16_f32 v171, v118, v119\n"
    "buffer_load_dwordx4 v[192:195], v1, s[20:23], s58 offen sc1\n"
    "v_exp_f32 v121, v121\n"
    "v_exp_f32 v122, v122\n"
    "v_cvt_pk_bf16_f32 v172, v120, v121\n"
    "v_exp_f32 v123, v123\n"
    "v_exp_f32 v124, v124\n"
    "v_mfma_f32_32x32x16_bf16 v[128:143], v[220:223], v[164:167], v[128:143]\n"
    "v_cvt_pk_bf16_f32 v173, v122, v123\n"
    "v_exp_f32 v125, v125\n"
    "s_waitcnt lgkmcnt(6)\n"
    "buffer_load_dwordx4 v[28:31], v1, s[20:23], s96 offen sc1\n"
    "v_exp_f32 v126, v126\n"
    "v_cvt_pk_bf16_f32 v174, v124, v125\n"
    "v_exp_f32 v127, v127\n"
    "v_exp_f32 v64, v64\n"
    "v_cvt_pk_bf16_f32 v175, v126, v127\n"
    "s_waitcnt vmcnt(12)\n"
    "v_mfma_f32_32x32x4_2b_bf16 v[96:127], v[204:205], v[184:185], 0\n"
    "v_exp_f32 v65, v65\n"
    "v_exp_f32 v66, v66\n"
    "v_cvt_pk_bf16_f32 v160, v64, v65\n"
    "v_exp_f32 v67, v67\n"
    "v_exp_f32 v68, v68\n"
    "v_cvt_pk_bf16_f32 v161, v66, v67\n"
    "v_mfma_f32_32x32x16_bf16 v[144:159], v[216:219], v[168:171], v[144:159]\n"
    "v_exp_f32 v69, v69\n"
    "v_exp_f32 v70, v70\n"
    "v_cvt_pk_bf16_f32 v162, v68, v69\n"
    "v_exp_f32 v71, v71\n"
    "v_exp_f32 v72, v72\n"
    "v_cvt_pk_bf16_f32 v163, v70, v71\n"
    "v_exp_f32 v73, v73\n"
    "v_mfma_f32_32x32x16_bf16 v[144:159], v[220:223], v[172:175], v[144:159]\n"
    "v_exp_f32 v74, v74\n"
    "v_cvt_pk_bf16_f32 v164, v72, v73\n"
    "buffer_load_dwordx4 v[216:219], v3, s[20:23], s58 offen offset:1024 sc1\n"
    "buffer_load_dwordx4 v[220:223], v3, s[20:23], s58 offen offset:1664 sc1\n"
    "v_exp_f32 v75, v75\n"
    "v_exp_f32 v76, v76\n"
    "v_cvt_pk_bf16_f32 v165, v74, v75\n"
    "v_exp_f32 v77, v77\n"
    "v_exp_f32 v78, v78\n"
    "s_waitcnt vmcnt(13)\n"
    "v_mfma_f32_32x32x16_bf16 v[96:111], v[196:199], v[176:179], v[96:111]\n"
    "v_cvt_pk_bf16_f32 v166, v76, v77\n"
    "v_exp_f32 v79, v79\n"
    "s_add_u32 s58, s58, 2432\n"
    "s_cmp_eq_u32 s58, s59\n"
    "s_cselect_b32 s41, 77824, 0\n"
    "s_sub_u32 s58, s58, s41\n"
    "buffer_load_dword v204, v4, s[20:23], s58 offen offset:2304 sc1\n"
    "v_exp_f32 v80, v80\n"
    "v_cvt_pk_bf16_f32 v167, v78, v79\n"
    "v_exp_f32 v81, v81\n"
    "v_exp_f32 v82, v82\n"
    "v_cvt_pk_bf16_f32 v168, v80, v81\n"
    "v_mfma_f32_32x32x16_bf16 v[128:143], v[224:227], v[160:163], v[128:143]\n"
    "v_exp_f32 v83, v83\n"
    "v_exp_f32 v84, v84\n"
    "v_cvt_pk_bf16_f32 v169, v82, v83\n"
    "v_exp_f32 v85, v85\n"
    "v_exp_f32 v86, v86\n"
    "v_cvt_pk_bf16_f32 v170, v84, v85\n"
    "v_exp_f32 v87, v87\n"
    "v_mfma_f32_32x32x16_bf16 v[112:127], v[196:199], v[180:183], v[112:127]\n"
    "v_exp_f32 v88, v88\n"
    "v_cvt_pk_bf16_f32 v171, v86, v87\n"
    "buffer_load_dwordx4 v[196:199], v1, s[20:23], s58 offen sc1\n"
    "v_exp_f32 v89, v89\n"
    "v_exp_f32 v90, v90\n"
    "v_cvt_pk_bf16_f32 v172, v88, v89\n"
    "v_exp_f32 v91, v91\n"
    "v_exp_f32 v92, v92\n"
    "v_mfma_f32_32x32x16_bf16 v[128:143], v[228:231], v[164:167], v[128:143]\n"
    "v_cvt_pk_bf16_f32 v173, v90, v91\n"
    "v_exp_f32 v93, v93\n"
    "s_waitcnt lgkmcnt(4)\n"
    "buffer_load_dwordx4 v[32:35], v252, s[20:23], s96 offen sc1\n"
    "v_exp_f32 v94, v94\n"
    "v_cvt_pk_bf16_f32 v174, v92, v93\n"
    "v_exp_f32 v95, v95\n"
    "v_exp_f32 v96, v96\n"
    "v_cvt_pk_bf16_f32 v175, v94, v95\n"
    "s_waitcnt vmcnt(13)\n"
    "v_mfma_f32_32x32x4_2b_bf16 v[64:95], v[200:201], v[184:185], 0\n"
    "v_exp_f32 v97, v97\n"
    "v_exp_f32 v98, v98\n"
    "v_cvt_pk_bf16_f32 v160, v96, v97\n"
    "v_exp_f32 v99, v99\n"
    "v_exp_f32 v100, v100\n"
    "v_cvt_pk_bf16_f32 v161, v98, v99\n"
    "v_mfma_f32_32x32x16_bf16 v[144:159], v[224:227], v[168:171], v[144:159]\n"
    "v_exp_f32 v101, v101\n"
    "v_exp_f32 v102, v102\n"
    "v_cvt_pk_bf16_f32 v162, v100, v101\n"
    "v_exp_f32 v103, v103\n"
    "v_exp_f32 v104, v104\n"
    "v_cvt_pk_bf16_f32 v163, v102, v103\n"
    "v_exp_f32 v105, v105\n"
    "v_mfma_f32_32x32x16_bf16 v[144:159], v[228:231], v[172:175], v[144:159]\n"
    "v_exp_f32 v106, v106\n"
    "v_cvt_pk_bf16_f32 v164, v104, v105\n"
    "buffer_load_dwordx4 v[224:227], v3, s[20:23], s58 offen offset:1024 sc1\n"
    "buffer_load_dwordx4 v[228:231], v3, s[20:23], s58 offen offset:1664 sc1\n"
    "v_exp_f32 v107, v107\n"
    "v_exp_f32 v108, v108\n"
    "v_cvt_pk_bf16_f32 v165, v106, v107\n"
    "v_exp_f32 v109, v109\n"
    "v_exp_f32 v110, v110\n"
    "s_waitcnt vmcnt(14)\n"
    "v_mfma_f32_32x32x16_bf16 v[64:79], v[188:191], v[176:179], v[64:79]\n"
    "v_cvt_pk_bf16_f32 v166, v108, v109\n"
    "v_exp_f32 v111, v111\n"
    "s_add_u32 s58, s58, 2432\n"
    "s_cmp_eq_u32 s58, s59\n"
    "s_cselect_b32 s41, 77824, 0\n"
    "s_sub_u32 s58, s58, s41\n"
    "buffer_load_dword v200, v4, s[20:23], s58 offen offset:2304 sc1\n"
    "v_exp_f32 v112, v112\n"
    "v_cvt_pk_bf16_f32 v167, v110, v111\n"
    "v_exp_f32 v113, v113\n"
    "v_exp_f32 v114, v114\n"
    "v_cvt_pk_bf16_f32 v168, v112, v113\n"
    "v_mfma_f32_32x32x16_bf16 v[128:143], v[232:235], v[160:163], v[128:143]\n"
    "v_exp_f32 v115, v115\n"
    "v_exp_f32 v116, v116\n"
    "v_cvt_pk_bf16_f32 v169, v114, v115\n"
    "v_exp_f32 v117, v117\n"
    "v_exp_f32 v118, v118\n"
    "v_cvt_pk_bf16_f32 v170, v116, v117\n"
    "v_exp_f32 v119, v119\n"
    "v_mfma_f32_32x32x16_bf16 v[80:95], v[188:191], v[180:183], v[80:95]\n"
    "v_exp_f32 v120, v120\n"
    "v_cvt_pk_bf16_f32 v171, v118, v119\n"
    "buffer_load_dwordx4 v[188:191], v1, s[20:23], s58 offen sc1\n"
    "v_exp_f32 v121, v121\n"
    "v_exp_f32 v122, v122\n"
    "v_cvt_pk_bf16_f32 v172, v120, v121\n"
    "v_exp_f32 v123, v123\n"
    "v_exp_f32 v124, v124\n"
    "v_mfma_f32_32x32x16_bf16 v[128:143], v[236:239], v[164:167], v[128:143]\n"
    "v_cvt_pk_bf16_f32 v173, v122, v123\n"
    "v_exp_f32 v125, v125\n"
    "s_waitcnt lgkmcnt(2)\n"
    "buffer_load_dwordx4 v[36:39], v253, s[20:23], s96 offen sc1\n"
    "v_exp_f32 v126, v126\n"
    "v_cvt_pk_bf16_f32 v174, v124, v125\n"
    "v_exp_f32 v127, v127\n"
    "v_exp_f32 v64, v64\n"
    "v_cvt_pk_bf16_f32 v175, v126, v127\n"
    "s_waitcnt vmcnt(12)\n"
    "v_mfma_f32_32x32x4_2b_bf16 v[96:127], v[202:203], v[184:185], 0\n"
    "v_exp_f32 v65, v65\n"
    "v_exp_f32 v66, v66\n"
    "v_cvt_pk_bf16_f32 v160, v64, v65\n"
    "v_exp_f32 v67, v67\n"
    "v_exp_f32 v68, v68\n"
    "v_cvt_pk_bf16_f32 v161, v66, v67\n"
    "v_mfma_f32_32x32x16_bf16 v[144:159], v[232:235], v[168:171], v[144:159]\n"
    "v_exp_f32 v69, v69\n"
    "v_exp_f32 v70, v70\n"
    "v_cvt_pk_bf16_f32 v162, v68, v69\n"
    "v_exp_f32 v71, v71\n"
    "v_exp_f32 v72, v72\n"
    "v_cvt_pk_bf16_f32 v163, v70, v71\n"
    "v_exp_f32 v73, v73\n"
    "v_mfma_f32_32x32x16_bf16 v[144:159], v[236:239], v[172:175], v[144:159]\n"
    "v_exp_f32 v74, v74\n"
    "v_cvt_pk_bf16_f32 v164, v72, v73\n"
    "buffer_load_dwordx4 v[232:235], v3, s[20:23], s58 offen offset:1024 sc1\n"
    "buffer_load_dwordx4 v[236:239], v3, s[20:23], s58 offen offset:1664 sc1\n"
    "v_exp_f32 v75, v75\n"
    "v_exp_f32 v76, v76\n"
    "v_cvt_pk_bf16_f32 v165, v74, v75\n"
    "v_exp_f32 v77, v77\n"
    "v_exp_f32 v78, v78\n"
    "s_waitcnt vmcnt(13)\n"
    "v_mfma_f32_32x32x16_bf16 v[96:111], v[192:195], v[176:179], v[96:111]\n"
    "v_cvt_pk_bf16_f32 v166, v76, v77\n"
    "v_exp_f32 v79, v79\n"
    "s_add_u32 s58, s58, 2432\n"
    "s_cmp_eq_u32 s58, s59\n"
    "s_cselect_b32 s41, 77824, 0\n"
    "s_sub_u32 s58, s58, s41\n"
    "buffer_load_dword v202, v4, s[20:23], s58 offen offset:2304 sc1\n"
    "v_exp_f32 v80, v80\n"
    "v_cvt_pk_bf16_f32 v167, v78, v79\n"
    "v_exp_f32 v81, v81\n"
    "v_exp_f32 v82, v82\n"
    "v_cvt_pk_bf16_f32 v168, v80, v81\n"
    "v_mfma_f32_32x32x16_bf16 v[128:143], v[208:211], v[160:163], v[128:143]\n"
    "v_exp_f32 v83, v83\n"
    "v_exp_f32 v84, v84\n"
    "v_cvt_pk_bf16_f32 v169, v82, v83\n"
    "v_exp_f32 v85, v85\n"
    "v_exp_f32 v86, v86\n"
    "v_cvt_pk_bf16_f32 v170, v84, v85\n"
    "v_exp_f32 v87, v87\n"
    "v_mfma_f32_32x32x16_bf16 v[112:127], v[192:195], v[180:183], v[112:127]\n"
    "v_exp_f32 v88, v88\n"
    "v_cvt_pk_bf16_f32 v171, v86, v87\n"
    "buffer_load_dwordx4 v[192:195], v1, s[20:23], s58 offen sc1\n"
    "v_exp_f32 v89, v89\n"
    "v_exp_f32 v90, v90\n"
    "v_cvt_pk_bf16_f32 v172, v88, v89\n"
    "v_exp_f32 v91, v91\n"
    "v_exp_f32 v92, v92\n"
    "v_mfma_f32_32x32x16_bf16 v[128:143], v[212:215], v[164:167], v[128:143]\n"
    "v_cvt_pk_bf16_f32 v173, v90, v91\n"
    "v_exp_f32 v93, v93\n"
    "s_waitcnt lgkmcnt(0)\n"
    "buffer_load_dwordx4 v[40:43], v254, s[20:23], s96 offen sc1\n"
    "v_exp_f32 v94, v94\n"
    "v_cvt_pk_bf16_f32 v174, v92, v93\n"
    "v_exp_f32 v95, v95\n"
    "v_exp_f32 v96, v96\n"
    "v_cvt_pk_bf16_f32 v175, v94, v95\n"
    "s_waitcnt vmcnt(12)\n"
    "v_mfma_f32_32x32x4_2b_bf16 v[64:95], v[204:205], v[184:185], 0\n"
    "v_exp_f32 v97, v97\n"
    "v_exp_f32 v98, v98\n"
    "v_cvt_pk_bf16_f32 v160, v96, v97\n"
    "v_exp_f32 v99, v99\n"
    "v_exp_f32 v100, v100\n"
    "v_cvt_pk_bf16_f32 v161, v98, v99\n"
    "v_mfma_f32_32x32x16_bf16 v[144:159], v[208:211], v[168:171], v[144:159]\n"
    "v_exp_f32 v101, v101\n"
    "v_exp_f32 v102, v102\n"
    "v_cvt_pk_bf16_f32 v162, v100, v101\n"
    "v_exp_f32 v103, v103\n"
    "v_exp_f32 v104, v104\n"
    "v_cvt_pk_bf16_f32 v163, v102, v103\n"
    "v_exp_f32 v105, v105\n"
    "v_mfma_f32_32x32x16_bf16 v[144:159], v[212:215], v[172:175], v[144:159]\n"
    "v_exp_f32 v106, v106\n"
    "v_cvt_pk_bf16_f32 v164, v104, v105\n"
    "buffer_load_dwordx4 v[208:211], v3, s[20:23], s58 offen offset:1024 sc1\n"
    "buffer_load_dwordx4 v[212:215], v3, s[20:23], s58 offen offset:1664 sc1\n"
    "v_exp_f32 v107, v107\n"
    "v_exp_f32 v108, v108\n"
    "v_cvt_pk_bf16_f32 v165, v106, v107\n"
    "v_exp_f32 v109, v109\n"
    "v_exp_f32 v110, v110\n"
    "s_waitcnt vmcnt(13)\n"
    "v_mfma_f32_32x32x16_bf16 v[64:79], v[196:199], v[176:179], v[64:79]\n"
    "v_cvt_pk_bf16_f32 v166, v108, v109\n"
    "v_exp_f32 v111, v111\n"
    "s_add_u32 s58, s58, 2432\n"
    "s_cmp_eq_u32 s58, s59\n"
    "s_cselect_b32 s41, 77824, 0\n"
    "s_sub_u32 s58, s58, s41\n"
    "buffer_load_dword v204, v4, s[20:23], s58 offen offset:2304 sc1\n"
    "v_exp_f32 v112, v112\n"
    "v_cvt_pk_bf16_f32 v167, v110, v111\n"
    "v_exp_f32 v113, v113\n"
    "v_exp_f32 v114, v114\n"
    "v_cvt_pk_bf16_f32 v168, v112, v113\n"
    "v_mfma_f32_32x32x16_bf16 v[128:143], v[216:219], v[160:163], v[128:143]\n"
    "v_exp_f32 v115, v115\n"
    "v_exp_f32 v116, v116\n"
    "v_cvt_pk_bf16_f32 v169, v114, v115\n"
    "v_exp_f32 v117, v117\n"
    "v_exp_f32 v118, v118\n"
    "v_cvt_pk_bf16_f32 v170, v116, v117\n"
    "v_exp_f32 v119, v119\n"
    "v_mfma_f32_32x32x16_bf16 v[80:95], v[196:199], v[180:183], v[80:95]\n"
    "v_exp_f32 v120, v120\n"
    "v_cvt_pk_bf16_f32 v171, v118, v119\n"
    "buffer_load_dwordx4 v[196:199], v1, s[20:23], s58 offen sc1\n"
    "v_exp_f32 v121, v121\n"
    "v_exp_f32 v122, v122\n"
    "v_cvt_pk_bf16_f32 v172, v120, v121\n"
    "v_exp_f32 v123, v123\n"
    "v_exp_f32 v124, v124\n"
    "v_mfma_f32_32x32x16_bf16 v[128:143], v[220:223], v[164:167], v[128:143]\n"
    "v_cvt_pk_bf16_f32 v173, v122, v123\n"
    "v_exp_f32 v125, v125\n"
    "buffer_load_dwordx4 v[44:47], v1, s[20:23], s96 offen offset:1024 sc1\n"
    "v_exp_f32 v126, v126\n"
    "v_cvt_pk_bf16_f32 v174, v124, v125\n"
    "v_exp_f32 v127, v127\n"
    "v_exp_f32 v64, v64\n"
    "v_cvt_pk_bf16_f32 v175, v126, v127\n"
    "s_waitcnt vmcnt(12)\n"
    "v_mfma_f32_32x32x4_2b_bf16 v[96:127], v[200:201], v[184:185], 0\n"
    "v_exp_f32 v65, v65\n"
    "v_exp_f32 v66, v66\n"
    "s_cmp_eq_u32 s40, 0\n"
    "s_cbranch_scc1 .Lk_pr_62\n"
    "s_setprio 0\n"
    ".Lk_pr_62:\n"
    "v_cvt_pk_bf16_f32 v160, v64, v65\n"
    "v_exp_f32 v67, v67\n"
    "v_exp_f32 v68, v68\n"
    "v_cvt_pk_bf16_f32 v161, v66, v67\n"
    "v_mfma_f32_32x32x16_bf16 v[144:159], v[216:219], v[168:171], v[144:159]\n"
    "v_exp_f32 v69, v69\n"
    "v_exp_f32 v70, v70\n"
    "v_cvt_pk_bf16_f32 v162, v68, v69\n"
    "v_exp_f32 v71, v71\n"
    "v_exp_f32 v72, v72\n"
    "v_cvt_pk_bf16_f32 v163, v70, v71\n"
    "v_exp_f32 v73, v73\n"
    "v_mfma_f32_32x32x16_bf16 v[144:159], v[220:223], v[172:175], v[144:159]\n"
    "v_exp_f32 v74, v74\n"
    "v_cvt_pk_bf16_f32 v164, v72, v73\n"
    "buffer_load_dwordx4 v[216:219], v3, s[20:23], s58 offen offset:1024 sc1\n"
    "buffer_load_dwordx4 v[220:223], v3, s[20:23], s58 offen offset:1664 sc1\n"
    "v_exp_f32 v75, v75\n"
    "v_exp_f32 v76, v76\n"
    "v_cvt_pk_bf16_f32 v165, v74, v75\n"
    "v_exp_f32 v77, v77\n"
    "v_exp_f32 v78, v78\n"
    "s_waitcnt vmcnt(13)\n"
    "v_mfma_f32_32x32x16_bf16 v[96:111], v[188:191], v[176:179], v[96:111]\n"
    "v_cvt_pk_bf16_f32 v166, v76, v77\n"
    "v_exp_f32 v79, v79\n"
    "s_add_u32 s58, s58, 2432\n"
    "s_cmp_eq_u32 s58, s59\n"
    "s_cselect_b32 s41, 77824, 0\n"
    "s_sub_u32 s58, s58, s41\n"
    "buffer_load_dword v200, v4, s[20:23], s58 offen offset:2304 sc1\n"
    "v_exp_f32 v80, v80\n"
    "v_cvt_pk_bf16_f32 v167, v78, v79\n"
    "v_exp_f32 v81, v81\n"
    "v_exp_f32 v82, v82\n"
    "v_cvt_pk_bf16_f32 v168, v80, v81\n"
    "v_mfma_f32_32x32x16_bf16 v[128:143], v[224:227], v[160:163], v[128:143]\n"
    "v_exp_f32 v83, v83\n"
    "v_exp_f32 v84, v84\n"
    "v_cvt_pk_bf16_f32 v169, v82, v83\n"
    "v_exp_f32 v85, v85\n"
    "v_exp_f32 v86, v86\n"
    "v_cvt_pk_bf16_f32 v170, v84, v85\n"
    "v_exp_f32 v87, v87\n"
    "v_mfma_f32_32x32x16_bf16 v[112:127], v[188:191], v[180:183], v[112:127]\n"
    "v_exp_f32 v88, v88\n"
    "v_cvt_pk_bf16_f32 v171, v86, v87\n"
    "buffer_load_dwordx4 v[188:191], v1, s[20:23], s58 offen sc1\n"
    "v_exp_f32 v89, v89\n"
    "v_exp_f32 v90, v90\n"
    "v_cvt_pk_bf16_f32 v172, v88, v89\n"
    "v_exp_f32 v91, v91\n"
    "v_exp_f32 v92, v92\n"
    "v_mfma_f32_32x32x16_bf16 v[128:143], v[228:231], v[164:167], v[128:143]\n"
    "v_cvt_pk_bf16_f32 v173, v90, v91\n"
    "v_exp_f32 v93, v93\n"
    "buffer_load_dwordx4 v[48:51], v252, s[20:23], s96 offen offset:1024 sc1\n"
    "v_exp_f32 v94, v94\n"
    "v_cvt_pk_bf16_f32 v174, v92, v93\n"
    "v_exp_f32 v95, v95\n"
    "v_exp_f32 v96, v96\n"
    "v_cvt_pk_bf16_f32 v175, v94, v95\n"
    "s_waitcnt vmcnt(12)\n"
    "v_mfma_f32_32x32x4_2b_bf16 v[64:95], v[202:203], v[184:185], 0\n"
    "v_exp_f32 v97, v97\n"
    "v_exp_f32 v98, v98\n"
    "v_cvt_pk_bf16_f32 v160, v96, v97\n"
    "v_exp_f32 v99, v99\n"
    "v_exp_f32 v100, v100\n"
    "v_cvt_pk_bf16_f32 v161, v98, v99\n"
    "v_mfma_f32_32x32x16_bf16 v[144:159], v[224:227], v[168:171], v[144:159]\n"
    "v_exp_f32 v101, v101\n"
    "v_exp_f32 v102, v102\n"
    "v_cvt_pk_bf16_f32 v162, v100, v101\n"
    "v_exp_f32 v103, v103\n"
    "v_exp_f32 v104, v104\n"
    "v_cvt_pk_bf16_f32 v163, v102, v103\n"
    "v_exp_f32 v105, v105\n"
    "v_mfma_f32_32x32x16_bf16 v[144:159], v[228:231], v[172:175], v[144:159]\n"
    "v_exp_f32 v106, v106\n"
    "v_cvt_pk_bf16_f32 v164, v104, v105\n"
    "buffer_load_dwordx4 v[224:227], v3, s[20:23], s58 offen offset:1024 sc1\n"
    "buffer_load_dwordx4 v[228:231], v3, s[20:23], s58 offen offset:1664 sc1\n"
    "v_exp_f32 v107, v107\n"
    "v_exp_f32 v108, v108\n"
    "v_cvt_pk_bf16_f32 v165, v106, v107\n"
    "v_exp_f32 v109, v109\n"
    "v_exp_f32 v110, v110\n"
    "s_waitcnt vmcnt(13)\n"
    "v_mfma_f32_32x32x16_bf16 v[64:79], v[192:195], v[176:179], v[64:79]\n"
    "v_cvt_pk_bf16_f32 v166, v108, v109\n"
    "v_exp_f32 v111, v111\n"
    "s_add_u32 s58, s58, 2432\n"
    "s_cmp_eq_u32 s58, s59\n"
    "s_cselect_b32 s41, 77824, 0\n"
    "s_sub_u32 s58, s58, s41\n"
    "buffer_load_dword v202, v4, s[20:23], s58 offen offset:2304 sc1\n"
    "v_exp_f32 v112, v112\n"
    "v_cvt_pk_bf16_f32 v167, v110, v111\n"
    "v_exp_f32 v113, v113\n"
    "v_exp_f32 v114, v114\n"
    "v_cvt_pk_bf16_f32 v168, v112, v113\n"
    "v_mfma_f32_32x32x16_bf16 v[128:143], v[232:235], v[160:163], v[128:143]\n"
    "v_exp_f32 v115, v115\n"
    "v_exp_f32 v116, v116\n"
    "v_cvt_pk_bf16_f32 v169, v114, v115\n"
    "v_exp_f32 v117, v117\n"
    "v_exp_f32 v118, v118\n"
    "v_cvt_pk_bf16_f32 v170, v116, v117\n"
    "v_exp_f32 v119, v119\n"
    "v_mfma_f32_32x32x16_bf16 v[80:95], v[192:195], v[180:183], v[80:95]\n"
    "v_exp_f32 v120, v120\n"
    "v_cvt_pk_bf16_f32 v171, v118, v119\n"
    "buffer_load_dwordx4 v[192:195], v1, s[20:23], s58 offen sc1\n"
    "v_exp_f32 v121, v121\n"
    "v_exp_f32 v122, v122\n"
    "v_cvt_pk_bf16_f32 v172, v120, v121\n"
    "v_exp_f32 v123, v123\n"
    "v_exp_f32 v124, v124\n"
    "v_mfma_f32_32x32x16_bf16 v[128:143], v[236:239], v[164:167], v[128:143]\n"
    "v_cvt_pk_bf16_f32 v173, v122, v123\n"
    "v_exp_f32 v125, v125\n"
    "buffer_load_dwordx4 v[52:55], v253, s[20:23], s96 offen offset:1024 sc1\n"
    "v_exp_f32 v126, v126\n"
    "v_cvt_pk_bf16_f32 v174, v124, v125\n"
    "v_exp_f32 v127, v127\n"
    "v_exp_f32 v64, v64\n"
    "v_cvt_pk_bf16_f32 v175, v126, v127\n"
    "s_waitcnt vmcnt(12)\n"
    "v_mfma_f32_32x32x4_2b_bf16 v[96:127], v[204:205], v[184:185], 0\n"
    "v_exp_f32 v65, v65\n"
    "v_exp_f32 v66, v66\n"
    "v_cvt_pk_bf16_f32 v160, v64, v65\n"
    "v_exp_f32 v67, v67\n"
    "v_exp_f32 v68, v68\n"
    "v_cvt_pk_bf16_f32 v161, v66, v67\n"
    "v_mfma_f32_32x32x16_bf16 v[144:159], v[232:235], v[168:171], v[144:159]\n"
    "v_exp_f32 v69, v69\n"
    "v_exp_f32 v70, v70\n"
    "v_cvt_pk_bf16_f32 v162, v68, v69\n"
    "v_exp_f32 v71, v71\n"
    "v_exp_f32 v72, v72\n"
    "v_cvt_pk_bf16_f32 v163, v70, v71\n"
    "v_exp_f32 v73, v73\n"
    "v_mfma_f32_32x32x16_bf16 v[144:159], v[236:239], v[172:175], v[144:159]\n"
    "v_exp_f32 v74, v74\n"
    "v_cvt_pk_bf16_f32 v164, v72, v73\n"
    "buffer_load_dwordx4 v[232:235], v3, s[20:23], s58 offen offset:1024 sc1\n"
    "buffer_load_dwordx4 v[236:239], v3, s[20:23], s58 offen offset:1664 sc1\n"
    "v_exp_f32 v75, v75\n"
    "v_exp_f32 v76, v76\n"
    "v_cvt_pk_bf16_f32 v165, v74, v75\n"
    "v_exp_f32 v77, v77\n"
    "v_exp_f32 v78, v78\n"
    "s_waitcnt vmcnt(13)\n"
    "v_mfma_f32_32x32x16_bf16 v[96:111], v[196:199], v[176:179], v[96:111]\n"
    "v_cvt_pk_bf16_f32 v166, v76, v77\n"
    "v_exp_f32 v79, v79\n"
    "v_exp_f32 v80, v80\n"
    "v_cvt_pk_bf16_f32 v167, v78, v79\n"
    "v_exp_f32 v81, v81\n"
    "v_exp_f32 v82, v82\n"
    "v_cvt_pk_bf16_f32 v168, v80, v81\n"
    "v_mfma_f32_32x32x16_bf16 v[128:143], v[208:211], v[160:163], v[128:143]\n"
    "v_exp_f32 v83, v83\n"
    "v_exp_f32 v84, v84\n"
    "v_cvt_pk_bf16_f32 v169, v82, v83\n"
    "v_exp_f32 v85, v85\n"
    "v_exp_f32 v86, v86\n"
    "v_cvt_pk_bf16_f32 v170, v84, v85\n"
    "v_exp_f32 v87, v87\n"
    "v_mfma_f32_32x32x16_bf16 v[112:127], v[196:199], v[180:183], v[112:127]\n"
    "v_exp_f32 v88, v88\n"
    "v_cvt_pk_bf16_f32 v171, v86, v87\n"
    "v_exp_f32 v89, v89\n"
    "v_exp_f32 v90, v90\n"
    "v_cvt_pk_bf16_f32 v172, v88, v89\n"
    "v_exp_f32 v91, v91\n"
    "v_exp_f32 v92, v92\n"
    "v_mfma_f32_32x32x16_bf16 v[128:143], v[212:215], v[164:167], v[128:143]\n"
    "v_cvt_pk_bf16_f32 v173, v90, v91\n"
    "v_exp_f32 v93, v93\n"
    "buffer_load_dwordx4 v[56:59], v254, s[20:23], s96 offen offset:1024 sc1\n"
    "v_exp_f32 v94, v94\n"
    "v_cvt_pk_bf16_f32 v174, v92, v93\n"
    "v_exp_f32 v95, v95\n"
    "v_exp_f32 v96, v96\n"
    "v_cvt_pk_bf16_f32 v175, v94, v95\n"
    "s_waitcnt vmcnt(10)\n"
    "v_mfma_f32_32x32x4_2b_bf16 v[64:95], v[200:201], v[184:185], 0\n"
    "v_exp_f32 v97, v97\n"
    "v_exp_f32 v98, v98\n"
    "v_cvt_pk_bf16_f32 v160, v96, v97\n"
    "v_exp_f32 v99, v99\n"
    "v_exp_f32 v100, v100\n"
    "v_cvt_pk_bf16_f32 v161, v98, v99\n"
    "v_mfma_f32_32x32x16_bf16 v[144:159], v[208:211], v[168:171], v[144:159]\n"
    "v_exp_f32 v101, v101\n"
    "v_exp_f32 v102, v102\n"
    "v_cvt_pk_bf16_f32 v162, v100, v101\n"
    "v_exp_f32 v103, v103\n"
    "v_exp_f32 v104, v104\n"
    "v_cvt_pk_bf16_f32 v163, v102, v103\n"
    "v_exp_f32 v105, v105\n"
    "v_mfma_f32_32x32x16_bf16 v[144:159], v[212:215], v[172:175], v[144:159]\n"
    "v_exp_f32 v106, v106\n"
    "v_cvt_pk_bf16_f32 v164, v104, v105\n"
    "v_exp_f32 v107, v107\n"
    "v_exp_f32 v108, v108\n"
    "v_cvt_pk_bf16_f32 v165, v106, v107\n"
    "v_exp_f32 v109, v109\n"
    "v_exp_f32 v110, v110\n"
    "s_waitcnt vmcnt(9)\n"
    "v_mfma_f32_32x32x16_bf16 v[64:79], v[188:191], v[176:179], v[64:79]\n"
    "v_cvt_pk_bf16_f32 v166, v108, v109\n"
    "v_exp_f32 v111, v111\n"
    "v_exp_f32 v112, v112\n"
    "v_cvt_pk_bf16_f32 v167, v110, v111\n"
    "v_exp_f32 v113, v113\n"
    "v_exp_f32 v114, v114\n"
    "v_cvt_pk_bf16_f32 v168, v112, v113\n"
    "v_mfma_f32_32x32x16_bf16 v[128:143], v[216:219], v[160:163], v[128:143]\n"
    "v_exp_f32 v115, v115\n"
    "v_exp_f32 v116, v116\n"
    "v_cvt_pk_bf16_f32 v169, v114, v115\n"
    "v_exp_f32 v117, v117\n"
    "v_exp_f32 v118, v118\n"
    "v_cvt_pk_bf16_f32 v170, v116, v117\n"
    "v_exp_f32 v119, v119\n"
    "v_mfma_f32_32x32x16_bf16 v[80:95], v[188:191], v[180:183], v[80:95]\n"
    "v_exp_f32 v120, v120\n"
    "v_cvt_pk_bf16_f32 v171, v118, v119\n"
    "v_exp_f32 v121, v121\n"
    "v_exp_f32 v122, v122\n"
    "v_cvt_pk_bf16_f32 v172, v120, v121\n"
    "v_exp_f32 v123, v123\n"
    "v_exp_f32 v124, v124\n"
    "v_mfma_f32_32x32x16_bf16 v[128:143], v[220:223], v[164:167], v[128:143]\n"
    "v_cvt_pk_bf16_f32 v173, v122, v123\n"
    "v_exp_f32 v125, v125\n"
    "buffer_load_dwordx4 v[60:63], v1, s[20:23], s96 offen offset:2048 sc1\n"
    "v_exp_f32 v126, v126\n"
    "v_cvt_pk_bf16_f32 v174, v124, v125\n"
    "v_exp_f32 v127, v127\n"
    "v_exp_f32 v64, v64\n"
    "v_cvt_pk_bf16_f32 v175, v126, v127\n"
    "s_waitcnt vmcnt(6)\n"
    "v_mfma_f32_32x32x4_2b_bf16 v[96:127], v[202:203], v[184:185], 0\n"
    "v_exp_f32 v65, v65\n"
    "v_exp_f32 v66, v66\n"
    "v_cvt_pk_bf16_f32 v160, v64, v65\n"
    "v_exp_f32 v67, v67\n"
    "v_exp_f32 v68, v68\n"
    "v_cvt_pk_bf16_f32 v161, v66, v67\n"
    "v_mfma_f32_32x32x16_bf16 v[144:159], v[216:219], v[168:171], v[144:159]\n"
    "v_exp_f32 v69, v69\n"
    "v_exp_f32 v70, v70\n"
    "v_cvt_pk_bf16_f32 v162, v68, v69\n"
    "v_exp_f32 v71, v71\n"
    "v_exp_f32 v72, v72\n"
    "v_cvt_pk_bf16_f32 v163, v70, v71\n"
    "v_exp_f32 v73, v73\n"
    "v_mfma_f32_32x32x16_bf16 v[144:159], v[220:223], v[172:175], v[144:159]\n"
    "v_exp_f32 v74, v74\n"
    "v_cvt_pk_bf16_f32 v164, v72, v73\n"
    "v_exp_f32 v75, v75\n"
    "v_exp_f32 v76, v76\n"
    "v_cvt_pk_bf16_f32 v165, v74, v75\n"
    "v_exp_f32 v77, v77\n"
    "v_exp_f32 v78, v78\n"
    "s_waitcnt vmcnt(5)\n"
    "v_mfma_f32_32x32x16_bf16 v[96:111], v[192:195], v[176:179], v[96:111]\n"
    "v_cvt_pk_bf16_f32 v166, v76, v77\n"
    "v_exp_f32 v79, v79\n"
    "v_exp_f32 v80, v80\n"
    "v_cvt_pk_bf16_f32 v167, v78, v79\n"
    "v_exp_f32 v81, v81\n"
    "v_exp_f32 v82, v82\n"
    "v_cvt_pk_bf16_f32 v168, v80, v81\n"
    "v_mfma_f32_32x32x16_bf16 v[128:143], v[224:227], v[160:163], v[128:143]\n"
    "v_exp_f32 v83, v83\n"
    "v_exp_f32 v84, v84\n"
    "v_cvt_pk_bf16_f32 v169, v82, v83\n"
    "v_exp_f32 v85, v85\n"
    "v_exp_f32 v86, v86\n"
    "v_cvt_pk_bf16_f32 v170, v84, v85\n"
    "v_exp_f32 v87, v87\n"
    "v_mfma_f32_32x32x16_bf16 v[112:127], v[192:195], v[180:183], v[112:127]\n"
    "v_exp_f32 v88, v88\n"
    "v_cvt_pk_bf16_f32 v171, v86, v87\n"
    "v_exp_f32 v89, v89\n"
    "v_exp_f32 v90, v90\n"
    "v_cvt_pk_bf16_f32 v172, v88, v89\n"
    "v_exp_f32 v91, v91\n"
    "v_exp_f32 v92, v92\n"
    "v_mfma_f32_32x32x16_bf16 v[128:143], v[228:231], v[164:167], v[128:143]\n"
    "v_cvt_pk_bf16_f32 v173, v90, v91\n"
    "v_exp_f32 v93, v93\n"
    "v_exp_f32 v94, v94\n"
    "v_cvt_pk_bf16_f32 v174, v92, v93\n"
    "v_exp_f32 v95, v95\n"
    "v_exp_f32 v96, v96\n"
    "v_cvt_pk_bf16_f32 v175, v94, v95\n"
    "v_exp_f32 v97, v97\n"
    "v_exp_f32 v98, v98\n"
    "v_cvt_pk_bf16_f32 v160, v96, v97\n"
    "v_exp_f32 v99, v99\n"
    "v_exp_f32 v100, v100\n"
    "v_cvt_pk_bf16_f32 v161, v98, v99\n"
    "v_mfma_f32_32x32x16_bf16 v[144:159], v[224:227], v[168:171], v[144:159]\n"
    "v_exp_f32 v101, v101\n"
    "v_exp_f32 v102, v102\n"
    "v_cvt_pk_bf16_f32 v162, v100, v101\n"
    "v_exp_f32 v103, v103\n"
    "v_exp_f32 v104, v104\n"
    "v_cvt_pk_bf16_f32 v163, v102, v103\n"
    "v_exp_f32 v105, v105\n"
    "v_mfma_f32_32x32x16_bf16 v[144:159], v[228:231], v[172:175], v[144:159]\n"
    "v_exp_f32 v106, v106\n"
    "v_cvt_pk_bf16_f32 v164, v104, v105\n"
    "v_exp_f32 v107, v107\n"
    "v_exp_f32 v108, v108\n"
    "v_cvt_pk_bf16_f32 v165, v106, v107\n"
    "v_exp_f32 v109, v109\n"
    "v_exp_f32 v110, v110\n"
    "v_cvt_pk_bf16_f32 v166, v108, v109\n"
    "v_exp_f32 v111, v111\n"
    "v_exp_f32 v112, v112\n"
    "v_cvt_pk_bf16_f32 v167, v110, v111\n"
    "v_exp_f32 v113, v113\n"
    "v_exp_f32 v114, v114\n"
    "v_cvt_pk_bf16_f32 v168, v112, v113\n"
    "s_waitcnt vmcnt(3)\n"
    "v_mfma_f32_32x32x16_bf16 v[128:143], v[232:235], v[160:163], v[128:143]\n"
    "v_exp_f32 v115, v115\n"
    "v_exp_f32 v116, v116\n"
    "v_cvt_pk_bf16_f32 v169, v114, v115\n"
    "v_exp_f32 v117, v117\n"
    "v_exp_f32 v118, v118\n"
    "v_cvt_pk_bf16_f32 v170, v116, v117\n"
    "v_exp_f32 v119, v119\n"
    "v_exp_f32 v120, v120\n"
    "v_cvt_pk_bf16_f32 v171, v118, v119\n"
    "v_exp_f32 v121, v121\n"
    "v_exp_f32 v122, v122\n"
    "v_cvt_pk_bf16_f32 v172, v120, v121\n"
    "v_exp_f32 v123, v123\n"
    "v_exp_f32 v124, v124\n"
    "s_waitcnt vmcnt(2)\n"
    "v_mfma_f32_32x32x16_bf16 v[128:143], v[236:239], v[164:167], v[128:143]\n"
    "v_cvt_pk_bf16_f32 v173, v122, v123\n"
    "v_exp_f32 v125, v125\n"
    "v_exp_f32 v126, v126\n"
    "v_cvt_pk_bf16_f32 v174, v124, v125\n"
    "v_exp_f32 v127, v127\n"
    "s_nop 0\n"
    "v_cvt_pk_bf16_f32 v175, v126, v127\n"
    "v_mfma_f32_32x32x16_bf16 v[144:159], v[232:235], v[168:171], v[144:159]\n"
    "s_nop 0\n"
    "v_mfma_f32_32x32x16_bf16 v[144:159], v[236:239], v[172:175], v[144:159]\n"
    "s_mul_i32 s48, s36, 9216\n"
    "s_add_u32 s48, s48, 92160\n"
    "s_mul_i32 s50, s36, 3072\n"
    "s_add_u32 s50, s50, 132096\n"
    "s_cmp_gt_u32 s36, 4\n"
    "s_cselect_b32 s48, s50, s48\n"
    "s_xor_b32 s44, s36, 4\n"
    "s_mul_i32 s49, s44, 9216\n"
    "s_add_u32 s49, s49, 92160\n"
    "s_mul_i32 s50, s44, 3072\n"
    "s_add_u32 s50, s50, 132096\n"
    "s_cmp_gt_u32 s44, 4\n"
    "s_cselect_b32 s49, s50, s49\n"
    "v_add_u32 v13, s48, v1\n"
    "v_add_u32 v14, s49, v1\n"
    "v_mul_f32 v20, 0x4d000000, v138\n"
    "v_add_f32 v20, abs(v20), abs(v128)\n"
    "v_add_f32 v20, v20, abs(v129)\n"
    "v_add_f32 v20, v20, abs(v130)\n"
    "v_add_f32 v20, v20, abs(v131)\n"
    "v_add_f32 v20, v20, abs(v132)\n"
    "v_add_f32 v20, v20, abs(v133)\n"
    "v_add_f32 v20, v20, abs(v134)\n"
    "v_add_f32 v20, v20, abs(v135)\n"
    "v_add_f32 v20, v20, abs(v136)\n"
    "v_add_f32 v20, v20, abs(v137)\n"
    "v_mul_f32 v21, 0x4d000000, v154\n"
    "v_add_f32 v21, abs(v21), abs(v144)\n"
    "v_add_f32 v21, v21, abs(v145)\n"
    "v_add_f32 v21, v21, abs(v146)\n"
    "v_add_f32 v21, v21, abs(v147)\n"
    "v_add_f32 v21, v21, abs(v148)\n"
    "v_add_f32 v21, v21, abs(v149)\n"
    "v_add_f32 v21, v21, abs(v150)\n"
    "v_add_f32 v21, v21, abs(v151)\n"
    "v_add_f32 v21, v21, abs(v152)\n"
    "v_add_f32 v21, v21, abs(v153)\n"
    "v_add_f32 v20, v20, v21\n"
    "v_mov_b32 v23, 0x7e800000\n"
    "v_cmp_nlt_f32 vcc, v20, v23\n"
    "s_or_b64 vcc, vcc, s[56:57]\n"
    "s_cmp_lg_u64 vcc, 0\n"
    "s_cselect_b32 s44, 1, 0\n"
    "v_mov_b32 v15, s44\n"
    "s_lshl_b32 s44, s36, 2\n"
    "s_add_u32 s44, s44, 161280\n"
    "v_mov_b32 v16, s44\n"
    "ds_write_b32 v16, v15\n"
    ".Lk_cmb_63:\n"
    "s_nop 7\n"
    "ds_write_b128 v13, v[144:147]\n"
    "ds_write_b128 v13, v[148:151] offset:1024\n"
    "ds_write_b128 v13, v[152:155] offset:2048\n"
    "v_and_b32 v17, 31, v2\n"
    "v_lshrrev_b32 v18, 4, v17\n"
    "v_and_b32 v17, 15, v17\n"
    "v_mul_u32_u24 v18, 18432, v18\n"
    "v_lshl_add_u32 v17, v17, 4, v18\n"
    "v_lshrrev_b32 v18, 5, v2\n"
    "v_lshl_add_u32 v18, v18, 8, v17\n"
    "v_cmp_gt_u32 vcc, 32, v2\n"
    "s_nop 1\n"
    "v_cndmask_b32 v17, v11, v17, vcc\n"
    "s_lshl_b32 s44, s38, 1\n"
    "s_add_u32 s44, s44, s40\n"
    "s_mul_i32 s44, s44, 36864\n"
    "s_add_u32 s44, s44, 7598080\n"
    "s_lshl_b32 s45, s37, 2\n"
    "s_add_u32 s45, s45, s39\n"
    "s_lshl_b32 s45, s45, 9\n"
    "s_add_u32 s45, s45, s44\n"
    "s_lshl_b32 s46, s37, 8\n"
    "s_lshl_b32 s47, s39, 2\n"
    "s_add_u32 s46, s46, s47\n"
    "s_add_u32 s46, s46, 16384\n"
    "s_add_u32 s46, s46, s44\n"
    "v_and_b32 v15, 7, v2\n"
    "v_lshlrev_b32 v15, 2, v15\n"
    "v_add_u32 v15, 161280, v15\n"
    "s_waitcnt lgkmcnt(0)\n"
    "s_barrier\n"
    "ds_read_b32 v16, v15\n"
    "ds_read_b128 v[64:67], v14\n"
    "ds_read_b128 v[68:71], v14 offset:1024\n"
    "ds_read_b128 v[72:75], v14 offset:2048\n"
    "s_waitcnt lgkmcnt(3)\n"
    "v_cmp_ne_u32 vcc, 0, v16\n"
    "s_cbranch_vccnz .Lk_fb_64\n"
    "s_waitcnt lgkmcnt(0)\n"
    "v_add_f32 v138, v138, v74\n"
    "v_mov_b32 v20, v138\n"
    "v_add_f32 v128, v128, v64\n"
    "v_add_f32 v129, v129, v65\n"
    "v_add_f32 v130, v130, v66\n"
    "v_permlane32_swap_b32 v138, v20\n"
    "v_add_f32 v131, v131, v67\n"
    "v_add_f32 v132, v132, v68\n"
    "v_add_f32 v133, v133, v69\n"
    "v_rcp_f32 v20, v138\n"
    "v_add_f32 v134, v134, v70\n"
    "v_add_f32 v135, v135, v71\n"
    "v_add_f32 v136, v136, v72\n"
    "v_add_f32 v137, v137, v73\n"
    "v_mul_f32 v128, v128, v20\n"
    "v_mul_f32 v129, v129, v20\n"
    "v_mul_f32 v130, v130, v20\n"
    "v_mul_f32 v131, v131, v20\n"
    "v_mul_f32 v132, v132, v20\n"
    "v_mul_f32 v133, v133, v20\n"
    "v_mul_f32 v134, v134, v20\n"
    "v_mul_f32 v135, v135, v20\n"
    "v_mul_f32 v136, v136, v20\n"
    "v_mul_f32 v137, v137, v20\n"
    "v_cvt_pk_bf16_f32 v160, v128, v129\n"
    "v_cvt_pk_bf16_f32 v161, v130, v131\n"
    "v_cvt_pk_bf16_f32 v162, v132, v133\n"
    "v_cvt_pk_bf16_f32 v163, v134, v135\n"
    "v_cvt_pk_bf16_f32 v164, v136, v137\n"
    "buffer_store_dwordx4 v[160:163], v18, s[20:23], s45 offen sc1\n"
    "buffer_store_dword v164, v17, s[20:23], s46 offen sc1\n"
    "s_mul_i32 s47, s40, 46080\n"
    "v_add_u32 v26, s47, v1\n"
    "s_lshl_b32 s47, s40, 11\n"
    "v_add_u32 v24, s47, v26\n"
    "v_subrev_u32 v25, s47, v26\n"
    "s_waitcnt vmcnt(0)\n"
    "s_barrier\n"
    "s_cmp_lg_u32 s36, 0\n"
    "s_cbranch_scc1 .Lk_pub_67\n"
    "buffer_store_dword v9, v5, s[20:23], s98 offen sc1\n"
    "buffer_load_dword v8, v10, s[20:23], s99 offen sc1\n"
    ".Lk_pub_67:\n"
    "v_lshlrev_b32 v12, 16, v240\n"
    "v_and_b32 v13, 0xffff0000, v240\n"
    "v_lshlrev_b32 v14, 16, v241\n"
    "v_and_b32 v15, 0xffff0000, v241\n"
    "v_lshlrev_b32 v16, 16, v242\n"
    "v_and_b32 v17, 0xffff0000, v242\n"
    "v_lshlrev_b32 v18, 16, v243\n"
    "v_and_b32 v19, 0xffff0000, v243\n"
    "v_lshlrev_b32 v20, 16, v244\n"
    "v_and_b32 v21, 0xffff0000, v244\n"
    "v_lshlrev_b32 v22, 16, v245\n"
    "v_and_b32 v23, 0xffff0000, v245\n"
    "buffer_load_dwordx4 v[224:227], v252, s[20:23], s96 offen offset:2048 sc1\n"
    "buffer_load_dwordx4 v[228:231], v253, s[20:23], s96 offen offset:2048 sc1\n"
    "buffer_load_dwordx4 v[232:235], v254, s[20:23], s96 offen offset:2048 sc1\n"
    "ds_read_b128 v[100:103], v24\n"
    "ds_read_b128 v[104:107], v24 offset:1024\n"
    "ds_read_b128 v[108:111], v25 offset:2048\n"
    "ds_read_b128 v[112:115], v25 offset:3072\n"
    "ds_read_b128 v[116:119], v26 offset:4096\n"
    "ds_read_b128 v[120:123], v24 offset:5120\n"
    "ds_read_b128 v[124:127], v24 offset:6144\n"
    "ds_read_b128 v[128:131], v25 offset:7168\n"
    "ds_read_b128 v[132:135], v25 offset:8192\n"
    "ds_read_b128 v[136:139], v26 offset:9216\n"
    "s_mul_i32 s47, s36, 3072\n"
    "s_add_u32 s47, s47, 92160\n"
    "v_add_u32 v27, s47, v1\n"
    "s_xor_b32 s47, s36, 4\n"
    "s_mul_i32 s47, s47, 3072\n"
    "s_add_u32 s47, s47, 92160\n"
    "v_add_u32 v192, s47, v1\n"
    "v_and_b32 v176, 15, v2\n"
    "v_lshrrev_b32 v177, 4, v2\n"
    "v_lshlrev_b32 v178, 4, v176\n"
    "v_cmp_gt_u32 vcc, 16, v2\n"
    "s_nop 1\n"
    "v_cndmask_b32 v191, v11, v178, vcc\n"
    "s_cmp_eq_u32 s40, 0\n"
    "s_cselect_b32 s47, 16, 0\n"
    "v_cmp_gt_u32 vcc, s47, v2\n"
    "s_nop 1\n"
    "v_cndmask_b32 v194, v11, v178, vcc\n"
    "s_lshl_b32 s47, s39, 8\n"
    "s_add_u32 s47, s47, 155648\n"
    "v_add_u32 v179, s47, v178\n"
    "v_add_u32 v180, 162816, v1\n"
    "v_cndmask_b32 v190, v180, v179, vcc\n"
    "s_mul_i32 s47, s36, 1024\n"
    "s_add_u32 s47, s47, 147456\n"
    "v_add_u32 v189, s47, v1\n"
    "s_lshl_b32 s47, s40, 7\n"
    "s_add_u32 s47, s47, 161792\n"
    "v_lshlrev_b32 v179, 5, v177\n"
    "v_add_u32 v195, s47, v179\n"
    "v_and_b32 v179, 1, v177\n"
    "v_lshlrev_b32 v179, 4, v179\n"
    "v_add_u32 v188, 162048, v179\n"
    "s_lshl_b32 s48, s15, 2\n"
    "s_add_u32 s48, s48, s39\n"
    "s_mul_i32 s44, s48, 18432\n"
    "s_add_u32 s44, s44, 7598080\n"
    "s_mul_i32 s47, s40, 9216\n"
    "s_add_u32 s44, s44, s47\n"
    "s_mul_i32 s45, s48, 18432\n"
    "s_add_u32 s45, s45, 10694656\n"
    "s_mul_i32 s47, s14, 2304\n"
    "s_add_u32 s47, s47, s45\n"
    "s_add_u32 s49, s47, 2048\n"
    "s_mul_i32 s45, s40, 1024\n"
    "s_add_u32 s45, s45, s47\n"
    "s_mul_i32 s46, s48, 2048\n"
    "s_add_u32 s46, s46, 13053952\n"
    "s_mul_i32 s47, s14, 256\n"
    "s_add_u32 s47, s47, s46\n"
    "s_mul_i32 s46, s40, 8\n"
    "s_add_u32 s46, s46, s47\n"
    "s_cmp_lg_u32 s36, 0\n"
    "s_cbranch_scc1 .Lk_polld_69\n"
    "s_mov_b32 s60, 0x8000\n"
    ".Lk_poll_68:\n"
    "s_waitcnt vmcnt(0)\n"
    "v_cmp_ne_u32 vcc, s95, v8\n"
    "s_cbranch_vccz .Lk_polld_69\n"
    "s_sub_u32 s60, s60, 1\n"
    "s_cbranch_scc1 .Lk_polld_69\n"
    "s_sleep 1\n"
    "buffer_load_dword v8, v10, s[20:23], s99 offen sc1\n"
    "s_branch .Lk_poll_68\n"
    ".Lk_polld_69:\n"
    "s_barrier\n"
    "buffer_load_dwordx4 v[64:67], v1, s[20:23], s44 offen sc1\n"
    "buffer_load_dwordx4 v[68:71], v1, s[20:23], s44 offen offset:1024 sc1\n"
    "buffer_load_dwordx4 v[72:75], v1, s[20:23], s44 offen offset:2048 sc1\n"
    "buffer_load_dwordx4 v[76:79], v1, s[20:23], s44 offen offset:3072 sc1\n"
    "s_add_u32 s44, s44, 4096\n"
    "buffer_load_dwordx4 v[80:83], v1, s[20:23], s44 offen sc1\n"
    "buffer_load_dwordx4 v[84:87], v1, s[20:23], s44 offen offset:1024 sc1\n"
    "buffer_load_dwordx4 v[88:91], v1, s[20:23], s44 offen offset:2048 sc1\n"
    "buffer_load_dwordx4 v[92:95], v1, s[20:23], s44 offen offset:3072 sc1\n"
    "s_add_u32 s44, s44, 4096\n"
    "buffer_load_dwordx4 v[96:99], v1, s[20:23], s44 offen sc1\n"
    "s_mul_i32 s98, s15, 1024\n"
    "s_add_u32 s98, s98, 98304\n"
    "s_lshl_b32 s47, s14, 2\n"
    "s_add_u32 s98, s98, s47\n"
    "s_mul_i32 s99, s38, 1024\n"
    "s_add_u32 s99, s99, 98304\n"
    "s_lshl_b32 s47, s37, 7\n"
    "s_add_u32 s99, s99, s47\n"
    "s_waitcnt vmcnt(8) lgkmcnt(9)\n"
    "v_mfma_f32_16x16x32_bf16 v[140:143], v[100:103], v[64:67], 0\n"
    "ds_read_b128 v[100:103], v24 offset:10240\n"
    "s_waitcnt lgkmcnt(9)\n"
    "v_mfma_f32_16x16x32_bf16 v[144:147], v[104:107], v[64:67], 0\n"
    "ds_read_b128 v[104:107], v24 offset:11264\n"
    "s_waitcnt lgkmcnt(9)\n"
    "v_mfma_f32_16x16x32_bf16 v[148:151], v[108:111], v[64:67], 0\n"
    "ds_read_b128 v[108:111], v25 offset:12288\n"
    "s_waitcnt lgkmcnt(9)\n"
    "v_mfma_f32_16x16x32_bf16 v[152:155], v[112:115], v[64:67], 0\n"
    "ds_read_b128 v[112:115], v25 offset:13312\n"
    "s_waitcnt lgkmcnt(9)\n"
    "v_mfma_f32_16x16x32_bf16 v[156:159], v[116:119], v[64:67], 0\n"
    "ds_read_b128 v[116:119], v26 offset:14336\n"
    "s_waitcnt vmcnt(7) lgkmcnt(9)\n"
    "v_mfma_f32_16x16x32_bf16 v[140:143], v[120:123], v[68:71], v[140:143]\n"
    "ds_read_b128 v[120:123], v24 offset:15360\n"
    "s_waitcnt lgkmcnt(9)\n"
    "v_mfma_f32_16x16x32_bf16 v[144:147], v[124:127], v[68:71], v[144:147]\n"
    "ds_read_b128 v[124:127], v24 offset:16384\n"
    "s_waitcnt lgkmcnt(9)\n"
    "v_mfma_f32_16x16x32_bf16 v[148:151], v[128:131], v[68:71], v[148:151]\n"
    "ds_read_b128 v[128:131], v25 offset:17408\n"
    "s_waitcnt lgkmcnt(9)\n"
    "v_mfma_f32_16x16x32_bf16 v[152:155], v[132:135], v[68:71], v[152:155]\n"
    "ds_read_b128 v[132:135], v25 offset:18432\n"
    "s_waitcnt lgkmcnt(9)\n"
    "v_mfma_f32_16x16x32_bf16 v[156:159], v[136:139], v[68:71], v[156:159]\n"
    "ds_read_b128 v[136:139], v26 offset:19456\n"
    "s_waitcnt vmcnt(6) lgkmcnt(9)\n"
    "v_mfma_f32_16x16x32_bf16 v[140:143], v[100:103], v[72:75], v[140:143]\n"
    "ds_read_b128 v[100:103], v24 offset:20480\n"
    "s_waitcnt lgkmcnt(9)\n"
    "v_mfma_f32_16x16x32_bf16 v[144:147], v[104:107], v[72:75], v[144:147]\n"
    "ds_read_b128 v[104:107], v24 offset:21504\n"
    "s_waitcnt lgkmcnt(9)\n"
    "v_mfma_f32_16x16x32_bf16 v[148:151], v[108:111], v[72:75], v[148:151]\n"
    "ds_read_b128 v[108:111], v25 offset:22528\n"
    "s_waitcnt lgkmcnt(9)\n"
    "v_mfma_f32_16x16x32_bf16 v[152:155], v[112:115], v[72:75], v[152:155]\n"
    "ds_read_b128 v[112:115], v25 offset:23552\n"
    "s_waitcnt lgkmcnt(9)\n"
    "v_mfma_f32_16x16x32_bf16 v[156:159], v[116:119], v[72:75], v[156:159]\n"
    "ds_read_b128 v[116:119], v26 offset:24576\n"
    "s_waitcnt vmcnt(5) lgkmcnt(9)\n"
    "v_mfma_f32_16x16x32_bf16 v[140:143], v[120:123], v[76:79], v[140:143]\n"
    "ds_read_b128 v[120:123], v24 offset:25600\n"
    "s_waitcnt lgkmcnt(9)\n"
    "v_mfma_f32_16x16x32_bf16 v[144:147], v[124:127], v[76:79], v[144:147]\n"
    "ds_read_b128 v[124:127], v24 offset:26624\n"
    "s_waitcnt lgkmcnt(9)\n"
    "v_mfma_f32_16x16x32_bf16 v[148:151], v[128:131], v[76:79], v[148:151]\n"
    "ds_read_b128 v[128:131], v25 offset:27648\n"
    "s_waitcnt lgkmcnt(9)\n"
    "v_mfma_f32_16x16x32_bf16 v[152:155], v[132:135], v[76:79], v[152:155]\n"
    "ds_read_b128 v[132:135], v25 offset:28672\n"
    "s_waitcnt lgkmcnt(9)\n"
    "v_mfma_f32_16x16x32_bf16 v[156:159], v[136:139], v[76:79], v[156:159]\n"
    "ds_read_b128 v[136:139], v26 offset:29696\n"
    "s_waitcnt vmcnt(4) lgkmcnt(9)\n"
    "v_mfma_f32_16x16x32_bf16 v[140:143], v[100:103], v[80:83], v[140:143]\n"
    "ds_read_b128 v[100:103], v24 offset:30720\n"
    "s_waitcnt lgkmcnt(9)\n"
    "v_mfma_f32_16x16x32_bf16 v[144:147], v[104:107], v[80:83], v[144:147]\n"
    "ds_read_b128 v[104:107], v24 offset:31744\n"
    "s_waitcnt lgkmcnt(9)\n"
    "v_mfma_f32_16x16x32_bf16 v[148:151], v[108:111], v[80:83], v[148:151]\n"
    "ds_read_b128 v[108:111], v25 offset:32768\n"
    "s_waitcnt lgkmcnt(9)\n"
    "v_mfma_f32_16x16x32_bf16 v[152:155], v[112:115], v[80:83], v[152:155]\n"
    "ds_read_b128 v[112:115], v25 offset:33792\n"
    "s_waitcnt lgkmcnt(9)\n"
    "v_mfma_f32_16x16x32_bf16 v[156:159], v[116:119], v[80:83], v[156:159]\n"
    "ds_read_b128 v[116:119], v26 offset:34816\n"
    "s_waitcnt vmcnt(3) lgkmcnt(9)\n"
    "v_mfma_f32_16x16x32_bf16 v[140:143], v[120:123], v[84:87], v[140:143]\n"
    "ds_read_b128 v[120:123], v24 offset:35840\n"
    "s_waitcnt lgkmcnt(9)\n"
    "v_mfma_f32_16x16x32_bf16 v[144:147], v[124:127], v[84:87], v[144:147]\n"
    "ds_read_b128 v[124:127], v24 offset:36864\n"
    "s_waitcnt lgkmcnt(9)\n"
    "v_mfma_f32_16x16x32_bf16 v[148:151], v[128:131], v[84:87], v[148:151]\n"
    "ds_read_b128 v[128:131], v25 offset:37888\n"
    "s_waitcnt lgkmcnt(9)\n"
    "v_mfma_f32_16x16x32_bf16 v[152:155], v[132:135], v[84:87], v[152:155]\n"
    "ds_read_b128 v[132:135], v25 offset:38912\n"
    "s_waitcnt lgkmcnt(9)\n"
    "v_mfma_f32_16x16x32_bf16 v[156:159], v[136:139], v[84:87], v[156:159]\n"
    "ds_read_b128 v[136:139], v26 offset:39936\n"
    "s_waitcnt vmcnt(2) lgkmcnt(9)\n"
    "v_mfma_f32_16x16x32_bf16 v[140:143], v[100:103], v[88:91], v[140:143]\n"
    "ds_read_b128 v[100:103], v24 offset:40960\n"
    "s_waitcnt lgkmcnt(9)\n"
    "v_mfma_f32_16x16x32_bf16 v[144:147], v[104:107], v[88:91], v[144:147]\n"
    "ds_read_b128 v[104:107], v24 offset:41984\n"
    "s_waitcnt lgkmcnt(9)\n"
    "v_mfma_f32_16x16x32_bf16 v[148:151], v[108:111], v[88:91], v[148:151]\n"
    "ds_read_b128 v[108:111], v25 offset:43008\n"
    "s_waitcnt lgkmcnt(9)\n"
    "v_mfma_f32_16x16x32_bf16 v[152:155], v[112:115], v[88:91], v[152:155]\n"
    "ds_read_b128 v[112:115], v25 offset:44032\n"
    "s_waitcnt lgkmcnt(9)\n"
    "v_mfma_f32_16x16x32_bf16 v[156:159], v[116:119], v[88:91], v[156:159]\n"
    "ds_read_b128 v[116:119], v26 offset:45056\n"
    "s_waitcnt vmcnt(1) lgkmcnt(9)\n"
    "v_mfma_f32_16x16x32_bf16 v[140:143], v[120:123], v[92:95], v[140:143]\n"
    "s_waitcnt lgkmcnt(8)\n"
    "v_mfma_f32_16x16x32_bf16 v[144:147], v[124:127], v[92:95], v[144:147]\n"
    "s_waitcnt lgkmcnt(7)\n"
    "v_mfma_f32_16x16x32_bf16 v[148:151], v[128:131], v[92:95], v[148:151]\n"
    "s_waitcnt lgkmcnt(6)\n"
    "v_mfma_f32_16x16x32_bf16 v[152:155], v[132:135], v[92:95], v[152:155]\n"
    "s_waitcnt lgkmcnt(5)\n"
    "v_mfma_f32_16x16x32_bf16 v[156:159], v[136:139], v[92:95], v[156:159]\n"
    "s_waitcnt vmcnt(0) lgkmcnt(4)\n"
    "v_mfma_f32_16x16x32_bf16 v[140:143], v[100:103], v[96:99], v[140:143]\n"
    "s_waitcnt lgkmcnt(3)\n"
    "v_mfma_f32_16x16x32_bf16 v[144:147], v[104:107], v[96:99], v[144:147]\n"
    "s_waitcnt lgkmcnt(2)\n"
    "v_mfma_f32_16x16x32_bf16 v[148:151], v[108:111], v[96:99], v[148:151]\n"
    "s_waitcnt lgkmcnt(1)\n"
    "v_mfma_f32_16x16x32_bf16 v[152:155], v[112:115], v[96:99], v[152:155]\n"
    "s_waitcnt lgkmcnt(0)\n"
    "v_mfma_f32_16x16x32_bf16 v[156:159], v[116:119], v[96:99], v[156:159]\n"
    "s_nop 3\n"
    "ds_write_b128 v27, v[148:151]\n"
    "s_nop 0\n"
    "ds_write_b128 v27, v[152:155] offset:1024\n"
    "s_nop 0\n"
    "ds_write_b128 v27, v[156:159] offset:2048\n"
    "ds_read_b128 v[100:103], v195\n"
    "ds_read_b128 v[104:107], v195 offset:16\n"
    "ds_read_b128 v[108:111], v188\n"
    "s_waitcnt lgkmcnt(0)\n"
    "s_barrier\n"
    "ds_read_b128 v[124:127], v192\n"
    "ds_read_b128 v[128:131], v192 offset:1024\n"
    "ds_read_b128 v[132:135], v192 offset:2048\n"
    "s_sub_u32 s47, s40, 1\n"
    "v_and_b32 v108, s47, v108\n"
    "v_and_b32 v109, s47, v109\n"
    "v_and_b32 v110, s47, v110\n"
    "v_and_b32 v111, s47, v111\n"
    "s_waitcnt lgkmcnt(2)\n"
    "v_pk_add_f32 v[140:141], v[140:141], v[124:125]\n"
    "v_pk_add_f32 v[142:143], v[142:143], v[126:127]\n"
    "s_waitcnt lgkmcnt(1)\n"
    "v_pk_add_f32 v[144:145], v[144:145], v[128:129]\n"
    "v_pk_add_f32 v[146:147], v[146:147], v[130:131]\n"
    "s_waitcnt lgkmcnt(0)\n"
    "v_pk_add_f32 v[156:157], v[156:157], v[132:133]\n"
    "v_pk_add_f32 v[158:159], v[158:159], v[134:135]\n"
    "v_pk_fma_f32 v[12:13], v[100:101], v[140:141], v[12:13]\n"
    "v_pk_fma_f32 v[14:15], v[102:103], v[142:143], v[14:15]\n"
    "v_pk_fma_f32 v[16:17], v[104:105], v[144:145], v[16:17]\n"
    "v_pk_fma_f32 v[18:19], v[106:107], v[146:147], v[18:19]\n"
    "v_pk_fma_f32 v[20:21], v[108:109], v[156:157], v[20:21]\n"
    "v_pk_fma_f32 v[22:23], v[110:111], v[158:159], v[22:23]\n"
    "v_cvt_pk_bf16_f32 v84, v12, v13\n"
    "v_cvt_pk_bf16_f32 v85, v14, v15\n"
    "v_cvt_pk_bf16_f32 v86, v16, v17\n"
    "v_cvt_pk_bf16_f32 v87, v18, v19\n"
    "v_cvt_pk_bf16_f32 v88, v20, v21\n"
    "v_cvt_pk_bf16_f32 v89, v22, v23\n"
    "v_pk_add_f32 v[92:93], v[12:13], v[14:15]\n"
    "v_pk_mul_f32 v[94:95], v[12:13], v[12:13]\n"
    "buffer_store_dwordx4 v[84:87], v1, s[20:23], s45 offen sc1\n"
    "v_permlane16_swap_b32 v88, v90\n"
    "v_permlane16_swap_b32 v89, v91\n"
    "v_pk_fma_f32 v[94:95], v[14:15], v[14:15], v[94:95]\n"
    "buffer_store_dwordx4 v[88:91], v194, s[20:23], s49 offen sc1\n"
    "v_pk_add_f32 v[92:93], v[92:93], v[16:17]\n"
    "v_pk_fma_f32 v[94:95], v[16:17], v[16:17], v[94:95]\n"
    "v_pk_add_f32 v[92:93], v[92:93], v[18:19]\n"
    "v_pk_fma_f32 v[94:95], v[18:19], v[18:19], v[94:95]\n"
    "v_pk_add_f32 v[92:93], v[92:93], v[20:21]\n"
    "v_pk_fma_f32 v[94:95], v[20:21], v[20:21], v[94:95]\n"
    "v_pk_add_f32 v[92:93], v[92:93], v[22:23]\n"
    "v_pk_fma_f32 v[94:95], v[22:23], v[22:23], v[94:95]\n"
    "v_add_f32 v92, v92, v93\n"
    "v_add_f32 v94, v94, v95\n"
    "s_nop 1\n"
    "v_permlane16_swap_b32 v92, v94\n"
    "v_add_f32 v92, v92, v94\n"
    "v_mov_b32 v93, v92\n"
    "s_nop 1\n"
    "v_permlane32_swap_b32 v92, v93\n"
    "v_add_f32 v92, v92, v93\n"
    "s_nop 1\n"
    "v_permlane16_swap_b32 v92, v93\n"
    "buffer_store_dwordx2 v[92:93], v191, s[20:23], s46 offen sc1\n"
    "s_cmp_eq_u32 s40, 1\n"
    "s_cbranch_scc1 .Lk_young_70\n"
    "s_waitcnt vmcnt(0)\n"
    "s_barrier\n"
    "s_cmp_lg_u32 s36, 0\n"
    "s_cbranch_scc1 .Lk_pub_72\n"
    "buffer_store_dword v9, v5, s[20:23], s98 offen sc1\n"
    "buffer_load_dword v8, v10, s[20:23], s99 offen sc1\n"
    ".Lk_pub_72:\n"
    "s_mul_i32 s47, s37, 1327104\n"
    "s_add_u32 s47, s47, 13316096\n"
    "s_mul_i32 s97, s36, 18432\n"
    "s_add_u32 s97, s97, 589824\n"
    "s_add_u32 s97, s97, s47\n"
    "buffer_load_dwordx4 v[236:239], v1, s[20:23], s97 offen sc1\n"
    "buffer_load_dwordx4 v[240:243], v1, s[20:23], s97 offen offset:1024 sc1\n"
    "buffer_load_dwordx4 v[244:247], v1, s[20:23], s97 offen offset:2048 sc1\n"
    "buffer_load_dwordx4 v[248:251], v1, s[20:23], s96 offen offset:3072 sc1\n"
    "s_add_u32 s48, s37, s36\n"
    "s_add_u32 s48, s48, 1\n"
    "s_and_b32 s48, s48, 7\n"
    "s_lshl_b32 s49, s48, 7\n"
    "v_mov_b32 v14, s49\n"
    "v_lshlrev_b32 v15, 7, v2\n"
    "v_add_u32 v15, 32, v15\n"
    "s_cmp_eq_u32 s36, 7\n"
    "s_cselect_b64 vcc, -1, 0\n"
    "s_cselect_b32 s49, 8, 1\n"
    "v_cndmask_b32 v14, v14, v15, vcc\n"
    "v_cmp_gt_u32 vcc, s49, v2\n"
    "s_nop 1\n"
    "v_cndmask_b32 v5, v11, v14, vcc\n"
    "s_cmp_eq_u32 s36, 7\n"
    "s_cselect_b32 s58, 8, s48\n"
    "s_mul_i32 s58, s58, 73728\n"
    "s_add_u32 s58, s58, 663552\n"
    "s_add_u32 s58, s58, s47\n"
    "s_cmp_eq_u32 s36, 7\n"
    "s_cselect_b32 s48, 0, s48\n"
    "s_cselect_b32 s49, 2048, 0\n"
    "s_mov_b32 s47, 256\n"
    "s_cselect_b32 s47, 73728, s47\n"
    "s_lshl_b32 s51, s38, 3\n"
    "s_add_u32 s51, s51, s48\n"
    "s_lshl_b32 s51, s51, 3\n"
    "s_add_u32 s51, s51, s37\n"
    "s_mul_i32 s51, s51, 9216\n"
    "s_add_u32 s51, s51, 27471872\n"
    "s_add_u32 s51, s51, s49\n"
    "s_lshl_b32 s48, s47, 2\n"
    "s_add_u32 s52, s51, s48\n"
    "v_and_b32 v14, 15, v2\n"
    "v_lshrrev_b32 v15, 4, v2\n"
    "v_lshlrev_b32 v14, 4, v14\n"
    "v_and_b32 v16, 1, v15\n"
    "v_lshrrev_b32 v17, 1, v15\n"
    "v_lshl_add_u32 v16, v16, 1, v17\n"
    "v_mul_u32_u24 v16, s47, v16\n"
    "v_add_u32 v3, v16, v14\n"
    "s_mul_i32 s47, s36, 8192\n"
    "s_add_u32 s47, s47, 73728\n"
    "v_add_u32 v22, s47, v1\n"
    "v_add_u32 v24, 73728, v1\n"
    "v_add_u32 v25, 139264, v1\n"
    "s_lshr_b32 s47, s36, 1\n"
    "s_lshl_b32 s47, s47, 12\n"
    "s_and_b32 s48, s36, 1\n"
    "s_mul_i32 s48, s48, 8\n"
    "s_add_u32 s48, s48, s47\n"
    "v_add_u32 v23, s48, v25\n"
    "s_mul_i32 s47, s39, 18432\n"
    "s_add_u32 s47, s47, 0\n"
    "v_add_u32 v26, s47, v1\n"
    "v_lshlrev_b32 v15, 6, v15\n"
    "v_add_u32 v27, 156672, v15\n"
    "ds_read_b128 v[208:211], v27\n"
    "ds_read_b128 v[212:215], v27 offset:16\n"
    "ds_read_b128 v[216:219], v27 offset:32\n"
    "ds_read_b128 v[220:223], v27 offset:48\n"
    "s_mul_i32 s44, s38, 73728\n"
    "s_add_u32 s44, s44, 10694656\n"
    "s_add_u32 s44, s44, s47\n"
    "s_lshl_b32 s45, s38, 2\n"
    "s_add_u32 s45, s45, s39\n"
    "s_mul_i32 s45, s45, 2048\n"
    "s_add_u32 s45, s45, 13053952\n"
    "s_cmp_lg_u32 s36, 0\n"
    "s_cbranch_scc1 .Lk_polld_74\n"
    "s_mov_b32 s60, 0x8000\n"
    ".Lk_poll_73:\n"
    "s_waitcnt vmcnt(0)\n"
    "v_cmp_ne_u32 vcc, s95, v8\n"
    "s_cbranch_vccz .Lk_polld_74\n"
    "s_sub_u32 s60, s60, 1\n"
    "s_cbranch_scc1 .Lk_polld_74\n"
    "s_sleep 1\n"
    "buffer_load_dword v8, v10, s[20:23], s99 offen sc1\n"
    "s_branch .Lk_poll_73\n"
    ".Lk_polld_74:\n"
    "s_barrier\n"
    "s_setprio 3\n"
    "buffer_load_dwordx4 v[148:151], v1, s[20:23], s45 offen sc1\n"
    "buffer_load_dwordx4 v[152:155], v1, s[20:23], s45 offen offset:1024 sc1\n"
    "buffer_load_dwordx4 v[144:147], v1, s[20:23], s44 offen sc1\n"
    "s_setprio 2\n"
    "buffer_load_dwordx4 v[176:179], v1, s[20:23], s44 offen offset:2048 sc1\n"
    "s_add_u32 s44, s44, 4096\n"
    "buffer_load_dwordx4 v[180:183], v1, s[20:23], s44 offen sc1\n"
    "s_setprio 1\n"
    "buffer_load_dwordx4 v[184:187], v1, s[20:23], s44 offen offset:2048 sc1\n"
    "s_add_u32 s44, s44, 4096\n"
    "buffer_load_dwordx4 v[188:191], v1, s[20:23], s44 offen sc1\n"
    "buffer_load_dwordx4 v[192:195], v1, s[20:23], s44 offen offset:2048 sc1\n"
    "s_setprio 0\n"
    "s_add_u32 s44, s44, 4096\n"
    "buffer_load_dwordx4 v[196:199], v1, s[20:23], s44 offen sc1\n"
    "buffer_load_dwordx4 v[200:203], v1, s[20:23], s44 offen offset:2048 sc1\n"
    "s_add_u32 s44, s44, 4096\n"
    "buffer_load_dwordx4 v[204:207], v1, s[20:23], s44 offen sc1\n"
    "s_add_u32 s98, s98, 32768\n"
    "s_add_u32 s99, s99, 32768\n"
    "s_mul_i32 s98, s38, 1024\n"
    "s_add_u32 s98, s98, 131072\n"
    "s_lshl_b32 s48, s37, 2\n"
    "s_add_u32 s98, s98, s48\n"
    "s_setprio 2\n"
    "s_waitcnt vmcnt(10)\n"
    "v_pk_add_f32 v[148:149], v[148:149], v[150:151]\n"
    "s_waitcnt vmcnt(9)\n"
    "v_pk_add_f32 v[152:153], v[152:153], v[154:155]\n"
    "v_pk_add_f32 v[148:149], v[148:149], v[152:153]\n"
    "v_mov_b32 v151, 0x3ae38e39\n"
    "s_nop 0\n"
    "v_permlane16_swap_b32 v148, v149\n"
    "v_add_f32 v148, v148, v149\n"
    "v_mov_b32 v149, v148\n"
    "s_nop 1\n"
    "v_permlane32_swap_b32 v148, v149\n"
    "v_add_f32 v148, v148, v149\n"
    "v_mov_b32 v149, v148\n"
    "s_nop 1\n"
    "v_permlane16_swap_b32 v148, v149\n"
    "v_mul_f32 v148, v151, v148\n"
    "v_mul_f32 v150, v148, v148\n"
    "v_fma_f32 v149, v149, v151, -v150\n"
    "v_max_f32 v149, 0, v149\n"
    "v_add_f32 v149, 0x3727c5ac, v149\n"
    "v_rsq_f32 v12, v149\n"
    "s_nop 0\n"
    "v_mul_f32 v13, -v148, v12\n"
    "s_waitcnt vmcnt(8)\n"
    "v_lshlrev_b32 v14, 16, v144\n"
    "v_and_b32 v15, 0xffff0000, v144\n"
    "v_lshlrev_b32 v16, 16, v145\n"
    "v_and_b32 v17, 0xffff0000, v145\n"
    "v_fma_f32 v14, v14, v12, v13\n"
    "v_fma_f32 v15, v15, v12, v13\n"
    "v_fma_f32 v16, v16, v12, v13\n"
    "v_fma_f32 v17, v17, v12, v13\n"
    "s_waitcnt lgkmcnt(2)\n"
    "v_fma_f32 v14, v14, v208, v212\n"
    "v_fma_f32 v15, v15, v209, v213\n"
    "v_fma_f32 v16, v16, v210, v214\n"
    "v_fma_f32 v17, v17, v211, v215\n"
    "ds_read_b128 v[208:211], v27 offset:512\n"
    "ds_read_b128 v[212:215], v27 offset:528\n"
    "v_cvt_pk_bf16_f32 v144, v14, v15\n"
    "v_cvt_pk_bf16_f32 v145, v16, v17\n"
    "v_lshlrev_b32 v18, 16, v146\n"
    "v_and_b32 v19, 0xffff0000, v146\n"
    "v_lshlrev_b32 v20, 16, v147\n"
    "v_and_b32 v21, 0xffff0000, v147\n"
    "v_fma_f32 v18, v18, v12, v13\n"
    "v_fma_f32 v19, v19, v12, v13\n"
    "v_fma_f32 v20, v20, v12, v13\n"
    "v_fma_f32 v21, v21, v12, v13\n"
    "s_waitcnt lgkmcnt(2)\n"
    "v_fma_f32 v18, v18, v216, v220\n"
    "v_fma_f32 v19, v19, v217, v221\n"
    "v_fma_f32 v20, v20, v218, v222\n"
    "v_fma_f32 v21, v21, v219, v223\n"
    "ds_read_b128 v[216:219], v27 offset:544\n"
    "ds_read_b128 v[220:223], v27 offset:560\n"
    "v_cvt_pk_bf16_f32 v146, v18, v19\n"
    "v_cvt_pk_bf16_f32 v147, v20, v21\n"
    "ds_write_b128 v26, v[144:147]\n"
    "s_waitcnt lgkmcnt(0)\n"
    "s_barrier\n"
    "ds_read_b128 v[144:147], v1\n"
    "ds_read_b128 v[148:151], v1 offset:18432\n"
    "ds_read_b128 v[152:155], v7\n"
    "ds_read_b128 v[156:159], v7 offset:18432\n"
    "s_waitcnt lgkmcnt(3)\n"
    "v_mfma_f32_16x16x32_bf16 v[64:67], v[28:31], v[144:147], 0\n"
    "ds_read_b128 v[160:163], v1 offset:1024\n"
    "s_waitcnt vmcnt(7)\n"
    "v_lshlrev_b32 v14, 16, v176\n"
    "v_and_b32 v15, 0xffff0000, v176\n"
    "v_lshlrev_b32 v16, 16, v177\n"
    "v_and_b32 v17, 0xffff0000, v177\n"
    "s_waitcnt lgkmcnt(3)\n"
    "v_mfma_f32_16x16x32_bf16 v[68:71], v[28:31], v[148:151], 0\n"
    "v_fma_f32 v14, v14, v12, v13\n"
    "v_fma_f32 v15, v15, v12, v13\n"
    "v_fma_f32 v16, v16, v12, v13\n"
    "s_waitcnt lgkmcnt(2)\n"
    "v_mfma_f32_16x16x32_bf16 v[72:75], v[28:31], v[152:155], 0\n"
    "v_fma_f32 v17, v17, v12, v13\n"
    "v_fma_f32 v14, v14, v208, v212\n"
    "v_fma_f32 v15, v15, v209, v213\n"
    "s_waitcnt lgkmcnt(1)\n"
    "v_mfma_f32_16x16x32_bf16 v[76:79], v[28:31], v[156:159], 0\n"
    "v_fma_f32 v16, v16, v210, v214\n"
    "v_fma_f32 v17, v17, v211, v215\n"
    "ds_read_b128 v[208:211], v27 offset:1024\n"
    "ds_read_b128 v[212:215], v27 offset:1040\n"
    "buffer_load_dwordx4 v[28:31], v252, s[20:23], s96 offen offset:3072 sc1\n"
    "v_mfma_f32_16x16x32_bf16 v[80:83], v[32:35], v[144:147], 0\n"
    "ds_read_b128 v[164:167], v1 offset:19456\n"
    "v_cvt_pk_bf16_f32 v176, v14, v15\n"
    "v_cvt_pk_bf16_f32 v177, v16, v17\n"
    "v_lshlrev_b32 v18, 16, v178\n"
    "v_mfma_f32_16x16x32_bf16 v[84:87], v[32:35], v[148:151], 0\n"
    "v_and_b32 v19, 0xffff0000, v178\n"
    "v_lshlrev_b32 v20, 16, v179\n"
    "v_and_b32 v21, 0xffff0000, v179\n"
    "v_mfma_f32_16x16x32_bf16 v[88:91], v[32:35], v[152:155], 0\n"
    "v_fma_f32 v18, v18, v12, v13\n"
    "v_fma_f32 v19, v19, v12, v13\n"
    "v_fma_f32 v20, v20, v12, v13\n"
    "v_fma_f32 v21, v21, v12, v13\n"
    "v_mfma_f32_16x16x32_bf16 v[92:95], v[32:35], v[156:159], 0\n"
    "v_fma_f32 v18, v18, v216, v220\n"
    "v_fma_f32 v19, v19, v217, v221\n"
    "v_fma_f32 v20, v20, v218, v222\n"
    "buffer_load_dwordx4 v[32:35], v253, s[20:23], s96 offen offset:3072 sc1\n"
    "v_mfma_f32_16x16x32_bf16 v[96:99], v[36:39], v[144:147], 0\n"
    "ds_read_b128 v[168:171], v7 offset:1024\n"
    "v_fma_f32 v21, v21, v219, v223\n"
    "ds_read_b128 v[216:219], v27 offset:1056\n"
    "ds_read_b128 v[220:223], v27 offset:1072\n"
    "v_mfma_f32_16x16x32_bf16 v[100:103], v[36:39], v[148:151], 0\n"
    "v_cvt_pk_bf16_f32 v178, v18, v19\n"
    "v_cvt_pk_bf16_f32 v179, v20, v21\n"
    "ds_write_b128 v26, v[176:179] offset:2048\n"
    "v_mfma_f32_16x16x32_bf16 v[104:107], v[36:39], v[152:155], 0\n"
    "s_waitcnt vmcnt(8)\n"
    "v_lshlrev_b32 v14, 16, v180\n"
    "v_and_b32 v15, 0xffff0000, v180\n"
    "v_lshlrev_b32 v16, 16, v181\n"
    "v_and_b32 v17, 0xffff0000, v181\n"
    "v_mfma_f32_16x16x32_bf16 v[108:111], v[36:39], v[156:159], 0\n"
    "v_fma_f32 v14, v14, v12, v13\n"
    "v_fma_f32 v15, v15, v12, v13\n"
    "v_fma_f32 v16, v16, v12, v13\n"
    "buffer_load_dwordx4 v[36:39], v254, s[20:23], s96 offen offset:3072 sc1\n"
    "v_mfma_f32_16x16x32_bf16 v[112:115], v[40:43], v[144:147], 0\n"
    "ds_read_b128 v[172:175], v7 offset:19456\n"
    "v_fma_f32 v17, v17, v12, v13\n"
    "s_waitcnt lgkmcnt(6)\n"
    "v_fma_f32 v14, v14, v208, v212\n"
    "v_fma_f32 v15, v15, v209, v213\n"
    "v_mfma_f32_16x16x32_bf16 v[116:119], v[40:43], v[148:151], 0\n"
    "v_fma_f32 v16, v16, v210, v214\n"
    "v_fma_f32 v17, v17, v211, v215\n"
    "ds_read_b128 v[208:211], v27 offset:1536\n"
    "ds_read_b128 v[212:215], v27 offset:1552\n"
    "v_mfma_f32_16x16x32_bf16 v[120:123], v[40:43], v[152:155], 0\n"
    "v_cvt_pk_bf16_f32 v180, v14, v15\n"
    "v_cvt_pk_bf16_f32 v181, v16, v17\n"
    "v_lshlrev_b32 v18, 16, v182\n"
    "v_mfma_f32_16x16x32_bf16 v[124:127], v[40:43], v[156:159], 0\n"
    "v_and_b32 v19, 0xffff0000, v182\n"
    "v_lshlrev_b32 v20, 16, v183\n"
    "v_and_b32 v21, 0xffff0000, v183\n"
    "buffer_load_dwordx4 v[40:43], v1, s[20:23], s97 offen offset:3072 sc1\n"
    "v_mfma_f32_16x16x32_bf16 v[128:131], v[236:239], v[144:147], 0\n"
    "v_fma_f32 v18, v18, v12, v13\n"
    "v_fma_f32 v19, v19, v12, v13\n"
    "v_fma_f32 v20, v20, v12, v13\n"
    "v_fma_f32 v21, v21, v12, v13\n"
    "v_mfma_f32_16x16x32_bf16 v[132:135], v[236:239], v[148:151], 0\n"
    "s_waitcnt lgkmcnt(4)\n"
    "v_fma_f32 v18, v18, v216, v220\n"
    "v_fma_f32 v19, v19, v217, v221\n"
    "v_fma_f32 v20, v20, v218, v222\n"
    "v_mfma_f32_16x16x32_bf16 v[136:139], v[236:239], v[152:155], 0\n"
    "v_fma_f32 v21, v21, v219, v223\n"
    "ds_read_b128 v[216:219], v27 offset:1568\n"
    "ds_read_b128 v[220:223], v27 offset:1584\n"
    "v_mfma_f32_16x16x32_bf16 v[140:143], v[236:239], v[156:159], 0\n"
    "v_cvt_pk_bf16_f32 v182, v18, v19\n"
    "v_cvt_pk_bf16_f32 v183, v20, v21\n"
    "ds_write_b128 v26, v[180:183] offset:4096\n"
    "s_add_u32 s96, s96, 4096\n"
    "buffer_load_dwordx4 v[236:239], v1, s[20:23], s96 offen sc1\n"
    "s_waitcnt lgkmcnt(0)\n"
    "s_barrier\n"
    "v_mfma_f32_16x16x32_bf16 v[64:67], v[44:47], v[160:163], v[64:67]\n"
    "ds_read_b128 v[144:147], v1 offset:2048\n"
    "s_waitcnt vmcnt(10)\n"
    "v_lshlrev_b32 v14, 16, v184\n"
    "v_and_b32 v15, 0xffff0000, v184\n"
    "v_mfma_f32_16x16x32_bf16 v[68:71], v[44:47], v[164:167], v[68:71]\n"
    "v_lshlrev_b32 v16, 16, v185\n"
    "v_mfma_f32_16x16x32_bf16 v[72:75], v[44:47], v[168:171], v[72:75]\n"
    "v_and_b32 v17, 0xffff0000, v185\n"
    "v_mfma_f32_16x16x32_bf16 v[76:79], v[44:47], v[172:175], v[76:79]\n"
    "v_fma_f32 v14, v14, v12, v13\n"
    "buffer_load_dwordx4 v[176:179], v252, s[20:23], s96 offen sc1\n"
    "v_mfma_f32_16x16x32_bf16 v[80:83], v[48:51], v[160:163], v[80:83]\n"
    "ds_read_b128 v[148:151], v1 offset:20480\n"
    "v_fma_f32 v15, v15, v12, v13\n"
    "v_fma_f32 v16, v16, v12, v13\n"
    "v_mfma_f32_16x16x32_bf16 v[84:87], v[48:51], v[164:167], v[84:87]\n"
    "v_fma_f32 v17, v17, v12, v13\n"
    "v_mfma_f32_16x16x32_bf16 v[88:91], v[48:51], v[168:171], v[88:91]\n"
    "v_fma_f32 v14, v14, v208, v212\n"
    "v_mfma_f32_16x16x32_bf16 v[92:95], v[48:51], v[172:175], v[92:95]\n"
    "v_fma_f32 v15, v15, v209, v213\n"
    "buffer_load_dwordx4 v[180:183], v253, s[20:23], s96 offen sc1\n"
    "v_mfma_f32_16x16x32_bf16 v[96:99], v[52:55], v[160:163], v[96:99]\n"
    "ds_read_b128 v[152:155], v7 offset:2048\n"
    "v_fma_f32 v16, v16, v210, v214\n"
    "v_fma_f32 v17, v17, v211, v215\n"
    "v_mfma_f32_16x16x32_bf16 v[100:103], v[52:55], v[164:167], v[100:103]\n"
    "ds_read_b128 v[208:211], v27 offset:2048\n"
    "v_mfma_f32_16x16x32_bf16 v[104:107], v[52:55], v[168:171], v[104:107]\n"
    "ds_read_b128 v[212:215], v27 offset:2064\n"
    "v_mfma_f32_16x16x32_bf16 v[108:111], v[52:55], v[172:175], v[108:111]\n"
    "v_cvt_pk_bf16_f32 v184, v14, v15\n"
    "buffer_load_dwordx4 v[44:47], v254, s[20:23], s96 offen sc1\n"
    "v_mfma_f32_16x16x32_bf16 v[112:115], v[56:59], v[160:163], v[112:115]\n"
    "ds_read_b128 v[156:159], v7 offset:20480\n"
    "v_cvt_pk_bf16_f32 v185, v16, v17\n"
    "v_lshlrev_b32 v18, 16, v186\n"
    "v_mfma_f32_16x16x32_bf16 v[116:119], v[56:59], v[164:167], v[116:119]\n"
    "v_and_b32 v19, 0xffff0000, v186\n"
    "v_mfma_f32_16x16x32_bf16 v[120:123], v[56:59], v[168:171], v[120:123]\n"
    "v_lshlrev_b32 v20, 16, v187\n"
    "v_mfma_f32_16x16x32_bf16 v[124:127], v[56:59], v[172:175], v[124:127]\n"
    "v_and_b32 v21, 0xffff0000, v187\n"
    "s_add_u32 s97, s97, 4096\n"
    "buffer_load_dwordx4 v[48:51], v1, s[20:23], s97 offen sc1\n"
    "v_mfma_f32_16x16x32_bf16 v[128:131], v[240:243], v[160:163], v[128:131]\n"
    "v_fma_f32 v18, v18, v12, v13\n"
    "v_fma_f32 v19, v19, v12, v13\n"
    "v_mfma_f32_16x16x32_bf16 v[132:135], v[240:243], v[164:167], v[132:135]\n"
    "v_fma_f32 v20, v20, v12, v13\n"
    "v_mfma_f32_16x16x32_bf16 v[136:139], v[240:243], v[168:171], v[136:139]\n"
    "v_fma_f32 v21, v21, v12, v13\n"
    "v_mfma_f32_16x16x32_bf16 v[140:143], v[240:243], v[172:175], v[140:143]\n"
    "v_fma_f32 v18, v18, v216, v220\n"
    "buffer_load_dwordx4 v[52:55], v1, s[20:23], s96 offen offset:1024 sc1\n"
    "s_waitcnt lgkmcnt(5)\n"
    "v_mfma_f32_16x16x32_bf16 v[64:67], v[60:63], v[144:147], v[64:67]\n"
    "ds_read_b128 v[160:163], v1 offset:3072\n"
    "v_fma_f32 v19, v19, v217, v221\n"
    "s_waitcnt lgkmcnt(5)\n"
    "v_mfma_f32_16x16x32_bf16 v[68:71], v[60:63], v[148:151], v[68:71]\n"
    "v_fma_f32 v20, v20, v218, v222\n"
    "v_fma_f32 v21, v21, v219, v223\n"
    "s_waitcnt lgkmcnt(4)\n"
    "v_mfma_f32_16x16x32_bf16 v[72:75], v[60:63], v[152:155], v[72:75]\n"
    "ds_read_b128 v[216:219], v27 offset:2080\n"
    "s_waitcnt lgkmcnt(2)\n"
    "v_mfma_f32_16x16x32_bf16 v[76:79], v[60:63], v[156:159], v[76:79]\n"
    "ds_read_b128 v[220:223], v27 offset:2096\n"
    "buffer_load_dwordx4 v[56:59], v252, s[20:23], s96 offen offset:1024 sc1\n"
    "v_mfma_f32_16x16x32_bf16 v[80:83], v[224:227], v[144:147], v[80:83]\n"
    "ds_read_b128 v[164:167], v1 offset:21504\n"
    "v_cvt_pk_bf16_f32 v186, v18, v19\n"
    "v_mfma_f32_16x16x32_bf16 v[84:87], v[224:227], v[148:151], v[84:87]\n"
    "v_cvt_pk_bf16_f32 v187, v20, v21\n"
    "ds_write_b128 v26, v[184:187] offset:6144\n"
    "v_mfma_f32_16x16x32_bf16 v[88:91], v[224:227], v[152:155], v[88:91]\n"
    "s_waitcnt vmcnt(15)\n"
    "v_lshlrev_b32 v14, 16, v188\n"
    "v_mfma_f32_16x16x32_bf16 v[92:95], v[224:227], v[156:159], v[92:95]\n"
    "v_and_b32 v15, 0xffff0000, v188\n"
    "buffer_load_dwordx4 v[240:243], v253, s[20:23], s96 offen offset:1024 sc1\n"
    "v_mfma_f32_16x16x32_bf16 v[96:99], v[228:231], v[144:147], v[96:99]\n"
    "ds_read_b128 v[168:171], v7 offset:3072\n"
    "v_lshlrev_b32 v16, 16, v189\n"
    "v_mfma_f32_16x16x32_bf16 v[100:103], v[228:231], v[148:151], v[100:103]\n"
    "v_and_b32 v17, 0xffff0000, v189\n"
    "v_fma_f32 v14, v14, v12, v13\n"
    "v_mfma_f32_16x16x32_bf16 v[104:107], v[228:231], v[152:155], v[104:107]\n"
    "v_fma_f32 v15, v15, v12, v13\n"
    "v_mfma_f32_16x16x32_bf16 v[108:111], v[228:231], v[156:159], v[108:111]\n"
    "v_fma_f32 v16, v16, v12, v13\n"
    "buffer_load_dwordx4 v[60:63], v254, s[20:23], s96 offen offset:1024 sc1\n"
    "v_mfma_f32_16x16x32_bf16 v[112:115], v[232:235], v[144:147], v[112:115]\n"
    "ds_read_b128 v[172:175], v7 offset:21504\n"
    "v_fma_f32 v17, v17, v12, v13\n"
    "v_mfma_f32_16x16x32_bf16 v[116:119], v[232:235], v[148:151], v[116:119]\n"
    "v_fma_f32 v14, v14, v208, v212\n"
    "v_fma_f32 v15, v15, v209, v213\n"
    "v_mfma_f32_16x16x32_bf16 v[120:123], v[232:235], v[152:155], v[120:123]\n"
    "v_fma_f32 v16, v16, v210, v214\n"
    "v_mfma_f32_16x16x32_bf16 v[124:127], v[232:235], v[156:159], v[124:127]\n"
    "v_fma_f32 v17, v17, v211, v215\n"
    "buffer_load_dwordx4 v[224:227], v1, s[20:23], s97 offen offset:1024 sc1\n"
    "v_mfma_f32_16x16x32_bf16 v[128:131], v[244:247], v[144:147], v[128:131]\n"
    "ds_read_b128 v[208:211], v27 offset:2560\n"
    "v_mfma_f32_16x16x32_bf16 v[132:135], v[244:247], v[148:151], v[132:135]\n"
    "ds_read_b128 v[212:215], v27 offset:2576\n"
    "v_cvt_pk_bf16_f32 v188, v14, v15\n"
    "v_mfma_f32_16x16x32_bf16 v[136:139], v[244:247], v[152:155], v[136:139]\n"
    "v_cvt_pk_bf16_f32 v189, v16, v17\n"
    "v_mfma_f32_16x16x32_bf16 v[140:143], v[244:247], v[156:159], v[140:143]\n"
    "v_lshlrev_b32 v18, 16, v190\n"
    "buffer_load_dwordx4 v[228:231], v1, s[20:23], s96 offen offset:2048 sc1\n"
    "s_waitcnt lgkmcnt(8)\n"
    "v_mfma_f32_16x16x32_bf16 v[64:67], v[248:251], v[160:163], v[64:67]\n"
    "ds_read_b128 v[144:147], v1 offset:4096\n"
    "v_and_b32 v19, 0xffff0000, v190\n"
    "s_waitcnt lgkmcnt(6)\n"
    "v_mfma_f32_16x16x32_bf16 v[68:71], v[248:251], v[164:167], v[68:71]\n"
    "v_lshlrev_b32 v20, 16, v191\n"
    "s_waitcnt lgkmcnt(4)\n"
    "v_mfma_f32_16x16x32_bf16 v[72:75], v[248:251], v[168:171], v[72:75]\n"
    "v_and_b32 v21, 0xffff0000, v191\n"
    "v_fma_f32 v18, v18, v12, v13\n"
    "s_waitcnt lgkmcnt(3)\n"
    "v_mfma_f32_16x16x32_bf16 v[76:79], v[248:251], v[172:175], v[76:79]\n"
    "v_fma_f32 v19, v19, v12, v13\n"
    "buffer_load_dwordx4 v[232:235], v252, s[20:23], s96 offen offset:2048 sc1\n"
    "s_waitcnt vmcnt(15)\n"
    "v_mfma_f32_16x16x32_bf16 v[80:83], v[28:31], v[160:163], v[80:83]\n"
    "ds_read_b128 v[148:151], v1 offset:22528\n"
    "v_fma_f32 v20, v20, v12, v13\n"
    "v_mfma_f32_16x16x32_bf16 v[84:87], v[28:31], v[164:167], v[84:87]\n"
    "v_fma_f32 v21, v21, v12, v13\n"
    "v_mfma_f32_16x16x32_bf16 v[88:91], v[28:31], v[168:171], v[88:91]\n"
    "v_fma_f32 v18, v18, v216, v220\n"
    "v_fma_f32 v19, v19, v217, v221\n"
    "v_mfma_f32_16x16x32_bf16 v[92:95], v[28:31], v[172:175], v[92:95]\n"
    "v_fma_f32 v20, v20, v218, v222\n"
    "buffer_load_dwordx4 v[244:247], v253, s[20:23], s96 offen offset:2048 sc1\n"
    "s_waitcnt vmcnt(15)\n"
    "v_mfma_f32_16x16x32_bf16 v[96:99], v[32:35], v[160:163], v[96:99]\n"
    "ds_read_b128 v[152:155], v7 offset:4096\n"
    "v_fma_f32 v21, v21, v219, v223\n"
    "v_mfma_f32_16x16x32_bf16 v[100:103], v[32:35], v[164:167], v[100:103]\n"
    "ds_read_b128 v[216:219], v27 offset:2592\n"
    "v_mfma_f32_16x16x32_bf16 v[104:107], v[32:35], v[168:171], v[104:107]\n"
    "ds_read_b128 v[220:223], v27 offset:2608\n"
    "v_cvt_pk_bf16_f32 v190, v18, v19\n"
    "v_mfma_f32_16x16x32_bf16 v[108:111], v[32:35], v[172:175], v[108:111]\n"
    "v_cvt_pk_bf16_f32 v191, v20, v21\n"
    "buffer_load_dwordx4 v[248:251], v254, s[20:23], s96 offen offset:2048 sc1\n"
    "s_waitcnt vmcnt(15)\n"
    "v_mfma_f32_16x16x32_bf16 v[112:115], v[36:39], v[160:163], v[112:115]\n"
    "ds_read_b128 v[156:159], v7 offset:22528\n"
    "ds_write_b128 v26, v[188:191] offset:8192\n"
    "v_mfma_f32_16x16x32_bf16 v[116:119], v[36:39], v[164:167], v[116:119]\n"
    "v_lshlrev_b32 v14, 16, v192\n"
    "v_mfma_f32_16x16x32_bf16 v[120:123], v[36:39], v[168:171], v[120:123]\n"
    "v_and_b32 v15, 0xffff0000, v192\n"
    "v_lshlrev_b32 v16, 16, v193\n"
    "v_mfma_f32_16x16x32_bf16 v[124:127], v[36:39], v[172:175], v[124:127]\n"
    "v_and_b32 v17, 0xffff0000, v193\n"
    "buffer_load_dwordx4 v[28:31], v1, s[20:23], s97 offen offset:2048 sc1\n"
    "s_waitcnt vmcnt(15)\n"
    "v_mfma_f32_16x16x32_bf16 v[128:131], v[40:43], v[160:163], v[128:131]\n"
    "v_fma_f32 v14, v14, v12, v13\n"
    "v_mfma_f32_16x16x32_bf16 v[132:135], v[40:43], v[164:167], v[132:135]\n"
    "v_fma_f32 v15, v15, v12, v13\n"
    "v_mfma_f32_16x16x32_bf16 v[136:139], v[40:43], v[168:171], v[136:139]\n"
    "v_fma_f32 v16, v16, v12, v13\n"
    "v_fma_f32 v17, v17, v12, v13\n"
    "v_mfma_f32_16x16x32_bf16 v[140:143], v[40:43], v[172:175], v[140:143]\n"
    "s_waitcnt lgkmcnt(7)\n"
    "v_fma_f32 v14, v14, v208, v212\n"
    "buffer_load_dwordx4 v[32:35], v1, s[20:23], s96 offen offset:3072 sc1\n"
    "s_waitcnt vmcnt(15) lgkmcnt(6)\n"
    "v_mfma_f32_16x16x32_bf16 v[64:67], v[236:239], v[144:147], v[64:67]\n"
    "ds_read_b128 v[160:163], v1 offset:5120\n"
    "v_fma_f32 v15, v15, v209, v213\n"
    "s_waitcnt lgkmcnt(6)\n"
    "v_mfma_f32_16x16x32_bf16 v[68:71], v[236:239], v[148:151], v[68:71]\n"
    "v_fma_f32 v16, v16, v210, v214\n"
    "s_waitcnt lgkmcnt(5)\n"
    "v_mfma_f32_16x16x32_bf16 v[72:75], v[236:239], v[152:155], v[72:75]\n"
    "v_fma_f32 v17, v17, v211, v215\n"
    "s_waitcnt lgkmcnt(2)\n"
    "v_mfma_f32_16x16x32_bf16 v[76:79], v[236:239], v[156:159], v[76:79]\n"
    "ds_read_b128 v[208:211], v27 offset:3072\n"
    "ds_read_b128 v[212:215], v27 offset:3088\n"
    "buffer_load_dwordx4 v[36:39], v252, s[20:23], s96 offen offset:3072 sc1\n"
    "s_waitcnt vmcnt(15)\n"
    "v_mfma_f32_16x16x32_bf16 v[80:83], v[176:179], v[144:147], v[80:83]\n"
    "ds_read_b128 v[164:167], v1 offset:23552\n"
    "v_cvt_pk_bf16_f32 v192, v14, v15\n"
    "v_mfma_f32_16x16x32_bf16 v[84:87], v[176:179], v[148:151], v[84:87]\n"
    "v_cvt_pk_bf16_f32 v193, v16, v17\n"
    "v_mfma_f32_16x16x32_bf16 v[88:91], v[176:179], v[152:155], v[88:91]\n"
    "v_lshlrev_b32 v18, 16, v194\n"
    "v_mfma_f32_16x16x32_bf16 v[92:95], v[176:179], v[156:159], v[92:95]\n"
    "v_and_b32 v19, 0xffff0000, v194\n"
    "v_lshlrev_b32 v20, 16, v195\n"
    "buffer_load_dwordx4 v[40:43], v253, s[20:23], s96 offen offset:3072 sc1\n"
    "s_waitcnt vmcnt(15)\n"
    "v_mfma_f32_16x16x32_bf16 v[96:99], v[180:183], v[144:147], v[96:99]\n"
    "ds_read_b128 v[168:171], v7 offset:5120\n"
    "v_and_b32 v21, 0xffff0000, v195\n"
    "v_mfma_f32_16x16x32_bf16 v[100:103], v[180:183], v[148:151], v[100:103]\n"
    "v_fma_f32 v18, v18, v12, v13\n"
    "v_mfma_f32_16x16x32_bf16 v[104:107], v[180:183], v[152:155], v[104:107]\n"
    "v_fma_f32 v19, v19, v12, v13\n"
    "v_mfma_f32_16x16x32_bf16 v[108:111], v[180:183], v[156:159], v[108:111]\n"
    "v_fma_f32 v20, v20, v12, v13\n"
    "v_fma_f32 v21, v21, v12, v13\n"
    "buffer_load_dwordx4 v[236:239], v254, s[20:23], s96 offen offset:3072 sc1\n"
    "s_waitcnt vmcnt(15)\n"
    "v_mfma_f32_16x16x32_bf16 v[112:115], v[44:47], v[144:147], v[112:115]\n"
    "ds_read_b128 v[172:175], v7 offset:23552\n"
    "v_fma_f32 v18, v18, v216, v220\n"
    "v_mfma_f32_16x16x32_bf16 v[116:119], v[44:47], v[148:151], v[116:119]\n"
    "v_fma_f32 v19, v19, v217, v221\n"
    "v_mfma_f32_16x16x32_bf16 v[120:123], v[44:47], v[152:155], v[120:123]\n"
    "v_fma_f32 v20, v20, v218, v222\n"
    "v_mfma_f32_16x16x32_bf16 v[124:127], v[44:47], v[156:159], v[124:127]\n"
    "v_fma_f32 v21, v21, v219, v223\n"
    "ds_read_b128 v[216:219], v27 offset:3104\n"
    "buffer_load_dwordx4 v[176:179], v1, s[20:23], s97 offen offset:3072 sc1\n"
    "s_waitcnt vmcnt(15)\n"
    "v_mfma_f32_16x16x32_bf16 v[128:131], v[48:51], v[144:147], v[128:131]\n"
    "ds_read_b128 v[220:223], v27 offset:3120\n"
    "v_mfma_f32_16x16x32_bf16 v[132:135], v[48:51], v[148:151], v[132:135]\n"
    "v_cvt_pk_bf16_f32 v194, v18, v19\n"
    "v_mfma_f32_16x16x32_bf16 v[136:139], v[48:51], v[152:155], v[136:139]\n"
    "v_cvt_pk_bf16_f32 v195, v20, v21\n"
    "v_mfma_f32_16x16x32_bf16 v[140:143], v[48:51], v[156:159], v[140:143]\n"
    "ds_write_b128 v26, v[192:195] offset:10240\n"
    "s_add_u32 s96, s96, 4096\n"
    "buffer_load_dwordx4 v[180:183], v1, s[20:23], s96 offen sc1\n"
    "s_waitcnt lgkmcnt(0)\n"
    "s_barrier\n"
    "s_waitcnt vmcnt(15)\n"
    "v_mfma_f32_16x16x32_bf16 v[64:67], v[52:55], v[160:163], v[64:67]\n"
    "ds_read_b128 v[144:147], v1 offset:6144\n"
    "v_lshlrev_b32 v14, 16, v196\n"
    "v_mfma_f32_16x16x32_bf16 v[68:71], v[52:55], v[164:167], v[68:71]\n"
    "v_and_b32 v15, 0xffff0000, v196\n"
    "v_mfma_f32_16x16x32_bf16 v[72:75], v[52:55], v[168:171], v[72:75]\n"
    "v_lshlrev_b32 v16, 16, v197\n"
    "v_mfma_f32_16x16x32_bf16 v[76:79], v[52:55], v[172:175], v[76:79]\n"
    "v_and_b32 v17, 0xffff0000, v197\n"
    "buffer_load_dwordx4 v[44:47], v252, s[20:23], s96 offen sc1\n"
    "s_waitcnt vmcnt(15)\n"
    "v_mfma_f32_16x16x32_bf16 v[80:83], v[56:59], v[160:163], v[80:83]\n"
    "ds_read_b128 v[148:151], v1 offset:24576\n"
    "v_mfma_f32_16x16x32_bf16 v[84:87], v[56:59], v[164:167], v[84:87]\n"
    "v_fma_f32 v14, v14, v12, v13\n"
    "v_mfma_f32_16x16x32_bf16 v[88:91], v[56:59], v[168:171], v[88:91]\n"
    "v_fma_f32 v15, v15, v12, v13\n"
    "v_mfma_f32_16x16x32_bf16 v[92:95], v[56:59], v[172:175], v[92:95]\n"
    "v_fma_f32 v16, v16, v12, v13\n"
    "buffer_load_dwordx4 v[48:51], v253, s[20:23], s96 offen sc1\n"
    "s_waitcnt vmcnt(15)\n"
    "v_mfma_f32_16x16x32_bf16 v[96:99], v[240:243], v[160:163], v[96:99]\n"
    "ds_read_b128 v[152:155], v7 offset:6144\n"
    "v_fma_f32 v17, v17, v12, v13\n"
    "v_mfma_f32_16x16x32_bf16 v[100:103], v[240:243], v[164:167], v[100:103]\n"
    "v_mfma_f32_16x16x32_bf16 v[104:107], v[240:243], v[168:171], v[104:107]\n"
    "v_fma_f32 v14, v14, v208, v212\n"
    "v_mfma_f32_16x16x32_bf16 v[108:111], v[240:243], v[172:175], v[108:111]\n"
    "v_fma_f32 v15, v15, v209, v213\n"
    "buffer_load_dwordx4 v[184:187], v254, s[20:23], s96 offen sc1\n"
    "s_waitcnt vmcnt(15)\n"
    "v_mfma_f32_16x16x32_bf16 v[112:115], v[60:63], v[160:163], v[112:115]\n"
    "ds_read_b128 v[156:159], v7 offset:24576\n"
    "v_fma_f32 v16, v16, v210, v214\n"
    "v_mfma_f32_16x16x32_bf16 v[116:119], v[60:63], v[164:167], v[116:119]\n"
    "v_fma_f32 v17, v17, v211, v215\n"
    "v_mfma_f32_16x16x32_bf16 v[120:123], v[60:63], v[168:171], v[120:123]\n"
    "v_mfma_f32_16x16x32_bf16 v[124:127], v[60:63], v[172:175], v[124:127]\n"
    "ds_read_b128 v[208:211], v27 offset:3584\n"
    "s_add_u32 s97, s97, 4096\n"
    "buffer_load_dwordx4 v[188:191], v1, s[20:23], s97 offen sc1\n"
    "s_waitcnt vmcnt(15)\n"
    "v_mfma_f32_16x16x32_bf16 v[128:131], v[224:227], v[160:163], v[128:131]\n"
    "ds_read_b128 v[212:215], v27 offset:3600\n"
    "v_mfma_f32_16x16x32_bf16 v[132:135], v[224:227], v[164:167], v[132:135]\n"
    "v_cvt_pk_bf16_f32 v196, v14, v15\n"
    "v_mfma_f32_16x16x32_bf16 v[136:139], v[224:227], v[168:171], v[136:139]\n"
    "v_cvt_pk_bf16_f32 v197, v16, v17\n"
    "v_mfma_f32_16x16x32_bf16 v[140:143], v[224:227], v[172:175], v[140:143]\n"
    "buffer_load_dwordx4 v[192:195], v1, s[20:23], s96 offen offset:1024 sc1\n"
    "s_waitcnt vmcnt(15) lgkmcnt(5)\n"
    "v_mfma_f32_16x16x32_bf16 v[64:67], v[228:231], v[144:147], v[64:67]\n"
    "ds_read_b128 v[160:163], v1 offset:7168\n"
    "v_lshlrev_b32 v18, 16, v198\n"
    "s_waitcnt lgkmcnt(5)\n"
    "v_mfma_f32_16x16x32_bf16 v[68:71], v[228:231], v[148:151], v[68:71]\n"
    "v_and_b32 v19, 0xffff0000, v198\n"
    "s_waitcnt lgkmcnt(4)\n"
    "v_mfma_f32_16x16x32_bf16 v[72:75], v[228:231], v[152:155], v[72:75]\n"
    "v_lshlrev_b32 v20, 16, v199\n"
    "s_waitcnt lgkmcnt(3)\n"
    "v_mfma_f32_16x16x32_bf16 v[76:79], v[228:231], v[156:159], v[76:79]\n"
    "buffer_load_dwordx4 v[52:55], v252, s[20:23], s96 offen offset:1024 sc1\n"
    "s_waitcnt vmcnt(15)\n"
    "v_mfma_f32_16x16x32_bf16 v[80:83], v[232:235], v[144:147], v[80:83]\n"
    "ds_read_b128 v[164:167], v1 offset:25600\n"
    "v_and_b32 v21, 0xffff0000, v199\n"
    "v_mfma_f32_16x16x32_bf16 v[84:87], v[232:235], v[148:151], v[84:87]\n"
    "v_fma_f32 v18, v18, v12, v13\n"
    "v_mfma_f32_16x16x32_bf16 v[88:91], v[232:235], v[152:155], v[88:91]\n"
    "v_fma_f32 v19, v19, v12, v13\n"
    "v_mfma_f32_16x16x32_bf16 v[92:95], v[232:235], v[156:159], v[92:95]\n"
    "v_fma_f32 v20, v20, v12, v13\n"
    "buffer_load_dwordx4 v[56:59], v253, s[20:23], s96 offen offset:1024 sc1\n"
    "s_waitcnt vmcnt(15)\n"
    "v_mfma_f32_16x16x32_bf16 v[96:99], v[244:247], v[144:147], v[96:99]\n"
    "ds_read_b128 v[168:171], v7 offset:7168\n"
    "v_mfma_f32_16x16x32_bf16 v[100:103], v[244:247], v[148:151], v[100:103]\n"
    "v_fma_f32 v21, v21, v12, v13\n"
    "v_mfma_f32_16x16x32_bf16 v[104:107], v[244:247], v[152:155], v[104:107]\n"
    "v_fma_f32 v18, v18, v216, v220\n"
    "v_mfma_f32_16x16x32_bf16 v[108:111], v[244:247], v[156:159], v[108:111]\n"
    "v_fma_f32 v19, v19, v217, v221\n"
    "buffer_load_dwordx4 v[240:243], v254, s[20:23], s96 offen offset:1024 sc1\n"
    "s_waitcnt vmcnt(15)\n"
    "v_mfma_f32_16x16x32_bf16 v[112:115], v[248:251], v[144:147], v[112:115]\n"
    "ds_read_b128 v[172:175], v7 offset:25600\n"
    "v_fma_f32 v20, v20, v218, v222\n"
    "v_mfma_f32_16x16x32_bf16 v[116:119], v[248:251], v[148:151], v[116:119]\n"
    "v_mfma_f32_16x16x32_bf16 v[120:123], v[248:251], v[152:155], v[120:123]\n"
    "v_fma_f32 v21, v21, v219, v223\n"
    "v_mfma_f32_16x16x32_bf16 v[124:127], v[248:251], v[156:159], v[124:127]\n"
    "ds_read_b128 v[216:219], v27 offset:3616\n"
    "buffer_load_dwordx4 v[60:63], v1, s[20:23], s97 offen offset:1024 sc1\n"
    "s_waitcnt vmcnt(15)\n"
    "v_mfma_f32_16x16x32_bf16 v[128:131], v[28:31], v[144:147], v[128:131]\n"
    "ds_read_b128 v[220:223], v27 offset:3632\n"
    "v_mfma_f32_16x16x32_bf16 v[132:135], v[28:31], v[148:151], v[132:135]\n"
    "v_cvt_pk_bf16_f32 v198, v18, v19\n"
    "v_mfma_f32_16x16x32_bf16 v[136:139], v[28:31], v[152:155], v[136:139]\n"
    "v_mfma_f32_16x16x32_bf16 v[140:143], v[28:31], v[156:159], v[140:143]\n"
    "v_cvt_pk_bf16_f32 v199, v20, v21\n"
    "buffer_load_dwordx4 v[224:227], v1, s[20:23], s96 offen offset:2048 sc1\n"
    "s_waitcnt vmcnt(15) lgkmcnt(5)\n"
    "v_mfma_f32_16x16x32_bf16 v[64:67], v[32:35], v[160:163], v[64:67]\n"
    "ds_read_b128 v[144:147], v1 offset:8192\n"
    "ds_write_b128 v26, v[196:199] offset:12288\n"
    "s_waitcnt lgkmcnt(6)\n"
    "v_mfma_f32_16x16x32_bf16 v[68:71], v[32:35], v[164:167], v[68:71]\n"
    "v_lshlrev_b32 v14, 16, v200\n"
    "s_waitcnt lgkmcnt(5)\n"
    "v_mfma_f32_16x16x32_bf16 v[72:75], v[32:35], v[168:171], v[72:75]\n"
    "v_and_b32 v15, 0xffff0000, v200\n"
    "s_waitcnt lgkmcnt(4)\n"
    "v_mfma_f32_16x16x32_bf16 v[76:79], v[32:35], v[172:175], v[76:79]\n"
    "buffer_load_dwordx4 v[228:231], v252, s[20:23], s96 offen offset:2048 sc1\n"
    "s_waitcnt vmcnt(15)\n"
    "v_mfma_f32_16x16x32_bf16 v[80:83], v[36:39], v[160:163], v[80:83]\n"
    "ds_read_b128 v[148:151], v1 offset:26624\n"
    "v_lshlrev_b32 v16, 16, v201\n"
    "v_mfma_f32_16x16x32_bf16 v[84:87], v[36:39], v[164:167], v[84:87]\n"
    "v_and_b32 v17, 0xffff0000, v201\n"
    "v_mfma_f32_16x16x32_bf16 v[88:91], v[36:39], v[168:171], v[88:91]\n"
    "v_fma_f32 v14, v14, v12, v13\n"
    "v_mfma_f32_16x16x32_bf16 v[92:95], v[36:39], v[172:175], v[92:95]\n"
    "buffer_load_dwordx4 v[232:235], v253, s[20:23], s96 offen offset:2048 sc1\n"
    "s_waitcnt vmcnt(15)\n"
    "v_mfma_f32_16x16x32_bf16 v[96:99], v[40:43], v[160:163], v[96:99]\n"
    "ds_read_b128 v[152:155], v7 offset:8192\n"
    "v_fma_f32 v15, v15, v12, v13\n"
    "v_mfma_f32_16x16x32_bf16 v[100:103], v[40:43], v[164:167], v[100:103]\n"
    "v_fma_f32 v16, v16, v12, v13\n"
    "v_mfma_f32_16x16x32_bf16 v[104:107], v[40:43], v[168:171], v[104:107]\n"
    "v_fma_f32 v17, v17, v12, v13\n"
    "v_mfma_f32_16x16x32_bf16 v[108:111], v[40:43], v[172:175], v[108:111]\n"
    "v_fma_f32 v14, v14, v208, v212\n"
    "buffer_load_dwordx4 v[244:247], v254, s[20:23], s96 offen offset:2048 sc1\n"
    "s_waitcnt vmcnt(15)\n"
    "v_mfma_f32_16x16x32_bf16 v[112:115], v[236:239], v[160:163], v[112:115]\n"
    "ds_read_b128 v[156:159], v7 offset:26624\n"
    "v_mfma_f32_16x16x32_bf16 v[116:119], v[236:239], v[164:167], v[116:119]\n"
    "v_fma_f32 v15, v15, v209, v213\n"
    "v_mfma_f32_16x16x32_bf16 v[120:123], v[236:239], v[168:171], v[120:123]\n"
    "v_fma_f32 v16, v16, v210, v214\n"
    "v_mfma_f32_16x16x32_bf16 v[124:127], v[236:239], v[172:175], v[124:127]\n"
    "v_fma_f32 v17, v17, v211, v215\n"
    "buffer_load_dwordx4 v[248:251], v1, s[20:23], s97 offen offset:2048 sc1\n"
    "s_waitcnt vmcnt(15)\n"
    "v_mfma_f32_16x16x32_bf16 v[128:131], v[176:179], v[160:163], v[128:131]\n"
    "ds_read_b128 v[208:211], v27 offset:4096\n"
    "v_mfma_f32_16x16x32_bf16 v[132:135], v[176:179], v[164:167], v[132:135]\n"
    "v_mfma_f32_16x16x32_bf16 v[136:139], v[176:179], v[168:171], v[136:139]\n"
    "ds_read_b128 v[212:215], v27 offset:4112\n"
    "v_mfma_f32_16x16x32_bf16 v[140:143], v[176:179], v[172:175], v[140:143]\n"
    "v_cvt_pk_bf16_f32 v200, v14, v15\n"
    "buffer_load_dwordx4 v[28:31], v1, s[20:23], s96 offen offset:3072 sc1\n"
    "s_waitcnt vmcnt(15) lgkmcnt(6)\n"
    "v_mfma_f32_16x16x32_bf16 v[64:67], v[180:183], v[144:147], v[64:67]\n"
    "ds_read_b128 v[160:163], v1 offset:9216\n"
    "v_cvt_pk_bf16_f32 v201, v16, v17\n"
    "s_waitcnt lgkmcnt(5)\n"
    "v_mfma_f32_16x16x32_bf16 v[68:71], v[180:183], v[148:151], v[68:71]\n"
    "v_lshlrev_b32 v18, 16, v202\n"
    "s_waitcnt lgkmcnt(4)\n"
    "v_mfma_f32_16x16x32_bf16 v[72:75], v[180:183], v[152:155], v[72:75]\n"
    "s_waitcnt lgkmcnt(3)\n"
    "v_mfma_f32_16x16x32_bf16 v[76:79], v[180:183], v[156:159], v[76:79]\n"
    "v_and_b32 v19, 0xffff0000, v202\n"
    "buffer_load_dwordx4 v[32:35], v252, s[20:23], s96 offen offset:3072 sc1\n"
    "s_waitcnt vmcnt(15)\n"
    "v_mfma_f32_16x16x32_bf16 v[80:83], v[44:47], v[144:147], v[80:83]\n"
    "ds_read_b128 v[164:167], v1 offset:27648\n"
    "v_lshlrev_b32 v20, 16, v203\n"
    "v_mfma_f32_16x16x32_bf16 v[84:87], v[44:47], v[148:151], v[84:87]\n"
    "v_and_b32 v21, 0xffff0000, v203\n"
    "v_mfma_f32_16x16x32_bf16 v[88:91], v[44:47], v[152:155], v[88:91]\n"
    "v_fma_f32 v18, v18, v12, v13\n"
    "v_mfma_f32_16x16x32_bf16 v[92:95], v[44:47], v[156:159], v[92:95]\n"
    "buffer_load_dwordx4 v[36:39], v253, s[20:23], s96 offen offset:3072 sc1\n"
    "s_waitcnt vmcnt(15)\n"
    "v_mfma_f32_16x16x32_bf16 v[96:99], v[48:51], v[144:147], v[96:99]\n"
    "ds_read_b128 v[168:171], v7 offset:9216\n"
    "v_fma_f32 v19, v19, v12, v13\n"
    "v_mfma_f32_16x16x32_bf16 v[100:103], v[48:51], v[148:151], v[100:103]\n"
    "v_fma_f32 v20, v20, v12, v13\n"
    "v_mfma_f32_16x16x32_bf16 v[104:107], v[48:51], v[152:155], v[104:107]\n"
    "v_fma_f32 v21, v21, v12, v13\n"
    "v_mfma_f32_16x16x32_bf16 v[108:111], v[48:51], v[156:159], v[108:111]\n"
    "buffer_load_dwordx4 v[40:43], v254, s[20:23], s96 offen offset:3072 sc1\n"
    "s_waitcnt vmcnt(15)\n"
    "v_mfma_f32_16x16x32_bf16 v[112:115], v[184:187], v[144:147], v[112:115]\n"
    "ds_read_b128 v[172:175], v7 offset:27648\n"
    "v_fma_f32 v18, v18, v216, v220\n"
    "v_mfma_f32_16x16x32_bf16 v[116:119], v[184:187], v[148:151], v[116:119]\n"
    "v_fma_f32 v19, v19, v217, v221\n"
    "v_mfma_f32_16x16x32_bf16 v[120:123], v[184:187], v[152:155], v[120:123]\n"
    "v_fma_f32 v20, v20, v218, v222\n"
    "v_mfma_f32_16x16x32_bf16 v[124:127], v[184:187], v[156:159], v[124:127]\n"
    "v_fma_f32 v21, v21, v219, v223\n"
    "buffer_load_dwordx4 v[236:239], v1, s[20:23], s97 offen offset:3072 sc1\n"
    "s_waitcnt vmcnt(15)\n"
    "v_mfma_f32_16x16x32_bf16 v[128:131], v[188:191], v[144:147], v[128:131]\n"
    "v_mfma_f32_16x16x32_bf16 v[132:135], v[188:191], v[148:151], v[132:135]\n"
    "ds_read_b128 v[216:219], v27 offset:4128\n"
    "v_mfma_f32_16x16x32_bf16 v[136:139], v[188:191], v[152:155], v[136:139]\n"
    "ds_read_b128 v[220:223], v27 offset:4144\n"
    "v_mfma_f32_16x16x32_bf16 v[140:143], v[188:191], v[156:159], v[140:143]\n"
    "v_cvt_pk_bf16_f32 v202, v18, v19\n"
    "s_add_u32 s96, s96, 4096\n"
    "buffer_load_dwordx4 v[176:179], v1, s[20:23], s96 offen sc1\n"
    "s_waitcnt vmcnt(15) lgkmcnt(5)\n"
    "v_mfma_f32_16x16x32_bf16 v[64:67], v[192:195], v[160:163], v[64:67]\n"
    "ds_read_b128 v[144:147], v1 offset:10240\n"
    "v_cvt_pk_bf16_f32 v203, v20, v21\n"
    "s_waitcnt lgkmcnt(5)\n"
    "v_mfma_f32_16x16x32_bf16 v[68:71], v[192:195], v[164:167], v[68:71]\n"
    "s_waitcnt lgkmcnt(4)\n"
    "v_mfma_f32_16x16x32_bf16 v[72:75], v[192:195], v[168:171], v[72:75]\n"
    "ds_write_b128 v26, v[200:203] offset:14336\n"
    "s_waitcnt lgkmcnt(4)\n"
    "v_mfma_f32_16x16x32_bf16 v[76:79], v[192:195], v[172:175], v[76:79]\n"
    "v_lshlrev_b32 v14, 16, v204\n"
    "buffer_load_dwordx4 v[180:183], v252, s[20:23], s96 offen sc1\n"
    "s_waitcnt vmcnt(15)\n"
    "v_mfma_f32_16x16x32_bf16 v[80:83], v[52:55], v[160:163], v[80:83]\n"
    "ds_read_b128 v[148:151], v1 offset:28672\n"
    "v_and_b32 v15, 0xffff0000, v204\n"
    "v_mfma_f32_16x16x32_bf16 v[84:87], v[52:55], v[164:167], v[84:87]\n"
    "v_lshlrev_b32 v16, 16, v205\n"
    "v_mfma_f32_16x16x32_bf16 v[88:91], v[52:55], v[168:171], v[88:91]\n"
    "v_mfma_f32_16x16x32_bf16 v[92:95], v[52:55], v[172:175], v[92:95]\n"
    "v_and_b32 v17, 0xffff0000, v205\n"
    "buffer_load_dwordx4 v[44:47], v253, s[20:23], s96 offen sc1\n"
    "s_waitcnt vmcnt(15)\n"
    "v_mfma_f32_16x16x32_bf16 v[96:99], v[56:59], v[160:163], v[96:99]\n"
    "ds_read_b128 v[152:155], v7 offset:10240\n"
    "v_fma_f32 v14, v14, v12, v13\n"
    "v_mfma_f32_16x16x32_bf16 v[100:103], v[56:59], v[164:167], v[100:103]\n"
    "v_fma_f32 v15, v15, v12, v13\n"
    "v_mfma_f32_16x16x32_bf16 v[104:107], v[56:59], v[168:171], v[104:107]\n"
    "v_fma_f32 v16, v16, v12, v13\n"
    "v_mfma_f32_16x16x32_bf16 v[108:111], v[56:59], v[172:175], v[108:111]\n"
    "buffer_load_dwordx4 v[48:51], v254, s[20:23], s96 offen sc1\n"
    "s_waitcnt vmcnt(15)\n"
    "v_mfma_f32_16x16x32_bf16 v[112:115], v[240:243], v[160:163], v[112:115]\n"
    "ds_read_b128 v[156:159], v7 offset:28672\n"
    "v_fma_f32 v17, v17, v12, v13\n"
    "v_mfma_f32_16x16x32_bf16 v[116:119], v[240:243], v[164:167], v[116:119]\n"
    "v_fma_f32 v14, v14, v208, v212\n"
    "v_mfma_f32_16x16x32_bf16 v[120:123], v[240:243], v[168:171], v[120:123]\n"
    "v_fma_f32 v15, v15, v209, v213\n"
    "v_mfma_f32_16x16x32_bf16 v[124:127], v[240:243], v[172:175], v[124:127]\n"
    "s_add_u32 s97, s97, 4096\n"
    "buffer_load_dwordx4 v[184:187], v1, s[20:23], s97 offen sc1\n"
    "s_waitcnt vmcnt(15)\n"
    "v_mfma_f32_16x16x32_bf16 v[128:131], v[60:63], v[160:163], v[128:131]\n"
    "v_fma_f32 v16, v16, v210, v214\n"
    "v_mfma_f32_16x16x32_bf16 v[132:135], v[60:63], v[164:167], v[132:135]\n"
    "v_fma_f32 v17, v17, v211, v215\n"
    "v_mfma_f32_16x16x32_bf16 v[136:139], v[60:63], v[168:171], v[136:139]\n"
    "v_cvt_pk_bf16_f32 v204, v14, v15\n"
    "v_mfma_f32_16x16x32_bf16 v[140:143], v[60:63], v[172:175], v[140:143]\n"
    "v_cvt_pk_bf16_f32 v205, v16, v17\n"
    "buffer_load_dwordx4 v[188:191], v1, s[20:23], s96 offen offset:1024 sc1\n"
    "s_waitcnt vmcnt(15) lgkmcnt(4)\n"
    "v_mfma_f32_16x16x32_bf16 v[64:67], v[224:227], v[144:147], v[64:67]\n"
    "ds_read_b128 v[160:163], v1 offset:11264\n"
    "s_waitcnt lgkmcnt(3)\n"
    "v_mfma_f32_16x16x32_bf16 v[68:71], v[224:227], v[148:151], v[68:71]\n"
    "v_lshlrev_b32 v18, 16, v206\n"
    "s_waitcnt lgkmcnt(2)\n"
    "v_mfma_f32_16x16x32_bf16 v[72:75], v[224:227], v[152:155], v[72:75]\n"
    "v_and_b32 v19, 0xffff0000, v206\n"
    "s_waitcnt lgkmcnt(1)\n"
    "v_mfma_f32_16x16x32_bf16 v[76:79], v[224:227], v[156:159], v[76:79]\n"
    "v_lshlrev_b32 v20, 16, v207\n"
    "buffer_load_dwordx4 v[192:195], v252, s[20:23], s96 offen offset:1024 sc1\n"
    "s_waitcnt vmcnt(15)\n"
    "v_mfma_f32_16x16x32_bf16 v[80:83], v[228:231], v[144:147], v[80:83]\n"
    "ds_read_b128 v[164:167], v1 offset:29696\n"
    "v_and_b32 v21, 0xffff0000, v207\n"
    "v_mfma_f32_16x16x32_bf16 v[84:87], v[228:231], v[148:151], v[84:87]\n"
    "v_mfma_f32_16x16x32_bf16 v[88:91], v[228:231], v[152:155], v[88:91]\n"
    "v_fma_f32 v18, v18, v12, v13\n"
    "v_mfma_f32_16x16x32_bf16 v[92:95], v[228:231], v[156:159], v[92:95]\n"
    "v_fma_f32 v19, v19, v12, v13\n"
    "buffer_load_dwordx4 v[52:55], v253, s[20:23], s96 offen offset:1024 sc1\n"
    "s_waitcnt vmcnt(15)\n"
    "v_mfma_f32_16x16x32_bf16 v[96:99], v[232:235], v[144:147], v[96:99]\n"
    "ds_read_b128 v[168:171], v7 offset:11264\n"
    "v_fma_f32 v20, v20, v12, v13\n"
    "v_mfma_f32_16x16x32_bf16 v[100:103], v[232:235], v[148:151], v[100:103]\n"
    "v_fma_f32 v21, v21, v12, v13\n"
    "v_mfma_f32_16x16x32_bf16 v[104:107], v[232:235], v[152:155], v[104:107]\n"
    "v_mfma_f32_16x16x32_bf16 v[108:111], v[232:235], v[156:159], v[108:111]\n"
    "v_fma_f32 v18, v18, v216, v220\n"
    "buffer_load_dwordx4 v[56:59], v254, s[20:23], s96 offen offset:1024 sc1\n"
    "s_waitcnt vmcnt(15)\n"
    "v_mfma_f32_16x16x32_bf16 v[112:115], v[244:247], v[144:147], v[112:115]\n"
    "ds_read_b128 v[172:175], v7 offset:29696\n"
    "v_fma_f32 v19, v19, v217, v221\n"
    "v_mfma_f32_16x16x32_bf16 v[116:119], v[244:247], v[148:151], v[116:119]\n"
    "v_fma_f32 v20, v20, v218, v222\n"
    "v_mfma_f32_16x16x32_bf16 v[120:123], v[244:247], v[152:155], v[120:123]\n"
    "v_fma_f32 v21, v21, v219, v223\n"
    "v_mfma_f32_16x16x32_bf16 v[124:127], v[244:247], v[156:159], v[124:127]\n"
    "buffer_load_dwordx4 v[240:243], v1, s[20:23], s97 offen offset:1024 sc1\n"
    "s_waitcnt vmcnt(15)\n"
    "v_mfma_f32_16x16x32_bf16 v[128:131], v[248:251], v[144:147], v[128:131]\n"
    "v_cvt_pk_bf16_f32 v206, v18, v19\n"
    "v_mfma_f32_16x16x32_bf16 v[132:135], v[248:251], v[148:151], v[132:135]\n"
    "v_cvt_pk_bf16_f32 v207, v20, v21\n"
    "v_mfma_f32_16x16x32_bf16 v[136:139], v[248:251], v[152:155], v[136:139]\n"
    "ds_write_b128 v26, v[204:207] offset:16384\n"
    "v_mfma_f32_16x16x32_bf16 v[140:143], v[248:251], v[156:159], v[140:143]\n"
    "buffer_load_dwordx4 v[60:63], v1, s[20:23], s96 offen offset:2048 sc1\n"
    "s_waitcnt lgkmcnt(0)\n"
    "s_barrier\n"
    "s_waitcnt vmcnt(15)\n"
    "v_mfma_f32_16x16x32_bf16 v[64:67], v[28:31], v[160:163], v[64:67]\n"
    "ds_read_b128 v[144:147], v1 offset:12288\n"
    "v_mfma_f32_16x16x32_bf16 v[68:71], v[28:31], v[164:167], v[68:71]\n"
    "v_mfma_f32_16x16x32_bf16 v[72:75], v[28:31], v[168:171], v[72:75]\n"
    "v_mfma_f32_16x16x32_bf16 v[76:79], v[28:31], v[172:175], v[76:79]\n"
    "buffer_load_dwordx4 v[224:227], v252, s[20:23], s96 offen offset:2048 sc1\n"
    "s_waitcnt vmcnt(15)\n"
    "v_mfma_f32_16x16x32_bf16 v[80:83], v[32:35], v[160:163], v[80:83]\n"
    "ds_read_b128 v[148:151], v1 offset:30720\n"
    "v_mfma_f32_16x16x32_bf16 v[84:87], v[32:35], v[164:167], v[84:87]\n"
    "v_mfma_f32_16x16x32_bf16 v[88:91], v[32:35], v[168:171], v[88:91]\n"
    "v_mfma_f32_16x16x32_bf16 v[92:95], v[32:35], v[172:175], v[92:95]\n"
    "buffer_load_dwordx4 v[228:231], v253, s[20:23], s96 offen offset:2048 sc1\n"
    "s_waitcnt vmcnt(15)\n"
    "v_mfma_f32_16x16x32_bf16 v[96:99], v[36:39], v[160:163], v[96:99]\n"
    "ds_read_b128 v[152:155], v7 offset:12288\n"
    "v_mfma_f32_16x16x32_bf16 v[100:103], v[36:39], v[164:167], v[100:103]\n"
    "v_mfma_f32_16x16x32_bf16 v[104:107], v[36:39], v[168:171], v[104:107]\n"
    "v_mfma_f32_16x16x32_bf16 v[108:111], v[36:39], v[172:175], v[108:111]\n"
    "buffer_load_dwordx4 v[232:235], v254, s[20:23], s96 offen offset:2048 sc1\n"
    "s_waitcnt vmcnt(15)\n"
    "v_mfma_f32_16x16x32_bf16 v[112:115], v[40:43], v[160:163], v[112:115]\n"
    "ds_read_b128 v[156:159], v7 offset:30720\n"
    "v_mfma_f32_16x16x32_bf16 v[116:119], v[40:43], v[164:167], v[116:119]\n"
    "v_mfma_f32_16x16x32_bf16 v[120:123], v[40:43], v[168:171], v[120:123]\n"
    "v_mfma_f32_16x16x32_bf16 v[124:127], v[40:43], v[172:175], v[124:127]\n"
    "buffer_load_dwordx4 v[244:247], v1, s[20:23], s97 offen offset:2048 sc1\n"
    "s_waitcnt vmcnt(15)\n"
    "v_mfma_f32_16x16x32_bf16 v[128:131], v[236:239], v[160:163], v[128:131]\n"
    "v_mfma_f32_16x16x32_bf16 v[132:135], v[236:239], v[164:167], v[132:135]\n"
    "v_mfma_f32_16x16x32_bf16 v[136:139], v[236:239], v[168:171], v[136:139]\n"
    "v_mfma_f32_16x16x32_bf16 v[140:143], v[236:239], v[172:175], v[140:143]\n"
    "buffer_load_dwordx4 v[248:251], v1, s[20:23], s96 offen offset:3072 sc1\n"
    "s_waitcnt vmcnt(15) lgkmcnt(3)\n"
    "v_mfma_f32_16x16x32_bf16 v[64:67], v[176:179], v[144:147], v[64:67]\n"
    "ds_read_b128 v[160:163], v1 offset:13312\n"
    "s_waitcnt lgkmcnt(3)\n"
    "v_mfma_f32_16x16x32_bf16 v[68:71], v[176:179], v[148:151], v[68:71]\n"
    "s_waitcnt lgkmcnt(2)\n"
    "v_mfma_f32_16x16x32_bf16 v[72:75], v[176:179], v[152:155], v[72:75]\n"
    "s_waitcnt lgkmcnt(1)\n"
    "v_mfma_f32_16x16x32_bf16 v[76:79], v[176:179], v[156:159], v[76:79]\n"
    "buffer_load_dwordx4 v[196:199], v252, s[20:23], s96 offen offset:3072 sc1\n"
    "s_waitcnt vmcnt(15)\n"
    "v_mfma_f32_16x16x32_bf16 v[80:83], v[180:183], v[144:147], v[80:83]\n"
    "ds_read_b128 v[164:167], v1 offset:31744\n"
    "v_mfma_f32_16x16x32_bf16 v[84:87], v[180:183], v[148:151], v[84:87]\n"
    "v_mfma_f32_16x16x32_bf16 v[88:91], v[180:183], v[152:155], v[88:91]\n"
    "v_mfma_f32_16x16x32_bf16 v[92:95], v[180:183], v[156:159], v[92:95]\n"
    "buffer_load_dwordx4 v[200:203], v253, s[20:23], s96 offen offset:3072 sc1\n"
    "s_waitcnt vmcnt(15)\n"
    "v_mfma_f32_16x16x32_bf16 v[96:99], v[44:47], v[144:147], v[96:99]\n"
    "ds_read_b128 v[168:171], v7 offset:13312\n"
    "v_mfma_f32_16x16x32_bf16 v[100:103], v[44:47], v[148:151], v[100:103]\n"
    "v_mfma_f32_16x16x32_bf16 v[104:107], v[44:47], v[152:155], v[104:107]\n"
    "v_mfma_f32_16x16x32_bf16 v[108:111], v[44:47], v[156:159], v[108:111]\n"
    "buffer_load_dwordx4 v[204:207], v254, s[20:23], s96 offen offset:3072 sc1\n"
    "s_waitcnt vmcnt(15)\n"
    "v_mfma_f32_16x16x32_bf16 v[112:115], v[48:51], v[144:147], v[112:115]\n"
    "ds_read_b128 v[172:175], v7 offset:31744\n"
    "v_mfma_f32_16x16x32_bf16 v[116:119], v[48:51], v[148:151], v[116:119]\n"
    "v_mfma_f32_16x16x32_bf16 v[120:123], v[48:51], v[152:155], v[120:123]\n"
    "v_mfma_f32_16x16x32_bf16 v[124:127], v[48:51], v[156:159], v[124:127]\n"
    "buffer_load_dwordx4 v[208:211], v1, s[20:23], s97 offen offset:3072 sc1\n"
    "s_waitcnt vmcnt(15)\n"
    "v_mfma_f32_16x16x32_bf16 v[128:131], v[184:187], v[144:147], v[128:131]\n"
    "v_mfma_f32_16x16x32_bf16 v[132:135], v[184:187], v[148:151], v[132:135]\n"
    "v_mfma_f32_16x16x32_bf16 v[136:139], v[184:187], v[152:155], v[136:139]\n"
    "v_mfma_f32_16x16x32_bf16 v[140:143], v[184:187], v[156:159], v[140:143]\n"
    "s_add_u32 s96, s96, 4096\n"
    "buffer_load_dwordx4 v[212:215], v1, s[20:23], s96 offen sc1\n"
    "s_waitcnt vmcnt(15) lgkmcnt(3)\n"
    "v_mfma_f32_16x16x32_bf16 v[64:67], v[188:191], v[160:163], v[64:67]\n"
    "ds_read_b128 v[144:147], v1 offset:14336\n"
    "s_waitcnt lgkmcnt(3)\n"
    "v_mfma_f32_16x16x32_bf16 v[68:71], v[188:191], v[164:167], v[68:71]\n"
    "s_waitcnt lgkmcnt(2)\n"
    "v_mfma_f32_16x16x32_bf16 v[72:75], v[188:191], v[168:171], v[72:75]\n"
    "s_waitcnt lgkmcnt(1)\n"
    "v_mfma_f32_16x16x32_bf16 v[76:79], v[188:191], v[172:175], v[76:79]\n"
    "buffer_load_dwordx4 v[216:219], v252, s[20:23], s96 offen sc1\n"
    "s_waitcnt vmcnt(15)\n"
    "v_mfma_f32_16x16x32_bf16 v[80:83], v[192:195], v[160:163], v[80:83]\n"
    "ds_read_b128 v[148:151], v1 offset:32768\n"
    "v_mfma_f32_16x16x32_bf16 v[84:87], v[192:195], v[164:167], v[84:87]\n"
    "v_mfma_f32_16x16x32_bf16 v[88:91], v[192:195], v[168:171], v[88:91]\n"
    "v_mfma_f32_16x16x32_bf16 v[92:95], v[192:195], v[172:175], v[92:95]\n"
    "buffer_load_dwordx4 v[220:223], v253, s[20:23], s96 offen sc1\n"
    "s_waitcnt vmcnt(15)\n"
    "v_mfma_f32_16x16x32_bf16 v[96:99], v[52:55], v[160:163], v[96:99]\n"
    "ds_read_b128 v[152:155], v7 offset:14336\n"
    "v_mfma_f32_16x16x32_bf16 v[100:103], v[52:55], v[164:167], v[100:103]\n"
    "v_mfma_f32_16x16x32_bf16 v[104:107], v[52:55], v[168:171], v[104:107]\n"
    "v_mfma_f32_16x16x32_bf16 v[108:111], v[52:55], v[172:175], v[108:111]\n"
    "buffer_load_dwordx4 v[28:31], v254, s[20:23], s96 offen sc1\n"
    "s_waitcnt vmcnt(15)\n"
    "v_mfma_f32_16x16x32_bf16 v[112:115], v[56:59], v[160:163], v[112:115]\n"
    "ds_read_b128 v[156:159], v7 offset:32768\n"
    "v_mfma_f32_16x16x32_bf16 v[116:119], v[56:59], v[164:167], v[116:119]\n"
    "v_mfma_f32_16x16x32_bf16 v[120:123], v[56:59], v[168:171], v[120:123]\n"
    "v_mfma_f32_16x16x32_bf16 v[124:127], v[56:59], v[172:175], v[124:127]\n"
    "s_add_u32 s97, s97, 4096\n"
    "buffer_load_dwordx4 v[32:35], v1, s[20:23], s97 offen sc1\n"
    "s_waitcnt vmcnt(15)\n"
    "v_mfma_f32_16x16x32_bf16 v[128:131], v[240:243], v[160:163], v[128:131]\n"
    "v_mfma_f32_16x16x32_bf16 v[132:135], v[240:243], v[164:167], v[132:135]\n"
    "v_mfma_f32_16x16x32_bf16 v[136:139], v[240:243], v[168:171], v[136:139]\n"
    "v_mfma_f32_16x16x32_bf16 v[140:143], v[240:243], v[172:175], v[140:143]\n"
    "buffer_load_dwordx4 v[36:39], v1, s[20:23], s96 offen offset:1024 sc1\n"
    "s_lshl_b32 s45, s38, 2\n"
    "s_add_u32 s45, s45, s39\n"
    "s_mul_i32 s45, s45, 18432\n"
    "s_add_u32 s45, s45, 10694656\n"
    "s_mul_i32 s46, s37, 2304\n"
    "s_add_u32 s46, s46, s45\n"
    "s_mul_i32 s45, s36, 1024\n"
    "s_add_u32 s45, s45, 147456\n"
    "s_mov_b32 m0, s45\n"
    "s_nop 0\n"
    "buffer_load_dwordx4 v1, s[20:23], s46 offen sc1 lds\n"
    "s_mul_i32 s45, s39, 256\n"
    "s_add_u32 s45, s45, 153600\n"
    "s_mov_b32 m0, s45\n"
    "s_mov_b64 exec, 0xffff\n"
    "buffer_load_dwordx4 v1, s[20:23], s46 offen offset:2048 sc1 lds\n"
    "s_mov_b64 exec, -1\n"
    "s_waitcnt vmcnt(17) lgkmcnt(3)\n"
    "v_mfma_f32_16x16x32_bf16 v[64:67], v[60:63], v[144:147], v[64:67]\n"
    "ds_read_b128 v[160:163], v1 offset:15360\n"
    "s_waitcnt lgkmcnt(3)\n"
    "v_mfma_f32_16x16x32_bf16 v[68:71], v[60:63], v[148:151], v[68:71]\n"
    "s_waitcnt lgkmcnt(2)\n"
    "v_mfma_f32_16x16x32_bf16 v[72:75], v[60:63], v[152:155], v[72:75]\n"
    "s_waitcnt lgkmcnt(1)\n"
    "v_mfma_f32_16x16x32_bf16 v[76:79], v[60:63], v[156:159], v[76:79]\n"
    "buffer_load_dwordx4 v[40:43], v252, s[20:23], s96 offen offset:1024 sc1\n"
    "s_waitcnt vmcnt(17)\n"
    "v_mfma_f32_16x16x32_bf16 v[80:83], v[224:227], v[144:147], v[80:83]\n"
    "ds_read_b128 v[164:167], v1 offset:33792\n"
    "v_mfma_f32_16x16x32_bf16 v[84:87], v[224:227], v[148:151], v[84:87]\n"
    "v_mfma_f32_16x16x32_bf16 v[88:91], v[224:227], v[152:155], v[88:91]\n"
    "v_mfma_f32_16x16x32_bf16 v[92:95], v[224:227], v[156:159], v[92:95]\n"
    "buffer_load_dwordx4 v[236:239], v253, s[20:23], s96 offen offset:1024 sc1\n"
    "s_waitcnt vmcnt(17)\n"
    "v_mfma_f32_16x16x32_bf16 v[96:99], v[228:231], v[144:147], v[96:99]\n"
    "ds_read_b128 v[168:171], v7 offset:15360\n"
    "v_mfma_f32_16x16x32_bf16 v[100:103], v[228:231], v[148:151], v[100:103]\n"
    "v_mfma_f32_16x16x32_bf16 v[104:107], v[228:231], v[152:155], v[104:107]\n"
    "v_mfma_f32_16x16x32_bf16 v[108:111], v[228:231], v[156:159], v[108:111]\n"
    "buffer_load_dwordx4 v[176:179], v254, s[20:23], s96 offen offset:1024 sc1\n"
    "s_waitcnt vmcnt(17)\n"
    "v_mfma_f32_16x16x32_bf16 v[112:115], v[232:235], v[144:147], v[112:115]\n"
    "ds_read_b128 v[172:175], v7 offset:33792\n"
    "v_mfma_f32_16x16x32_bf16 v[116:119], v[232:235], v[148:151], v[116:119]\n"
    "v_mfma_f32_16x16x32_bf16 v[120:123], v[232:235], v[152:155], v[120:123]\n"
    "v_mfma_f32_16x16x32_bf16 v[124:127], v[232:235], v[156:159], v[124:127]\n"
    "buffer_load_dwordx4 v[180:183], v1, s[20:23], s97 offen offset:1024 sc1\n"
    "s_waitcnt vmcnt(17)\n"
    "v_mfma_f32_16x16x32_bf16 v[128:131], v[244:247], v[144:147], v[128:131]\n"
    "v_mfma_f32_16x16x32_bf16 v[132:135], v[244:247], v[148:151], v[132:135]\n"
    "v_mfma_f32_16x16x32_bf16 v[136:139], v[244:247], v[152:155], v[136:139]\n"
    "v_mfma_f32_16x16x32_bf16 v[140:143], v[244:247], v[156:159], v[140:143]\n"
    "buffer_load_dwordx4 v[44:47], v1, s[20:23], s58 offen sc1\n"
    "s_waitcnt vmcnt(17) lgkmcnt(3)\n"
    "v_mfma_f32_16x16x32_bf16 v[64:67], v[248:251], v[160:163], v[64:67]\n"
    "ds_read_b128 v[144:147], v1 offset:16384\n"
    "s_waitcnt lgkmcnt(3)\n"
    "v_mfma_f32_16x16x32_bf16 v[68:71], v[248:251], v[164:167], v[68:71]\n"
    "s_waitcnt lgkmcnt(2)\n"
    "v_mfma_f32_16x16x32_bf16 v[72:75], v[248:251], v[168:171], v[72:75]\n"
    "s_waitcnt lgkmcnt(1)\n"
    "v_mfma_f32_16x16x32_bf16 v[76:79], v[248:251], v[172:175], v[76:79]\n"
    "buffer_load_dwordx4 v[48:51], v252, s[20:23], s58 offen sc1\n"
    "s_waitcnt vmcnt(17)\n"
    "v_mfma_f32_16x16x32_bf16 v[80:83], v[196:199], v[160:163], v[80:83]\n"
    "ds_read_b128 v[148:151], v1 offset:34816\n"
    "v_mfma_f32_16x16x32_bf16 v[84:87], v[196:199], v[164:167], v[84:87]\n"
    "v_mfma_f32_16x16x32_bf16 v[88:91], v[196:199], v[168:171], v[88:91]\n"
    "v_mfma_f32_16x16x32_bf16 v[92:95], v[196:199], v[172:175], v[92:95]\n"
    "buffer_load_dwordx4 v[184:187], v253, s[20:23], s58 offen sc1\n"
    "s_waitcnt vmcnt(17)\n"
    "v_mfma_f32_16x16x32_bf16 v[96:99], v[200:203], v[160:163], v[96:99]\n"
    "ds_read_b128 v[152:155], v7 offset:16384\n"
    "v_mfma_f32_16x16x32_bf16 v[100:103], v[200:203], v[164:167], v[100:103]\n"
    "v_mfma_f32_16x16x32_bf16 v[104:107], v[200:203], v[168:171], v[104:107]\n"
    "v_mfma_f32_16x16x32_bf16 v[108:111], v[200:203], v[172:175], v[108:111]\n"
    "buffer_load_dwordx4 v[188:191], v254, s[20:23], s58 offen sc1\n"
    "s_waitcnt vmcnt(17)\n"
    "v_mfma_f32_16x16x32_bf16 v[112:115], v[204:207], v[160:163], v[112:115]\n"
    "ds_read_b128 v[156:159], v7 offset:34816\n"
    "v_mfma_f32_16x16x32_bf16 v[116:119], v[204:207], v[164:167], v[116:119]\n"
    "v_mfma_f32_16x16x32_bf16 v[120:123], v[204:207], v[168:171], v[120:123]\n"
    "v_mfma_f32_16x16x32_bf16 v[124:127], v[204:207], v[172:175], v[124:127]\n"
    "buffer_load_dwordx4 v[192:195], v1, s[20:23], s58 offen offset:1024 sc1\n"
    "s_waitcnt vmcnt(17)\n"
    "v_mfma_f32_16x16x32_bf16 v[128:131], v[208:211], v[160:163], v[128:131]\n"
    "v_mfma_f32_16x16x32_bf16 v[132:135], v[208:211], v[164:167], v[132:135]\n"
    "v_mfma_f32_16x16x32_bf16 v[136:139], v[208:211], v[168:171], v[136:139]\n"
    "v_mfma_f32_16x16x32_bf16 v[140:143], v[208:211], v[172:175], v[140:143]\n"
    "buffer_load_dwordx4 v[52:55], v252, s[20:23], s58 offen offset:1024 sc1\n"
    "s_waitcnt vmcnt(17) lgkmcnt(3)\n"
    "v_mfma_f32_16x16x32_bf16 v[64:67], v[212:215], v[144:147], v[64:67]\n"
    "ds_read_b128 v[160:163], v1 offset:17408\n"
    "s_waitcnt lgkmcnt(3)\n"
    "v_mfma_f32_16x16x32_bf16 v[68:71], v[212:215], v[148:151], v[68:71]\n"
    "s_waitcnt lgkmcnt(2)\n"
    "v_mfma_f32_16x16x32_bf16 v[72:75], v[212:215], v[152:155], v[72:75]\n"
    "s_waitcnt lgkmcnt(1)\n"
    "v_mfma_f32_16x16x32_bf16 v[76:79], v[212:215], v[156:159], v[76:79]\n"
    "buffer_load_dwordx4 v[56:59], v253, s[20:23], s58 offen offset:1024 sc1\n"
    "s_waitcnt vmcnt(17)\n"
    "v_mfma_f32_16x16x32_bf16 v[80:83], v[216:219], v[144:147], v[80:83]\n"
    "ds_read_b128 v[164:167], v1 offset:35840\n"
    "v_mfma_f32_16x16x32_bf16 v[84:87], v[216:219], v[148:151], v[84:87]\n"
    "v_mfma_f32_16x16x32_bf16 v[88:91], v[216:219], v[152:155], v[88:91]\n"
    "v_mfma_f32_16x16x32_bf16 v[92:95], v[216:219], v[156:159], v[92:95]\n"
    "buffer_load_dwordx4 v[240:243], v254, s[20:23], s58 offen offset:1024 sc1\n"
    "s_waitcnt vmcnt(17)\n"
    "v_mfma_f32_16x16x32_bf16 v[96:99], v[220:223], v[144:147], v[96:99]\n"
    "ds_read_b128 v[168:171], v7 offset:17408\n"
    "v_mfma_f32_16x16x32_bf16 v[100:103], v[220:223], v[148:151], v[100:103]\n"
    "v_mfma_f32_16x16x32_bf16 v[104:107], v[220:223], v[152:155], v[104:107]\n"
    "v_mfma_f32_16x16x32_bf16 v[108:111], v[220:223], v[156:159], v[108:111]\n"
    "buffer_load_dwordx4 v[60:63], v1, s[20:23], s58 offen offset:2048 sc1\n"
    "s_waitcnt vmcnt(17)\n"
    "v_mfma_f32_16x16x32_bf16 v[112:115], v[28:31], v[144:147], v[112:115]\n"
    "ds_read_b128 v[172:175], v7 offset:35840\n"
    "v_mfma_f32_16x16x32_bf16 v[116:119], v[28:31], v[148:151], v[116:119]\n"
    "v_mfma_f32_16x16x32_bf16 v[120:123], v[28:31], v[152:155], v[120:123]\n"
    "v_mfma_f32_16x16x32_bf16 v[124:127], v[28:31], v[156:159], v[124:127]\n"
    "buffer_load_dwordx4 v[224:227], v252, s[20:23], s58 offen offset:2048 sc1\n"
    "s_waitcnt vmcnt(17)\n"
    "v_mfma_f32_16x16x32_bf16 v[128:131], v[32:35], v[144:147], v[128:131]\n"
    "v_mfma_f32_16x16x32_bf16 v[132:135], v[32:35], v[148:151], v[132:135]\n"
    "v_mfma_f32_16x16x32_bf16 v[136:139], v[32:35], v[152:155], v[136:139]\n"
    "v_mfma_f32_16x16x32_bf16 v[140:143], v[32:35], v[156:159], v[140:143]\n"
    "buffer_load_dwordx4 v[228:231], v253, s[20:23], s58 offen offset:2048 sc1\n"
    "s_waitcnt vmcnt(17) lgkmcnt(3)\n"
    "v_mfma_f32_16x16x32_bf16 v[64:67], v[36:39], v[160:163], v[64:67]\n"
    "s_waitcnt lgkmcnt(2)\n"
    "v_mfma_f32_16x16x32_bf16 v[68:71], v[36:39], v[164:167], v[68:71]\n"
    "s_waitcnt lgkmcnt(1)\n"
    "v_mfma_f32_16x16x32_bf16 v[72:75], v[36:39], v[168:171], v[72:75]\n"
    "s_waitcnt lgkmcnt(0)\n"
    "v_mfma_f32_16x16x32_bf16 v[76:79], v[36:39], v[172:175], v[76:79]\n"
    "buffer_load_dwordx4 v[232:235], v254, s[20:23], s58 offen offset:2048 sc1\n"
    "s_waitcnt vmcnt(15)\n"
    "v_mfma_f32_16x16x32_bf16 v[80:83], v[40:43], v[160:163], v[80:83]\n"
    "v_mfma_f32_16x16x32_bf16 v[84:87], v[40:43], v[164:167], v[84:87]\n"
    "v_mfma_f32_16x16x32_bf16 v[88:91], v[40:43], v[168:171], v[88:91]\n"
    "v_mfma_f32_16x16x32_bf16 v[92:95], v[40:43], v[172:175], v[92:95]\n"
    "buffer_load_dwordx4 v[244:247], v1, s[20:23], s58 offen offset:3072 sc1\n"
    "s_waitcnt vmcnt(15)\n"
    "v_mfma_f32_16x16x32_bf16 v[96:99], v[236:239], v[160:163], v[96:99]\n"
    "v_mfma_f32_16x16x32_bf16 v[100:103], v[236:239], v[164:167], v[100:103]\n"
    "v_mfma_f32_16x16x32_bf16 v[104:107], v[236:239], v[168:171], v[104:107]\n"
    "v_mfma_f32_16x16x32_bf16 v[108:111], v[236:239], v[172:175], v[108:111]\n"
    "buffer_load_dwordx4 v[248:251], v252, s[20:23], s58 offen offset:3072 sc1\n"
    "s_waitcnt vmcnt(15)\n"
    "v_mfma_f32_16x16x32_bf16 v[112:115], v[176:179], v[160:163], v[112:115]\n"
    "v_mfma_f32_16x16x32_bf16 v[116:119], v[176:179], v[164:167], v[116:119]\n"
    "v_mfma_f32_16x16x32_bf16 v[120:123], v[176:179], v[168:171], v[120:123]\n"
    "v_mfma_f32_16x16x32_bf16 v[124:127], v[176:179], v[172:175], v[124:127]\n"
    "buffer_load_dwordx4 v[196:199], v253, s[20:23], s58 offen offset:3072 sc1\n"
    "s_waitcnt vmcnt(15)\n"
    "v_mfma_f32_16x16x32_bf16 v[128:131], v[180:183], v[160:163], v[128:131]\n"
    "v_mfma_f32_16x16x32_bf16 v[132:135], v[180:183], v[164:167], v[132:135]\n"
    "v_mfma_f32_16x16x32_bf16 v[136:139], v[180:183], v[168:171], v[136:139]\n"
    "v_mfma_f32_16x16x32_bf16 v[140:143], v[180:183], v[172:175], v[140:143]\n"
    "buffer_load_dwordx4 v[200:203], v254, s[20:23], s58 offen offset:3072 sc1\n"
    "s_setprio 0\n"
    "v_exp_f32 v14, v64\n"
    "v_exp_f32 v15, v65\n"
    "v_exp_f32 v16, v66\n"
    "v_exp_f32 v17, v67\n"
    "v_add_f32 v14, 1.0, v14\n"
    "v_add_f32 v15, 1.0, v15\n"
    "v_add_f32 v16, 1.0, v16\n"
    "v_add_f32 v17, 1.0, v17\n"
    "v_rcp_f32 v14, v14\n"
    "v_rcp_f32 v15, v15\n"
    "v_rcp_f32 v16, v16\n"
    "v_rcp_f32 v17, v17\n"
    "v_mul_f32 v64, v64, v14\n"
    "v_mul_f32 v65, v65, v15\n"
    "v_mul_f32 v66, v66, v16\n"
    "v_mul_f32 v67, v67, v17\n"
    "v_cvt_pk_bf16_f32 v64, v64, v65\n"
    "v_cvt_pk_bf16_f32 v65, v66, v67\n"
    "v_exp_f32 v18, v80\n"
    "v_exp_f32 v19, v81\n"
    "v_exp_f32 v20, v82\n"
    "v_exp_f32 v21, v83\n"
    "v_add_f32 v18, 1.0, v18\n"
    "v_add_f32 v19, 1.0, v19\n"
    "v_add_f32 v20, 1.0, v20\n"
    "v_add_f32 v21, 1.0, v21\n"
    "v_rcp_f32 v18, v18\n"
    "v_rcp_f32 v19, v19\n"
    "v_rcp_f32 v20, v20\n"
    "v_rcp_f32 v21, v21\n"
    "v_mul_f32 v80, v80, v18\n"
    "s_add_u32 s58, s58, 4096\n"
    "buffer_load_dwordx4 v[204:207], v1, s[20:23], s58 offen sc1\n"
    "v_mul_f32 v81, v81, v19\n"
    "v_mul_f32 v82, v82, v20\n"
    "v_mul_f32 v83, v83, v21\n"
    "v_cvt_pk_bf16_f32 v66, v80, v81\n"
    "v_cvt_pk_bf16_f32 v67, v82, v83\n"
    "ds_write_b128 v22, v[64:67]\n"
    "v_exp_f32 v14, v68\n"
    "v_exp_f32 v15, v69\n"
    "v_exp_f32 v16, v70\n"
    "v_exp_f32 v17, v71\n"
    "v_add_f32 v14, 1.0, v14\n"
    "v_add_f32 v15, 1.0, v15\n"
    "v_add_f32 v16, 1.0, v16\n"
    "v_add_f32 v17, 1.0, v17\n"
    "v_rcp_f32 v14, v14\n"
    "v_rcp_f32 v15, v15\n"
    "v_rcp_f32 v16, v16\n"
    "v_rcp_f32 v17, v17\n"
    "v_mul_f32 v68, v68, v14\n"
    "v_mul_f32 v69, v69, v15\n"
    "v_mul_f32 v70, v70, v16\n"
    "v_mul_f32 v71, v71, v17\n"
    "v_cvt_pk_bf16_f32 v68, v68, v69\n"
    "v_cvt_pk_bf16_f32 v69, v70, v71\n"
    "v_exp_f32 v18, v84\n"
    "v_exp_f32 v19, v85\n"
    "v_exp_f32 v20, v86\n"
    "v_exp_f32 v21, v87\n"
    "v_add_f32 v18, 1.0, v18\n"
    "v_add_f32 v19, 1.0, v19\n"
    "v_add_f32 v20, 1.0, v20\n"
    "buffer_load_dwordx4 v[208:211], v252, s[20:23], s58 offen sc1\n"
    "v_add_f32 v21, 1.0, v21\n"
    "v_rcp_f32 v18, v18\n"
    "v_rcp_f32 v19, v19\n"
    "v_rcp_f32 v20, v20\n"
    "v_rcp_f32 v21, v21\n"
    "v_mul_f32 v84, v84, v18\n"
    "v_mul_f32 v85, v85, v19\n"
    "v_mul_f32 v86, v86, v20\n"
    "v_mul_f32 v87, v87, v21\n"
    "v_cvt_pk_bf16_f32 v70, v84, v85\n"
    "v_cvt_pk_bf16_f32 v71, v86, v87\n"
    "ds_write_b128 v22, v[68:71] offset:1024\n"
    "v_exp_f32 v14, v72\n"
    "v_exp_f32 v15, v73\n"
    "v_exp_f32 v16, v74\n"
    "v_exp_f32 v17, v75\n"
    "v_add_f32 v14, 1.0, v14\n"
    "v_add_f32 v15, 1.0, v15\n"
    "v_add_f32 v16, 1.0, v16\n"
    "v_add_f32 v17, 1.0, v17\n"
    "v_rcp_f32 v14, v14\n"
    "v_rcp_f32 v15, v15\n"
    "v_rcp_f32 v16, v16\n"
    "v_rcp_f32 v17, v17\n"
    "v_mul_f32 v72, v72, v14\n"
    "v_mul_f32 v73, v73, v15\n"
    "v_mul_f32 v74, v74, v16\n"
    "v_mul_f32 v75, v75, v17\n"
    "v_cvt_pk_bf16_f32 v72, v72, v73\n"
    "v_cvt_pk_bf16_f32 v73, v74, v75\n"
    "v_exp_f32 v18, v88\n"
    "buffer_load_dwordx4 v[212:215], v253, s[20:23], s58 offen sc1\n"
    "v_exp_f32 v19, v89\n"
    "v_exp_f32 v20, v90\n"
    "v_exp_f32 v21, v91\n"
    "v_add_f32 v18, 1.0, v18\n"
    "v_add_f32 v19, 1.0, v19\n"
    "v_add_f32 v20, 1.0, v20\n"
    "v_add_f32 v21, 1.0, v21\n"
    "v_rcp_f32 v18, v18\n"
    "v_rcp_f32 v19, v19\n"
    "v_rcp_f32 v20, v20\n"
    "v_rcp_f32 v21, v21\n"
    "v_mul_f32 v88, v88, v18\n"
    "v_mul_f32 v89, v89, v19\n"
    "v_mul_f32 v90, v90, v20\n"
    "v_mul_f32 v91, v91, v21\n"
    "v_cvt_pk_bf16_f32 v74, v88, v89\n"
    "v_cvt_pk_bf16_f32 v75, v90, v91\n"
    "ds_write_b128 v22, v[72:75] offset:2048\n"
    "v_exp_f32 v14, v76\n"
    "v_exp_f32 v15, v77\n"
    "v_exp_f32 v16, v78\n"
    "v_exp_f32 v17, v79\n"
    "v_add_f32 v14, 1.0, v14\n"
    "v_add_f32 v15, 1.0, v15\n"
    "v_add_f32 v16, 1.0, v16\n"
    "v_add_f32 v17, 1.0, v17\n"
    "v_rcp_f32 v14, v14\n"
    "v_rcp_f32 v15, v15\n"
    "v_rcp_f32 v16, v16\n"
    "v_rcp_f32 v17, v17\n"
    "v_mul_f32 v76, v76, v14\n"
    "buffer_load_dwordx4 v[216:219], v254, s[20:23], s58 offen sc1\n"
    "v_mul_f32 v77, v77, v15\n"
    "v_mul_f32 v78, v78, v16\n"
    "v_mul_f32 v79, v79, v17\n"
    "v_cvt_pk_bf16_f32 v76, v76, v77\n"
    "v_cvt_pk_bf16_f32 v77, v78, v79\n"
    "v_exp_f32 v18, v92\n"
    "v_exp_f32 v19, v93\n"
    "v_exp_f32 v20, v94\n"
    "v_exp_f32 v21, v95\n"
    "v_add_f32 v18, 1.0, v18\n"
    "v_add_f32 v19, 1.0, v19\n"
    "v_add_f32 v20, 1.0, v20\n"
    "v_add_f32 v21, 1.0, v21\n"
    "v_rcp_f32 v18, v18\n"
    "v_rcp_f32 v19, v19\n"
    "v_rcp_f32 v20, v20\n"
    "v_rcp_f32 v21, v21\n"
    "v_mul_f32 v92, v92, v18\n"
    "v_mul_f32 v93, v93, v19\n"
    "v_mul_f32 v94, v94, v20\n"
    "v_mul_f32 v95, v95, v21\n"
    "v_cvt_pk_bf16_f32 v78, v92, v93\n"
    "v_cvt_pk_bf16_f32 v79, v94, v95\n"
    "ds_write_b128 v22, v[76:79] offset:3072\n"
    "v_exp_f32 v14, v96\n"
    "v_exp_f32 v15, v97\n"
    "v_exp_f32 v16, v98\n"
    "v_exp_f32 v17, v99\n"
    "v_add_f32 v14, 1.0, v14\n"
    "v_add_f32 v15, 1.0, v15\n"
    "v_add_f32 v16, 1.0, v16\n"
    "buffer_load_dwordx4 v[220:223], v1, s[20:23], s58 offen offset:1024 sc1\n"
    "v_add_f32 v17, 1.0, v17\n"
    "v_rcp_f32 v14, v14\n"
    "v_rcp_f32 v15, v15\n"
    "v_rcp_f32 v16, v16\n"
    "v_rcp_f32 v17, v17\n"
    "v_mul_f32 v96, v96, v14\n"
    "v_mul_f32 v97, v97, v15\n"
    "v_mul_f32 v98, v98, v16\n"
    "v_mul_f32 v99, v99, v17\n"
    "v_cvt_pk_bf16_f32 v96, v96, v97\n"
    "v_cvt_pk_bf16_f32 v97, v98, v99\n"
    "v_exp_f32 v18, v112\n"
    "v_exp_f32 v19, v113\n"
    "v_exp_f32 v20, v114\n"
    "v_exp_f32 v21, v115\n"
    "v_add_f32 v18, 1.0, v18\n"
    "v_add_f32 v19, 1.0, v19\n"
    "v_add_f32 v20, 1.0, v20\n"
    "v_add_f32 v21, 1.0, v21\n"
    "v_rcp_f32 v18, v18\n"
    "v_rcp_f32 v19, v19\n"
    "v_rcp_f32 v20, v20\n"
    "v_rcp_f32 v21, v21\n"
    "v_mul_f32 v112, v112, v18\n"
    "v_mul_f32 v113, v113, v19\n"
    "v_mul_f32 v114, v114, v20\n"
    "v_mul_f32 v115, v115, v21\n"
    "v_cvt_pk_bf16_f32 v98, v112, v113\n"
    "v_cvt_pk_bf16_f32 v99, v114, v115\n"
    "ds_write_b128 v22, v[96:99] offset:4096\n"
    "v_exp_f32 v14, v100\n"
    "buffer_load_dwordx4 v[28:31], v252, s[20:23], s58 offen offset:1024 sc1\n"
    "v_exp_f32 v15, v101\n"
    "v_exp_f32 v16, v102\n"
    "v_exp_f32 v17, v103\n"
    "v_add_f32 v14, 1.0, v14\n"
    "v_add_f32 v15, 1.0, v15\n"
    "v_add_f32 v16, 1.0, v16\n"
    "v_add_f32 v17, 1.0, v17\n"
    "v_rcp_f32 v14, v14\n"
    "v_rcp_f32 v15, v15\n"
    "v_rcp_f32 v16, v16\n"
    "v_rcp_f32 v17, v17\n"
    "v_mul_f32 v100, v100, v14\n"
    "v_mul_f32 v101, v101, v15\n"
    "v_mul_f32 v102, v102, v16\n"
    "v_mul_f32 v103, v103, v17\n"
    "v_cvt_pk_bf16_f32 v100, v100, v101\n"
    "v_cvt_pk_bf16_f32 v101, v102, v103\n"
    "v_exp_f32 v18, v116\n"
    "v_exp_f32 v19, v117\n"
    "v_exp_f32 v20, v118\n"
    "v_exp_f32 v21, v119\n"
    "v_add_f32 v18, 1.0, v18\n"
    "v_add_f32 v19, 1.0, v19\n"
    "v_add_f32 v20, 1.0, v20\n"
    "v_add_f32 v21, 1.0, v21\n"
    "v_rcp_f32 v18, v18\n"
    "v_rcp_f32 v19, v19\n"
    "v_rcp_f32 v20, v20\n"
    "v_rcp_f32 v21, v21\n"
    "v_mul_f32 v116, v116, v18\n"
    "v_mul_f32 v117, v117, v19\n"
    "buffer_load_dwordx4 v[32:35], v253, s[20:23], s58 offen offset:1024 sc1\n"
    "v_mul_f32 v118, v118, v20\n"
    "v_mul_f32 v119, v119, v21\n"
    "v_cvt_pk_bf16_f32 v102, v116, v117\n"
    "v_cvt_pk_bf16_f32 v103, v118, v119\n"
    "ds_write_b128 v22, v[100:103] offset:5120\n"
    "v_exp_f32 v14, v104\n"
    "v_exp_f32 v15, v105\n"
    "v_exp_f32 v16, v106\n"
    "v_exp_f32 v17, v107\n"
    "v_add_f32 v14, 1.0, v14\n"
    "v_add_f32 v15, 1.0, v15\n"
    "v_add_f32 v16, 1.0, v16\n"
    "v_add_f32 v17, 1.0, v17\n"
    "v_rcp_f32 v14, v14\n"
    "v_rcp_f32 v15, v15\n"
    "v_rcp_f32 v16, v16\n"
    "v_rcp_f32 v17, v17\n"
    "v_mul_f32 v104, v104, v14\n"
    "v_mul_f32 v105, v105, v15\n"
    "v_mul_f32 v106, v106, v16\n"
    "v_mul_f32 v107, v107, v17\n"
    "v_cvt_pk_bf16_f32 v104, v104, v105\n"
    "v_cvt_pk_bf16_f32 v105, v106, v107\n"
    "v_exp_f32 v18, v120\n"
    "v_exp_f32 v19, v121\n"
    "v_exp_f32 v20, v122\n"
    "v_exp_f32 v21, v123\n"
    "v_add_f32 v18, 1.0, v18\n"
    "v_add_f32 v19, 1.0, v19\n"
    "v_add_f32 v20, 1.0, v20\n"
    "v_add_f32 v21, 1.0, v21\n"
    "buffer_load_dwordx4 v[36:39], v254, s[20:23], s58 offen offset:1024 sc1\n"
    "v_rcp_f32 v18, v18\n"
    "v_rcp_f32 v19, v19\n"
    "v_rcp_f32 v20, v20\n"
    "v_rcp_f32 v21, v21\n"
    "v_mul_f32 v120, v120, v18\n"
    "v_mul_f32 v121, v121, v19\n"
    "v_mul_f32 v122, v122, v20\n"
    "v_mul_f32 v123, v123, v21\n"
    "v_cvt_pk_bf16_f32 v106, v120, v121\n"
    "v_cvt_pk_bf16_f32 v107, v122, v123\n"
    "ds_write_b128 v22, v[104:107] offset:6144\n"
    "v_exp_f32 v14, v108\n"
    "v_exp_f32 v15, v109\n"
    "v_exp_f32 v16, v110\n"
    "v_exp_f32 v17, v111\n"
    "v_add_f32 v14, 1.0, v14\n"
    "v_add_f32 v15, 1.0, v15\n"
    "v_add_f32 v16, 1.0, v16\n"
    "v_add_f32 v17, 1.0, v17\n"
    "v_rcp_f32 v14, v14\n"
    "v_rcp_f32 v15, v15\n"
    "v_rcp_f32 v16, v16\n"
    "v_rcp_f32 v17, v17\n"
    "v_mul_f32 v108, v108, v14\n"
    "v_mul_f32 v109, v109, v15\n"
    "v_mul_f32 v110, v110, v16\n"
    "v_mul_f32 v111, v111, v17\n"
    "v_cvt_pk_bf16_f32 v108, v108, v109\n"
    "v_cvt_pk_bf16_f32 v109, v110, v111\n"
    "v_exp_f32 v18, v124\n"
    "v_exp_f32 v19, v125\n"
    "buffer_load_dwordx4 v[40:43], v1, s[20:23], s58 offen offset:2048 sc1\n"
    "v_exp_f32 v20, v126\n"
    "v_exp_f32 v21, v127\n"
    "v_add_f32 v18, 1.0, v18\n"
    "v_add_f32 v19, 1.0, v19\n"
    "v_add_f32 v20, 1.0, v20\n"
    "v_add_f32 v21, 1.0, v21\n"
    "v_rcp_f32 v18, v18\n"
    "v_rcp_f32 v19, v19\n"
    "v_rcp_f32 v20, v20\n"
    "v_rcp_f32 v21, v21\n"
    "v_mul_f32 v124, v124, v18\n"
    "v_mul_f32 v125, v125, v19\n"
    "v_mul_f32 v126, v126, v20\n"
    "v_mul_f32 v127, v127, v21\n"
    "v_cvt_pk_bf16_f32 v110, v124, v125\n"
    "v_cvt_pk_bf16_f32 v111, v126, v127\n"
    "ds_write_b128 v22, v[108:111] offset:7168\n"
    "v_exp_f32 v14, v128\n"
    "v_exp_f32 v15, v129\n"
    "v_exp_f32 v16, v130\n"
    "v_exp_f32 v17, v131\n"
    "v_add_f32 v14, 1.0, v14\n"
    "v_add_f32 v15, 1.0, v15\n"
    "v_add_f32 v16, 1.0, v16\n"
    "v_add_f32 v17, 1.0, v17\n"
    "v_rcp_f32 v14, v14\n"
    "v_rcp_f32 v15, v15\n"
    "v_rcp_f32 v16, v16\n"
    "v_rcp_f32 v17, v17\n"
    "v_mul_f32 v128, v128, v14\n"
    "v_mul_f32 v129, v129, v15\n"
    "buffer_load_dwordx4 v[236:239], v252, s[20:23], s58 offen offset:2048 sc1\n"
    "v_mul_f32 v130, v130, v16\n"
    "v_mul_f32 v131, v131, v17\n"
    "v_cvt_pk_bf16_f32 v128, v128, v129\n"
    "v_cvt_pk_bf16_f32 v129, v130, v131\n"
    "ds_write_b64 v23, v[128:129]\n"
    "v_exp_f32 v18, v132\n"
    "v_exp_f32 v19, v133\n"
    "v_exp_f32 v20, v134\n"
    "v_exp_f32 v21, v135\n"
    "v_add_f32 v18, 1.0, v18\n"
    "v_add_f32 v19, 1.0, v19\n"
    "v_add_f32 v20, 1.0, v20\n"
    "v_add_f32 v21, 1.0, v21\n"
    "v_rcp_f32 v18, v18\n"
    "v_rcp_f32 v19, v19\n"
    "v_rcp_f32 v20, v20\n"
    "v_rcp_f32 v21, v21\n"
    "v_mul_f32 v132, v132, v18\n"
    "v_mul_f32 v133, v133, v19\n"
    "v_mul_f32 v134, v134, v20\n"
    "v_mul_f32 v135, v135, v21\n"
    "v_cvt_pk_bf16_f32 v132, v132, v133\n"
    "v_cvt_pk_bf16_f32 v133, v134, v135\n"
    "ds_write_b64 v23, v[132:133] offset:1024\n"
    "v_exp_f32 v14, v136\n"
    "v_exp_f32 v15, v137\n"
    "v_exp_f32 v16, v138\n"
    "v_exp_f32 v17, v139\n"
    "v_add_f32 v14, 1.0, v14\n"
    "v_add_f32 v15, 1.0, v15\n"
    "v_add_f32 v16, 1.0, v16\n"
    "buffer_load_dwordx4 v[176:179], v253, s[20:23], s58 offen offset:2048 sc1\n"
    "v_add_f32 v17, 1.0, v17\n"
    "v_rcp_f32 v14, v14\n"
    "v_rcp_f32 v15, v15\n"
    "v_rcp_f32 v16, v16\n"
    "v_rcp_f32 v17, v17\n"
    "v_mul_f32 v136, v136, v14\n"
    "v_mul_f32 v137, v137, v15\n"
    "v_mul_f32 v138, v138, v16\n"
    "v_mul_f32 v139, v139, v17\n"
    "v_cvt_pk_bf16_f32 v136, v136, v137\n"
    "v_cvt_pk_bf16_f32 v137, v138, v139\n"
    "ds_write_b64 v23, v[136:137] offset:2048\n"
    "v_exp_f32 v18, v140\n"
    "v_exp_f32 v19, v141\n"
    "v_exp_f32 v20, v142\n"
    "v_exp_f32 v21, v143\n"
    "v_add_f32 v18, 1.0, v18\n"
    "v_add_f32 v19, 1.0, v19\n"
    "v_add_f32 v20, 1.0, v20\n"
    "v_add_f32 v21, 1.0, v21\n"
    "v_rcp_f32 v18, v18\n"
    "v_rcp_f32 v19, v19\n"
    "v_rcp_f32 v20, v20\n"
    "v_rcp_f32 v21, v21\n"
    "v_mul_f32 v140, v140, v18\n"
    "v_mul_f32 v141, v141, v19\n"
    "v_mul_f32 v142, v142, v20\n"
    "v_mul_f32 v143, v143, v21\n"
    "v_cvt_pk_bf16_f32 v140, v140, v141\n"
    "v_cvt_pk_bf16_f32 v141, v142, v143\n"
    "ds_write_b64 v23, v[140:141] offset:3072\n"
    "buffer_load_dwordx4 v[180:183], v254, s[20:23], s58 offen offset:2048 sc1\n"
    "s_waitcnt lgkmcnt(0)\n"
    "s_barrier\n"
    "s_setprio 2\n"
    "ds_read_b128 v[144:147], v24\n"
    "ds_read_b128 v[148:151], v24 offset:1024\n"
    "ds_read_b128 v[152:155], v24 offset:2048\n"
    "ds_read_b128 v[156:159], v24 offset:3072\n"
    "buffer_load_dwordx4 v[128:131], v1, s[20:23], s58 offen offset:3072 sc1\n"
    "buffer_load_dwordx4 v[132:135], v252, s[20:23], s58 offen offset:3072 sc1\n"
    "buffer_load_dwordx4 v[136:139], v253, s[20:23], s58 offen offset:3072 sc1\n"
    "buffer_load_dwordx4 v[140:143], v254, s[20:23], s58 offen offset:3072 sc1\n"
    "s_waitcnt vmcnt(31) lgkmcnt(3)\n"
    "v_mfma_f32_16x16x32_bf16 v[64:67], v[44:47], v[144:147], 0\n"
    "ds_read_b128 v[160:163], v24 offset:4096\n"
    "s_waitcnt lgkmcnt(3)\n"
    "v_mfma_f32_16x16x32_bf16 v[68:71], v[44:47], v[148:151], 0\n"
    "s_waitcnt lgkmcnt(2)\n"
    "v_mfma_f32_16x16x32_bf16 v[72:75], v[44:47], v[152:155], 0\n"
    "s_waitcnt lgkmcnt(1)\n"
    "v_mfma_f32_16x16x32_bf16 v[76:79], v[44:47], v[156:159], 0\n"
    "s_add_u32 s58, s58, 12288\n"
    "buffer_load_dwordx4 v[44:47], v1, s[20:23], s58 offen sc1\n"
    "s_waitcnt vmcnt(31)\n"
    "v_mfma_f32_16x16x32_bf16 v[80:83], v[48:51], v[144:147], 0\n"
    "ds_read_b128 v[164:167], v24 offset:5120\n"
    "v_mfma_f32_16x16x32_bf16 v[84:87], v[48:51], v[148:151], 0\n"
    "v_mfma_f32_16x16x32_bf16 v[88:91], v[48:51], v[152:155], 0\n"
    "v_mfma_f32_16x16x32_bf16 v[92:95], v[48:51], v[156:159], 0\n"
    "buffer_load_dwordx4 v[48:51], v252, s[20:23], s58 offen sc1\n"
    "s_waitcnt vmcnt(31)\n"
    "v_mfma_f32_16x16x32_bf16 v[96:99], v[184:187], v[144:147], 0\n"
    "ds_read_b128 v[168:171], v24 offset:6144\n"
    "v_mfma_f32_16x16x32_bf16 v[100:103], v[184:187], v[148:151], 0\n"
    "v_mfma_f32_16x16x32_bf16 v[104:107], v[184:187], v[152:155], 0\n"
    "v_mfma_f32_16x16x32_bf16 v[108:111], v[184:187], v[156:159], 0\n"
    "buffer_load_dwordx4 v[184:187], v253, s[20:23], s58 offen sc1\n"
    "s_waitcnt vmcnt(31)\n"
    "v_mfma_f32_16x16x32_bf16 v[112:115], v[188:191], v[144:147], 0\n"
    "ds_read_b128 v[172:175], v24 offset:7168\n"
    "v_mfma_f32_16x16x32_bf16 v[116:119], v[188:191], v[148:151], 0\n"
    "v_mfma_f32_16x16x32_bf16 v[120:123], v[188:191], v[152:155], 0\n"
    "v_mfma_f32_16x16x32_bf16 v[124:127], v[188:191], v[156:159], 0\n"
    "buffer_load_dwordx4 v[188:191], v254, s[20:23], s58 offen sc1\n"
    "s_waitcnt vmcnt(31) lgkmcnt(3)\n"
    "v_mfma_f32_16x16x32_bf16 v[64:67], v[192:195], v[160:163], v[64:67]\n"
    "ds_read_b128 v[144:147], v24 offset:8192\n"
    "s_waitcnt lgkmcnt(3)\n"
    "v_mfma_f32_16x16x32_bf16 v[68:71], v[192:195], v[164:167], v[68:71]\n"
    "s_waitcnt lgkmcnt(2)\n"
    "v_mfma_f32_16x16x32_bf16 v[72:75], v[192:195], v[168:171], v[72:75]\n"
    "s_waitcnt lgkmcnt(1)\n"
    "v_mfma_f32_16x16x32_bf16 v[76:79], v[192:195], v[172:175], v[76:79]\n"
    "buffer_load_dwordx4 v[192:195], v1, s[20:23], s58 offen offset:1024 sc1\n"
    "s_waitcnt vmcnt(31)\n"
    "v_mfma_f32_16x16x32_bf16 v[80:83], v[52:55], v[160:163], v[80:83]\n"
    "ds_read_b128 v[148:151], v24 offset:9216\n"
    "v_mfma_f32_16x16x32_bf16 v[84:87], v[52:55], v[164:167], v[84:87]\n"
    "v_mfma_f32_16x16x32_bf16 v[88:91], v[52:55], v[168:171], v[88:91]\n"
    "v_mfma_f32_16x16x32_bf16 v[92:95], v[52:55], v[172:175], v[92:95]\n"
    "buffer_load_dwordx4 v[52:55], v252, s[20:23], s58 offen offset:1024 sc1\n"
    "s_waitcnt vmcnt(31)\n"
    "v_mfma_f32_16x16x32_bf16 v[96:99], v[56:59], v[160:163], v[96:99]\n"
    "ds_read_b128 v[152:155], v24 offset:10240\n"
    "v_mfma_f32_16x16x32_bf16 v[100:103], v[56:59], v[164:167], v[100:103]\n"
    "v_mfma_f32_16x16x32_bf16 v[104:107], v[56:59], v[168:171], v[104:107]\n"
    "v_mfma_f32_16x16x32_bf16 v[108:111], v[56:59], v[172:175], v[108:111]\n"
    "buffer_load_dwordx4 v[56:59], v253, s[20:23], s58 offen offset:1024 sc1\n"
    "s_waitcnt vmcnt(31)\n"
    "v_mfma_f32_16x16x32_bf16 v[112:115], v[240:243], v[160:163], v[112:115]\n"
    "ds_read_b128 v[156:159], v24 offset:11264\n"
    "v_mfma_f32_16x16x32_bf16 v[116:119], v[240:243], v[164:167], v[116:119]\n"
    "v_mfma_f32_16x16x32_bf16 v[120:123], v[240:243], v[168:171], v[120:123]\n"
    "v_mfma_f32_16x16x32_bf16 v[124:127], v[240:243], v[172:175], v[124:127]\n"
    "buffer_load_dwordx4 v[240:243], v254, s[20:23], s58 offen offset:1024 sc1\n"
    "s_waitcnt vmcnt(31) lgkmcnt(3)\n"
    "v_mfma_f32_16x16x32_bf16 v[64:67], v[60:63], v[144:147], v[64:67]\n"
    "ds_read_b128 v[160:163], v24 offset:12288\n"
    "s_waitcnt lgkmcnt(3)\n"
    "v_mfma_f32_16x16x32_bf16 v[68:71], v[60:63], v[148:151], v[68:71]\n"
    "s_waitcnt lgkmcnt(2)\n"
    "v_mfma_f32_16x16x32_bf16 v[72:75], v[60:63], v[152:155], v[72:75]\n"
    "s_waitcnt lgkmcnt(1)\n"
    "v_mfma_f32_16x16x32_bf16 v[76:79], v[60:63], v[156:159], v[76:79]\n"
    "s_sub_u32 s58, s58, 8192\n"
    "buffer_load_dwordx4 v[60:63], v1, s[20:23], s58 offen sc1\n"
    "s_waitcnt vmcnt(31)\n"
    "v_mfma_f32_16x16x32_bf16 v[80:83], v[224:227], v[144:147], v[80:83]\n"
    "ds_read_b128 v[164:167], v24 offset:13312\n"
    "v_mfma_f32_16x16x32_bf16 v[84:87], v[224:227], v[148:151], v[84:87]\n"
    "v_mfma_f32_16x16x32_bf16 v[88:91], v[224:227], v[152:155], v[88:91]\n"
    "v_mfma_f32_16x16x32_bf16 v[92:95], v[224:227], v[156:159], v[92:95]\n"
    "buffer_load_dwordx4 v[224:227], v252, s[20:23], s58 offen sc1\n"
    "s_waitcnt vmcnt(31)\n"
    "v_mfma_f32_16x16x32_bf16 v[96:99], v[228:231], v[144:147], v[96:99]\n"
    "ds_read_b128 v[168:171], v24 offset:14336\n"
    "v_mfma_f32_16x16x32_bf16 v[100:103], v[228:231], v[148:151], v[100:103]\n"
    "v_mfma_f32_16x16x32_bf16 v[104:107], v[228:231], v[152:155], v[104:107]\n"
    "v_mfma_f32_16x16x32_bf16 v[108:111], v[228:231], v[156:159], v[108:111]\n"
    "buffer_load_dwordx4 v[228:231], v253, s[20:23], s58 offen sc1\n"
    "s_waitcnt vmcnt(31)\n"
    "v_mfma_f32_16x16x32_bf16 v[112:115], v[232:235], v[144:147], v[112:115]\n"
    "ds_read_b128 v[172:175], v24 offset:15360\n"
    "v_mfma_f32_16x16x32_bf16 v[116:119], v[232:235], v[148:151], v[116:119]\n"
    "v_mfma_f32_16x16x32_bf16 v[120:123], v[232:235], v[152:155], v[120:123]\n"
    "v_mfma_f32_16x16x32_bf16 v[124:127], v[232:235], v[156:159], v[124:127]\n"
    "buffer_load_dwordx4 v[232:235], v254, s[20:23], s58 offen sc1\n"
    "s_waitcnt vmcnt(31) lgkmcnt(3)\n"
    "v_mfma_f32_16x16x32_bf16 v[64:67], v[244:247], v[160:163], v[64:67]\n"
    "ds_read_b128 v[144:147], v24 offset:16384\n"
    "s_waitcnt lgkmcnt(3)\n"
    "v_mfma_f32_16x16x32_bf16 v[68:71], v[244:247], v[164:167], v[68:71]\n"
    "s_waitcnt lgkmcnt(2)\n"
    "v_mfma_f32_16x16x32_bf16 v[72:75], v[244:247], v[168:171], v[72:75]\n"
    "s_waitcnt lgkmcnt(1)\n"
    "v_mfma_f32_16x16x32_bf16 v[76:79], v[244:247], v[172:175], v[76:79]\n"
    "buffer_load_dwordx4 v[244:247], v1, s[20:23], s58 offen offset:1024 sc1\n"
    "s_waitcnt vmcnt(31)\n"
    "v_mfma_f32_16x16x32_bf16 v[80:83], v[248:251], v[160:163], v[80:83]\n"
    "ds_read_b128 v[148:151], v24 offset:17408\n"
    "v_mfma_f32_16x16x32_bf16 v[84:87], v[248:251], v[164:167], v[84:87]\n"
    "v_mfma_f32_16x16x32_bf16 v[88:91], v[248:251], v[168:171], v[88:91]\n"
    "v_mfma_f32_16x16x32_bf16 v[92:95], v[248:251], v[172:175], v[92:95]\n"
    "buffer_load_dwordx4 v[248:251], v252, s[20:23], s58 offen offset:1024 sc1\n"
    "s_waitcnt vmcnt(31)\n"
    "v_mfma_f32_16x16x32_bf16 v[96:99], v[196:199], v[160:163], v[96:99]\n"
    "ds_read_b128 v[152:155], v24 offset:18432\n"
    "v_mfma_f32_16x16x32_bf16 v[100:103], v[196:199], v[164:167], v[100:103]\n"
    "v_mfma_f32_16x16x32_bf16 v[104:107], v[196:199], v[168:171], v[104:107]\n"
    "v_mfma_f32_16x16x32_bf16 v[108:111], v[196:199], v[172:175], v[108:111]\n"
    "buffer_load_dwordx4 v[196:199], v253, s[20:23], s58 offen offset:1024 sc1\n"
    "s_waitcnt vmcnt(31)\n"
    "v_mfma_f32_16x16x32_bf16 v[112:115], v[200:203], v[160:163], v[112:115]\n"
    "ds_read_b128 v[156:159], v24 offset:19456\n"
    "v_mfma_f32_16x16x32_bf16 v[116:119], v[200:203], v[164:167], v[116:119]\n"
    "v_mfma_f32_16x16x32_bf16 v[120:123], v[200:203], v[168:171], v[120:123]\n"
    "v_mfma_f32_16x16x32_bf16 v[124:127], v[200:203], v[172:175], v[124:127]\n"
    "buffer_load_dwordx4 v[200:203], v254, s[20:23], s58 offen offset:1024 sc1\n"
    "s_waitcnt vmcnt(31) lgkmcnt(3)\n"
    "v_mfma_f32_16x16x32_bf16 v[64:67], v[204:207], v[144:147], v[64:67]\n"
    "ds_read_b128 v[160:163], v24 offset:20480\n"
    "s_waitcnt lgkmcnt(3)\n"
    "v_mfma_f32_16x16x32_bf16 v[68:71], v[204:207], v[148:151], v[68:71]\n"
    "s_waitcnt lgkmcnt(2)\n"
    "v_mfma_f32_16x16x32_bf16 v[72:75], v[204:207], v[152:155], v[72:75]\n"
    "s_waitcnt lgkmcnt(1)\n"
    "v_mfma_f32_16x16x32_bf16 v[76:79], v[204:207], v[156:159], v[76:79]\n"
    "buffer_load_dwordx4 v[204:207], v1, s[20:23], s58 offen offset:2048 sc1\n"
    "s_waitcnt vmcnt(31)\n"
    "v_mfma_f32_16x16x32_bf16 v[80:83], v[208:211], v[144:147], v[80:83]\n"
    "ds_read_b128 v[164:167], v24 offset:21504\n"
    "v_mfma_f32_16x16x32_bf16 v[84:87], v[208:211], v[148:151], v[84:87]\n"
    "v_mfma_f32_16x16x32_bf16 v[88:91], v[208:211], v[152:155], v[88:91]\n"
    "v_mfma_f32_16x16x32_bf16 v[92:95], v[208:211], v[156:159], v[92:95]\n"
    "buffer_load_dwordx4 v[208:211], v252, s[20:23], s58 offen offset:2048 sc1\n"
    "s_waitcnt vmcnt(31)\n"
    "v_mfma_f32_16x16x32_bf16 v[96:99], v[212:215], v[144:147], v[96:99]\n"
    "ds_read_b128 v[168:171], v24 offset:22528\n"
    "v_mfma_f32_16x16x32_bf16 v[100:103], v[212:215], v[148:151], v[100:103]\n"
    "v_mfma_f32_16x16x32_bf16 v[104:107], v[212:215], v[152:155], v[104:107]\n"
    "v_mfma_f32_16x16x32_bf16 v[108:111], v[212:215], v[156:159], v[108:111]\n"
    "buffer_load_dwordx4 v[212:215], v253, s[20:23], s58 offen offset:2048 sc1\n"
    "s_waitcnt vmcnt(31)\n"
    "v_mfma_f32_16x16x32_bf16 v[112:115], v[216:219], v[144:147], v[112:115]\n"
    "ds_read_b128 v[172:175], v24 offset:23552\n"
    "v_mfma_f32_16x16x32_bf16 v[116:119], v[216:219], v[148:151], v[116:119]\n"
    "v_mfma_f32_16x16x32_bf16 v[120:123], v[216:219], v[152:155], v[120:123]\n"
    "v_mfma_f32_16x16x32_bf16 v[124:127], v[216:219], v[156:159], v[124:127]\n"
    "buffer_load_dwordx4 v[216:219], v254, s[20:23], s58 offen offset:2048 sc1\n"
    "s_waitcnt vmcnt(31) lgkmcnt(3)\n"
    "v_mfma_f32_16x16x32_bf16 v[64:67], v[220:223], v[160:163], v[64:67]\n"
    "ds_read_b128 v[144:147], v24 offset:24576\n"
    "s_waitcnt lgkmcnt(3)\n"
    "v_mfma_f32_16x16x32_bf16 v[68:71], v[220:223], v[164:167], v[68:71]\n"
    "s_waitcnt lgkmcnt(2)\n"
    "v_mfma_f32_16x16x32_bf16 v[72:75], v[220:223], v[168:171], v[72:75]\n"
    "s_waitcnt lgkmcnt(1)\n"
    "v_mfma_f32_16x16x32_bf16 v[76:79], v[220:223], v[172:175], v[76:79]\n"
    "buffer_load_dwordx4 v[220:223], v1, s[20:23], s58 offen offset:3072 sc1\n"
    "s_waitcnt vmcnt(31)\n"
    "v_mfma_f32_16x16x32_bf16 v[80:83], v[28:31], v[160:163], v[80:83]\n"
    "ds_read_b128 v[148:151], v24 offset:25600\n"
    "v_mfma_f32_16x16x32_bf16 v[84:87], v[28:31], v[164:167], v[84:87]\n"
    "v_mfma_f32_16x16x32_bf16 v[88:91], v[28:31], v[168:171], v[88:91]\n"
    "v_mfma_f32_16x16x32_bf16 v[92:95], v[28:31], v[172:175], v[92:95]\n"
    "buffer_load_dwordx4 v[28:31], v252, s[20:23], s58 offen offset:3072 sc1\n"
    "s_waitcnt vmcnt(31)\n"
    "v_mfma_f32_16x16x32_bf16 v[96:99], v[32:35], v[160:163], v[96:99]\n"
    "ds_read_b128 v[152:155], v24 offset:26624\n"
    "v_mfma_f32_16x16x32_bf16 v[100:103], v[32:35], v[164:167], v[100:103]\n"
    "v_mfma_f32_16x16x32_bf16 v[104:107], v[32:35], v[168:171], v[104:107]\n"
    "v_mfma_f32_16x16x32_bf16 v[108:111], v[32:35], v[172:175], v[108:111]\n"
    "buffer_load_dwordx4 v[32:35], v253, s[20:23], s58 offen offset:3072 sc1\n"
    "s_waitcnt vmcnt(31)\n"
    "v_mfma_f32_16x16x32_bf16 v[112:115], v[36:39], v[160:163], v[112:115]\n"
    "ds_read_b128 v[156:159], v24 offset:27648\n"
    "v_mfma_f32_16x16x32_bf16 v[116:119], v[36:39], v[164:167], v[116:119]\n"
    "v_mfma_f32_16x16x32_bf16 v[120:123], v[36:39], v[168:171], v[120:123]\n"
    "v_mfma_f32_16x16x32_bf16 v[124:127], v[36:39], v[172:175], v[124:127]\n"
    "buffer_load_dwordx4 v[36:39], v254, s[20:23], s58 offen offset:3072 sc1\n"
    "s_waitcnt vmcnt(31) lgkmcnt(3)\n"
    "v_mfma_f32_16x16x32_bf16 v[64:67], v[40:43], v[144:147], v[64:67]\n"
    "ds_read_b128 v[160:163], v24 offset:28672\n"
    "s_waitcnt lgkmcnt(3)\n"
    "v_mfma_f32_16x16x32_bf16 v[68:71], v[40:43], v[148:151], v[68:71]\n"
    "s_waitcnt lgkmcnt(2)\n"
    "v_mfma_f32_16x16x32_bf16 v[72:75], v[40:43], v[152:155], v[72:75]\n"
    "s_waitcnt lgkmcnt(1)\n"
    "v_mfma_f32_16x16x32_bf16 v[76:79], v[40:43], v[156:159], v[76:79]\n"
    "s_add_u32 s58, s58, 4096\n"
    "buffer_load_dwordx4 v[40:43], v1, s[20:23], s58 offen sc1\n"
    "s_waitcnt vmcnt(31)\n"
    "v_mfma_f32_16x16x32_bf16 v[80:83], v[236:239], v[144:147], v[80:83]\n"
    "ds_read_b128 v[164:167], v24 offset:29696\n"
    "v_mfma_f32_16x16x32_bf16 v[84:87], v[236:239], v[148:151], v[84:87]\n"
    "v_mfma_f32_16x16x32_bf16 v[88:91], v[236:239], v[152:155], v[88:91]\n"
    "v_mfma_f32_16x16x32_bf16 v[92:95], v[236:239], v[156:159], v[92:95]\n"
    "buffer_load_dwordx4 v[236:239], v252, s[20:23], s58 offen sc1\n"
    "s_waitcnt vmcnt(31)\n"
    "v_mfma_f32_16x16x32_bf16 v[96:99], v[176:179], v[144:147], v[96:99]\n"
    "ds_read_b128 v[168:171], v24 offset:30720\n"
    "v_mfma_f32_16x16x32_bf16 v[100:103], v[176:179], v[148:151], v[100:103]\n"
    "v_mfma_f32_16x16x32_bf16 v[104:107], v[176:179], v[152:155], v[104:107]\n"
    "v_mfma_f32_16x16x32_bf16 v[108:111], v[176:179], v[156:159], v[108:111]\n"
    "buffer_load_dwordx4 v[176:179], v253, s[20:23], s58 offen sc1\n"
    "s_waitcnt vmcnt(31)\n"
    "v_mfma_f32_16x16x32_bf16 v[112:115], v[180:183], v[144:147], v[112:115]\n"
    "ds_read_b128 v[172:175], v24 offset:31744\n"
    "v_mfma_f32_16x16x32_bf16 v[116:119], v[180:183], v[148:151], v[116:119]\n"
    "v_mfma_f32_16x16x32_bf16 v[120:123], v[180:183], v[152:155], v[120:123]\n"
    "v_mfma_f32_16x16x32_bf16 v[124:127], v[180:183], v[156:159], v[124:127]\n"
    "buffer_load_dwordx4 v[180:183], v254, s[20:23], s58 offen sc1\n"
    "s_waitcnt vmcnt(31) lgkmcnt(3)\n"
    "v_mfma_f32_16x16x32_bf16 v[64:67], v[128:131], v[160:163], v[64:67]\n"
    "ds_read_b128 v[144:147], v25\n"
    "s_waitcnt lgkmcnt(3)\n"
    "v_mfma_f32_16x16x32_bf16 v[68:71], v[128:131], v[164:167], v[68:71]\n"
    "s_waitcnt lgkmcnt(2)\n"
    "v_mfma_f32_16x16x32_bf16 v[72:75], v[128:131], v[168:171], v[72:75]\n"
    "s_waitcnt lgkmcnt(1)\n"
    "v_mfma_f32_16x16x32_bf16 v[76:79], v[128:131], v[172:175], v[76:79]\n"
    "buffer_load_dwordx4 v[128:131], v1, s[20:23], s58 offen offset:1024 sc1\n"
    "s_waitcnt vmcnt(31)\n"
    "v_mfma_f32_16x16x32_bf16 v[80:83], v[132:135], v[160:163], v[80:83]\n"
    "ds_read_b128 v[148:151], v25 offset:1024\n"
    "v_mfma_f32_16x16x32_bf16 v[84:87], v[132:135], v[164:167], v[84:87]\n"
    "v_mfma_f32_16x16x32_bf16 v[88:91], v[132:135], v[168:171], v[88:91]\n"
    "v_mfma_f32_16x16x32_bf16 v[92:95], v[132:135], v[172:175], v[92:95]\n"
    "buffer_load_dwordx4 v[132:135], v252, s[20:23], s58 offen offset:1024 sc1\n"
    "s_waitcnt vmcnt(31)\n"
    "v_mfma_f32_16x16x32_bf16 v[96:99], v[136:139], v[160:163], v[96:99]\n"
    "ds_read_b128 v[152:155], v25 offset:2048\n"
    "v_mfma_f32_16x16x32_bf16 v[100:103], v[136:139], v[164:167], v[100:103]\n"
    "v_mfma_f32_16x16x32_bf16 v[104:107], v[136:139], v[168:171], v[104:107]\n"
    "v_mfma_f32_16x16x32_bf16 v[108:111], v[136:139], v[172:175], v[108:111]\n"
    "buffer_load_dwordx4 v[136:139], v253, s[20:23], s58 offen offset:1024 sc1\n"
    "s_waitcnt vmcnt(31)\n"
    "v_mfma_f32_16x16x32_bf16 v[112:115], v[140:143], v[160:163], v[112:115]\n"
    "ds_read_b128 v[156:159], v25 offset:3072\n"
    "v_mfma_f32_16x16x32_bf16 v[116:119], v[140:143], v[164:167], v[116:119]\n"
    "v_mfma_f32_16x16x32_bf16 v[120:123], v[140:143], v[168:171], v[120:123]\n"
    "v_mfma_f32_16x16x32_bf16 v[124:127], v[140:143], v[172:175], v[124:127]\n"
    "buffer_load_dwordx4 v[140:143], v254, s[20:23], s58 offen offset:1024 sc1\n"
    "s_waitcnt vmcnt(31) lgkmcnt(3)\n"
    "v_mfma_f32_16x16x32_bf16 v[64:67], v[44:47], v[144:147], v[64:67]\n"
    "ds_read_b128 v[160:163], v25 offset:4096\n"
    "s_waitcnt lgkmcnt(3)\n"
    "v_mfma_f32_16x16x32_bf16 v[68:71], v[44:47], v[148:151], v[68:71]\n"
    "s_waitcnt lgkmcnt(2)\n"
    "v_mfma_f32_16x16x32_bf16 v[72:75], v[44:47], v[152:155], v[72:75]\n"
    "s_waitcnt lgkmcnt(1)\n"
    "v_mfma_f32_16x16x32_bf16 v[76:79], v[44:47], v[156:159], v[76:79]\n"
    "buffer_load_dwordx4 v[44:47], v1, s[20:23], s58 offen offset:2048 sc1\n"
    "s_waitcnt vmcnt(31)\n"
    "v_mfma_f32_16x16x32_bf16 v[80:83], v[48:51], v[144:147], v[80:83]\n"
    "ds_read_b128 v[164:167], v25 offset:5120\n"
    "v_mfma_f32_16x16x32_bf16 v[84:87], v[48:51], v[148:151], v[84:87]\n"
    "v_mfma_f32_16x16x32_bf16 v[88:91], v[48:51], v[152:155], v[88:91]\n"
    "v_mfma_f32_16x16x32_bf16 v[92:95], v[48:51], v[156:159], v[92:95]\n"
    "buffer_load_dwordx4 v[48:51], v252, s[20:23], s58 offen offset:2048 sc1\n"
    "s_waitcnt vmcnt(31)\n"
    "v_mfma_f32_16x16x32_bf16 v[96:99], v[184:187], v[144:147], v[96:99]\n"
    "ds_read_b128 v[168:171], v25 offset:6144\n"
    "v_mfma_f32_16x16x32_bf16 v[100:103], v[184:187], v[148:151], v[100:103]\n"
    "v_mfma_f32_16x16x32_bf16 v[104:107], v[184:187], v[152:155], v[104:107]\n"
    "v_mfma_f32_16x16x32_bf16 v[108:111], v[184:187], v[156:159], v[108:111]\n"
    "buffer_load_dwordx4 v[184:187], v253, s[20:23], s58 offen offset:2048 sc1\n"
    "s_waitcnt vmcnt(31)\n"
    "v_mfma_f32_16x16x32_bf16 v[112:115], v[188:191], v[144:147], v[112:115]\n"
    "ds_read_b128 v[172:175], v25 offset:7168\n"
    "v_mfma_f32_16x16x32_bf16 v[116:119], v[188:191], v[148:151], v[116:119]\n"
    "v_mfma_f32_16x16x32_bf16 v[120:123], v[188:191], v[152:155], v[120:123]\n"
    "v_mfma_f32_16x16x32_bf16 v[124:127], v[188:191], v[156:159], v[124:127]\n"
    "buffer_load_dwordx4 v[188:191], v254, s[20:23], s58 offen offset:2048 sc1\n"
    "s_barrier\n"
    "s_waitcnt vmcnt(31) lgkmcnt(3)\n"
    "v_mfma_f32_16x16x32_bf16 v[64:67], v[192:195], v[160:163], v[64:67]\n"
    "ds_read_b128 v[144:147], v24 offset:32768\n"
    "s_waitcnt lgkmcnt(3)\n"
    "v_mfma_f32_16x16x32_bf16 v[68:71], v[192:195], v[164:167], v[68:71]\n"
    "s_waitcnt lgkmcnt(2)\n"
    "v_mfma_f32_16x16x32_bf16 v[72:75], v[192:195], v[168:171], v[72:75]\n"
    "s_waitcnt lgkmcnt(1)\n"
    "v_mfma_f32_16x16x32_bf16 v[76:79], v[192:195], v[172:175], v[76:79]\n"
    "buffer_load_dwordx4 v[192:195], v1, s[20:23], s58 offen offset:3072 sc1\n"
    "s_waitcnt vmcnt(31)\n"
    "v_mfma_f32_16x16x32_bf16 v[80:83], v[52:55], v[160:163], v[80:83]\n"
    "ds_read_b128 v[148:151], v24 offset:33792\n"
    "v_mfma_f32_16x16x32_bf16 v[84:87], v[52:55], v[164:167], v[84:87]\n"
    "v_mfma_f32_16x16x32_bf16 v[88:91], v[52:55], v[168:171], v[88:91]\n"
    "v_mfma_f32_16x16x32_bf16 v[92:95], v[52:55], v[172:175], v[92:95]\n"
    "buffer_load_dwordx4 v[52:55], v252, s[20:23], s58 offen offset:3072 sc1\n"
    "s_waitcnt vmcnt(31)\n"
    "v_mfma_f32_16x16x32_bf16 v[96:99], v[56:59], v[160:163], v[96:99]\n"
    "ds_read_b128 v[152:155], v24 offset:34816\n"
    "v_mfma_f32_16x16x32_bf16 v[100:103], v[56:59], v[164:167], v[100:103]\n"
    "v_mfma_f32_16x16x32_bf16 v[104:107], v[56:59], v[168:171], v[104:107]\n"
    "v_mfma_f32_16x16x32_bf16 v[108:111], v[56:59], v[172:175], v[108:111]\n"
    "buffer_load_dwordx4 v[56:59], v253, s[20:23], s58 offen offset:3072 sc1\n"
    "s_waitcnt vmcnt(31)\n"
    "v_mfma_f32_16x16x32_bf16 v[112:115], v[240:243], v[160:163], v[112:115]\n"
    "ds_read_b128 v[156:159], v24 offset:35840\n"
    "v_mfma_f32_16x16x32_bf16 v[116:119], v[240:243], v[164:167], v[116:119]\n"
    "v_mfma_f32_16x16x32_bf16 v[120:123], v[240:243], v[168:171], v[120:123]\n"
    "v_mfma_f32_16x16x32_bf16 v[124:127], v[240:243], v[172:175], v[124:127]\n"
    "buffer_load_dwordx4 v[240:243], v254, s[20:23], s58 offen offset:3072 sc1\n"
    "s_waitcnt vmcnt(31) lgkmcnt(3)\n"
    "v_mfma_f32_16x16x32_bf16 v[64:67], v[60:63], v[144:147], v[64:67]\n"
    "ds_read_b128 v[160:163], v24 offset:36864\n"
    "s_waitcnt lgkmcnt(3)\n"
    "v_mfma_f32_16x16x32_bf16 v[68:71], v[60:63], v[148:151], v[68:71]\n"
    "s_waitcnt lgkmcnt(2)\n"
    "v_mfma_f32_16x16x32_bf16 v[72:75], v[60:63], v[152:155], v[72:75]\n"
    "s_waitcnt lgkmcnt(1)\n"
    "v_mfma_f32_16x16x32_bf16 v[76:79], v[60:63], v[156:159], v[76:79]\n"
    "s_waitcnt vmcnt(30)\n"
    "v_mfma_f32_16x16x32_bf16 v[80:83], v[224:227], v[144:147], v[80:83]\n"
    "ds_read_b128 v[164:167], v24 offset:37888\n"
    "v_mfma_f32_16x16x32_bf16 v[84:87], v[224:227], v[148:151], v[84:87]\n"
    "v_mfma_f32_16x16x32_bf16 v[88:91], v[224:227], v[152:155], v[88:91]\n"
    "v_mfma_f32_16x16x32_bf16 v[92:95], v[224:227], v[156:159], v[92:95]\n"
    "s_waitcnt vmcnt(29)\n"
    "v_mfma_f32_16x16x32_bf16 v[96:99], v[228:231], v[144:147], v[96:99]\n"
    "ds_read_b128 v[168:171], v24 offset:38912\n"
    "v_mfma_f32_16x16x32_bf16 v[100:103], v[228:231], v[148:151], v[100:103]\n"
    "v_mfma_f32_16x16x32_bf16 v[104:107], v[228:231], v[152:155], v[104:107]\n"
    "v_mfma_f32_16x16x32_bf16 v[108:111], v[228:231], v[156:159], v[108:111]\n"
    "s_waitcnt vmcnt(28)\n"
    "v_mfma_f32_16x16x32_bf16 v[112:115], v[232:235], v[144:147], v[112:115]\n"
    "ds_read_b128 v[172:175], v24 offset:39936\n"
    "v_mfma_f32_16x16x32_bf16 v[116:119], v[232:235], v[148:151], v[116:119]\n"
    "v_mfma_f32_16x16x32_bf16 v[120:123], v[232:235], v[152:155], v[120:123]\n"
    "v_mfma_f32_16x16x32_bf16 v[124:127], v[232:235], v[156:159], v[124:127]\n"
    "s_waitcnt vmcnt(27) lgkmcnt(3)\n"
    "v_mfma_f32_16x16x32_bf16 v[64:67], v[244:247], v[160:163], v[64:67]\n"
    "ds_read_b128 v[144:147], v24 offset:40960\n"
    "s_waitcnt lgkmcnt(3)\n"
    "v_mfma_f32_16x16x32_bf16 v[68:71], v[244:247], v[164:167], v[68:71]\n"
    "s_waitcnt lgkmcnt(2)\n"
    "v_mfma_f32_16x16x32_bf16 v[72:75], v[244:247], v[168:171], v[72:75]\n"
    "s_waitcnt lgkmcnt(1)\n"
    "v_mfma_f32_16x16x32_bf16 v[76:79], v[244:247], v[172:175], v[76:79]\n"
    "s_waitcnt vmcnt(26)\n"
    "v_mfma_f32_16x16x32_bf16 v[80:83], v[248:251], v[160:163], v[80:83]\n"
    "ds_read_b128 v[148:151], v24 offset:41984\n"
    "v_mfma_f32_16x16x32_bf16 v[84:87], v[248:251], v[164:167], v[84:87]\n"
    "v_mfma_f32_16x16x32_bf16 v[88:91], v[248:251], v[168:171], v[88:91]\n"
    "v_mfma_f32_16x16x32_bf16 v[92:95], v[248:251], v[172:175], v[92:95]\n"
    "s_waitcnt vmcnt(25)\n"
    "v_mfma_f32_16x16x32_bf16 v[96:99], v[196:199], v[160:163], v[96:99]\n"
    "ds_read_b128 v[152:155], v24 offset:43008\n"
    "v_mfma_f32_16x16x32_bf16 v[100:103], v[196:199], v[164:167], v[100:103]\n"
    "v_mfma_f32_16x16x32_bf16 v[104:107], v[196:199], v[168:171], v[104:107]\n"
    "v_mfma_f32_16x16x32_bf16 v[108:111], v[196:199], v[172:175], v[108:111]\n"
    "s_waitcnt vmcnt(24)\n"
    "v_mfma_f32_16x16x32_bf16 v[112:115], v[200:203], v[160:163], v[112:115]\n"
    "ds_read_b128 v[156:159], v24 offset:44032\n"
    "v_mfma_f32_16x16x32_bf16 v[116:119], v[200:203], v[164:167], v[116:119]\n"
    "v_mfma_f32_16x16x32_bf16 v[120:123], v[200:203], v[168:171], v[120:123]\n"
    "v_mfma_f32_16x16x32_bf16 v[124:127], v[200:203], v[172:175], v[124:127]\n"
    "s_waitcnt vmcnt(23) lgkmcnt(3)\n"
    "v_mfma_f32_16x16x32_bf16 v[64:67], v[204:207], v[144:147], v[64:67]\n"
    "ds_read_b128 v[160:163], v24 offset:45056\n"
    "s_waitcnt lgkmcnt(3)\n"
    "v_mfma_f32_16x16x32_bf16 v[68:71], v[204:207], v[148:151], v[68:71]\n"
    "s_waitcnt lgkmcnt(2)\n"
    "v_mfma_f32_16x16x32_bf16 v[72:75], v[204:207], v[152:155], v[72:75]\n"
    "s_waitcnt lgkmcnt(1)\n"
    "v_mfma_f32_16x16x32_bf16 v[76:79], v[204:207], v[156:159], v[76:79]\n"
    "s_waitcnt vmcnt(22)\n"
    "v_mfma_f32_16x16x32_bf16 v[80:83], v[208:211], v[144:147], v[80:83]\n"
    "ds_read_b128 v[164:167], v24 offset:46080\n"
    "v_mfma_f32_16x16x32_bf16 v[84:87], v[208:211], v[148:151], v[84:87]\n"
    "v_mfma_f32_16x16x32_bf16 v[88:91], v[208:211], v[152:155], v[88:91]\n"
    "v_mfma_f32_16x16x32_bf16 v[92:95], v[208:211], v[156:159], v[92:95]\n"
    "s_waitcnt vmcnt(21)\n"
    "v_mfma_f32_16x16x32_bf16 v[96:99], v[212:215], v[144:147], v[96:99]\n"
    "ds_read_b128 v[168:171], v24 offset:47104\n"
    "v_mfma_f32_16x16x32_bf16 v[100:103], v[212:215], v[148:151], v[100:103]\n"
    "v_mfma_f32_16x16x32_bf16 v[104:107], v[212:215], v[152:155], v[104:107]\n"
    "v_mfma_f32_16x16x32_bf16 v[108:111], v[212:215], v[156:159], v[108:111]\n"
    "s_waitcnt vmcnt(20)\n"
    "v_mfma_f32_16x16x32_bf16 v[112:115], v[216:219], v[144:147], v[112:115]\n"
    "ds_read_b128 v[172:175], v24 offset:48128\n"
    "v_mfma_f32_16x16x32_bf16 v[116:119], v[216:219], v[148:151], v[116:119]\n"
    "v_mfma_f32_16x16x32_bf16 v[120:123], v[216:219], v[152:155], v[120:123]\n"
    "v_mfma_f32_16x16x32_bf16 v[124:127], v[216:219], v[156:159], v[124:127]\n"
    "s_waitcnt vmcnt(19) lgkmcnt(3)\n"
    "v_mfma_f32_16x16x32_bf16 v[64:67], v[220:223], v[160:163], v[64:67]\n"
    "ds_read_b128 v[144:147], v24 offset:49152\n"
    "s_waitcnt lgkmcnt(3)\n"
    "v_mfma_f32_16x16x32_bf16 v[68:71], v[220:223], v[164:167], v[68:71]\n"
    "s_waitcnt lgkmcnt(2)\n"
    "v_mfma_f32_16x16x32_bf16 v[72:75], v[220:223], v[168:171], v[72:75]\n"
    "s_waitcnt lgkmcnt(1)\n"
    "v_mfma_f32_16x16x32_bf16 v[76:79], v[220:223], v[172:175], v[76:79]\n"
    "s_waitcnt vmcnt(18)\n"
    "v_mfma_f32_16x16x32_bf16 v[80:83], v[28:31], v[160:163], v[80:83]\n"
    "ds_read_b128 v[148:151], v24 offset:50176\n"
    "v_mfma_f32_16x16x32_bf16 v[84:87], v[28:31], v[164:167], v[84:87]\n"
    "v_mfma_f32_16x16x32_bf16 v[88:91], v[28:31], v[168:171], v[88:91]\n"
    "v_mfma_f32_16x16x32_bf16 v[92:95], v[28:31], v[172:175], v[92:95]\n"
    "s_waitcnt vmcnt(17)\n"
    "v_mfma_f32_16x16x32_bf16 v[96:99], v[32:35], v[160:163], v[96:99]\n"
    "ds_read_b128 v[152:155], v24 offset:51200\n"
    "v_mfma_f32_16x16x32_bf16 v[100:103], v[32:35], v[164:167], v[100:103]\n"
    "v_mfma_f32_16x16x32_bf16 v[104:107], v[32:35], v[168:171], v[104:107]\n"
    "v_mfma_f32_16x16x32_bf16 v[108:111], v[32:35], v[172:175], v[108:111]\n"
    "s_waitcnt vmcnt(16)\n"
    "v_mfma_f32_16x16x32_bf16 v[112:115], v[36:39], v[160:163], v[112:115]\n"
    "ds_read_b128 v[156:159], v24 offset:52224\n"
    "v_mfma_f32_16x16x32_bf16 v[116:119], v[36:39], v[164:167], v[116:119]\n"
    "v_mfma_f32_16x16x32_bf16 v[120:123], v[36:39], v[168:171], v[120:123]\n"
    "v_mfma_f32_16x16x32_bf16 v[124:127], v[36:39], v[172:175], v[124:127]\n"
    "s_waitcnt vmcnt(15) lgkmcnt(3)\n"
    "v_mfma_f32_16x16x32_bf16 v[64:67], v[40:43], v[144:147], v[64:67]\n"
    "ds_read_b128 v[160:163], v24 offset:53248\n"
    "s_waitcnt lgkmcnt(3)\n"
    "v_mfma_f32_16x16x32_bf16 v[68:71], v[40:43], v[148:151], v[68:71]\n"
    "s_waitcnt lgkmcnt(2)\n"
    "v_mfma_f32_16x16x32_bf16 v[72:75], v[40:43], v[152:155], v[72:75]\n"
    "s_waitcnt lgkmcnt(1)\n"
    "v_mfma_f32_16x16x32_bf16 v[76:79], v[40:43], v[156:159], v[76:79]\n"
    "s_waitcnt vmcnt(14)\n"
    "v_mfma_f32_16x16x32_bf16 v[80:83], v[236:239], v[144:147], v[80:83]\n"
    "ds_read_b128 v[164:167], v24 offset:54272\n"
    "v_mfma_f32_16x16x32_bf16 v[84:87], v[236:239], v[148:151], v[84:87]\n"
    "v_mfma_f32_16x16x32_bf16 v[88:91], v[236:239], v[152:155], v[88:91]\n"
    "v_mfma_f32_16x16x32_bf16 v[92:95], v[236:239], v[156:159], v[92:95]\n"
    "s_waitcnt vmcnt(13)\n"
    "v_mfma_f32_16x16x32_bf16 v[96:99], v[176:179], v[144:147], v[96:99]\n"
    "ds_read_b128 v[168:171], v24 offset:55296\n"
    "v_mfma_f32_16x16x32_bf16 v[100:103], v[176:179], v[148:151], v[100:103]\n"
    "v_mfma_f32_16x16x32_bf16 v[104:107], v[176:179], v[152:155], v[104:107]\n"
    "v_mfma_f32_16x16x32_bf16 v[108:111], v[176:179], v[156:159], v[108:111]\n"
    "s_waitcnt vmcnt(12)\n"
    "v_mfma_f32_16x16x32_bf16 v[112:115], v[180:183], v[144:147], v[112:115]\n"
    "ds_read_b128 v[172:175], v24 offset:56320\n"
    "v_mfma_f32_16x16x32_bf16 v[116:119], v[180:183], v[148:151], v[116:119]\n"
    "v_mfma_f32_16x16x32_bf16 v[120:123], v[180:183], v[152:155], v[120:123]\n"
    "v_mfma_f32_16x16x32_bf16 v[124:127], v[180:183], v[156:159], v[124:127]\n"
    "s_waitcnt vmcnt(11) lgkmcnt(3)\n"
    "v_mfma_f32_16x16x32_bf16 v[64:67], v[128:131], v[160:163], v[64:67]\n"
    "ds_read_b128 v[144:147], v24 offset:57344\n"
    "s_waitcnt lgkmcnt(3)\n"
    "v_mfma_f32_16x16x32_bf16 v[68:71], v[128:131], v[164:167], v[68:71]\n"
    "s_waitcnt lgkmcnt(2)\n"
    "v_mfma_f32_16x16x32_bf16 v[72:75], v[128:131], v[168:171], v[72:75]\n"
    "s_waitcnt lgkmcnt(1)\n"
    "v_mfma_f32_16x16x32_bf16 v[76:79], v[128:131], v[172:175], v[76:79]\n"
    "s_waitcnt vmcnt(10)\n"
    "v_mfma_f32_16x16x32_bf16 v[80:83], v[132:135], v[160:163], v[80:83]\n"
    "ds_read_b128 v[148:151], v24 offset:58368\n"
    "v_mfma_f32_16x16x32_bf16 v[84:87], v[132:135], v[164:167], v[84:87]\n"
    "v_mfma_f32_16x16x32_bf16 v[88:91], v[132:135], v[168:171], v[88:91]\n"
    "v_mfma_f32_16x16x32_bf16 v[92:95], v[132:135], v[172:175], v[92:95]\n"
    "s_waitcnt vmcnt(9)\n"
    "v_mfma_f32_16x16x32_bf16 v[96:99], v[136:139], v[160:163], v[96:99]\n"
    "ds_read_b128 v[152:155], v24 offset:59392\n"
    "v_mfma_f32_16x16x32_bf16 v[100:103], v[136:139], v[164:167], v[100:103]\n"
    "v_mfma_f32_16x16x32_bf16 v[104:107], v[136:139], v[168:171], v[104:107]\n"
    "v_mfma_f32_16x16x32_bf16 v[108:111], v[136:139], v[172:175], v[108:111]\n"
    "s_waitcnt vmcnt(8)\n"
    "v_mfma_f32_16x16x32_bf16 v[112:115], v[140:143], v[160:163], v[112:115]\n"
    "ds_read_b128 v[156:159], v24 offset:60416\n"
    "v_mfma_f32_16x16x32_bf16 v[116:119], v[140:143], v[164:167], v[116:119]\n"
    "v_mfma_f32_16x16x32_bf16 v[120:123], v[140:143], v[168:171], v[120:123]\n"
    "v_mfma_f32_16x16x32_bf16 v[124:127], v[140:143], v[172:175], v[124:127]\n"
    "s_waitcnt vmcnt(7) lgkmcnt(3)\n"
    "v_mfma_f32_16x16x32_bf16 v[64:67], v[44:47], v[144:147], v[64:67]\n"
    "ds_read_b128 v[160:163], v24 offset:61440\n"
    "s_waitcnt lgkmcnt(3)\n"
    "v_mfma_f32_16x16x32_bf16 v[68:71], v[44:47], v[148:151], v[68:71]\n"
    "s_waitcnt lgkmcnt(2)\n"
    "v_mfma_f32_16x16x32_bf16 v[72:75], v[44:47], v[152:155], v[72:75]\n"
    "s_waitcnt lgkmcnt(1)\n"
    "v_mfma_f32_16x16x32_bf16 v[76:79], v[44:47], v[156:159], v[76:79]\n"
    "s_waitcnt vmcnt(6)\n"
    "v_mfma_f32_16x16x32_bf16 v[80:83], v[48:51], v[144:147], v[80:83]\n"
    "ds_read_b128 v[164:167], v24 offset:62464\n"
    "v_mfma_f32_16x16x32_bf16 v[84:87], v[48:51], v[148:151], v[84:87]\n"
    "v_mfma_f32_16x16x32_bf16 v[88:91], v[48:51], v[152:155], v[88:91]\n"
    "v_mfma_f32_16x16x32_bf16 v[92:95], v[48:51], v[156:159], v[92:95]\n"
    "s_waitcnt vmcnt(5)\n"
    "v_mfma_f32_16x16x32_bf16 v[96:99], v[184:187], v[144:147], v[96:99]\n"
    "ds_read_b128 v[168:171], v24 offset:63488\n"
    "v_mfma_f32_16x16x32_bf16 v[100:103], v[184:187], v[148:151], v[100:103]\n"
    "v_mfma_f32_16x16x32_bf16 v[104:107], v[184:187], v[152:155], v[104:107]\n"
    "v_mfma_f32_16x16x32_bf16 v[108:111], v[184:187], v[156:159], v[108:111]\n"
    "s_waitcnt vmcnt(4)\n"
    "v_mfma_f32_16x16x32_bf16 v[112:115], v[188:191], v[144:147], v[112:115]\n"
    "ds_read_b128 v[172:175], v24 offset:64512\n"
    "v_mfma_f32_16x16x32_bf16 v[116:119], v[188:191], v[148:151], v[116:119]\n"
    "v_mfma_f32_16x16x32_bf16 v[120:123], v[188:191], v[152:155], v[120:123]\n"
    "v_mfma_f32_16x16x32_bf16 v[124:127], v[188:191], v[156:159], v[124:127]\n"
    "s_waitcnt vmcnt(3) lgkmcnt(3)\n"
    "v_mfma_f32_16x16x32_bf16 v[64:67], v[192:195], v[160:163], v[64:67]\n"
    "s_waitcnt lgkmcnt(2)\n"
    "v_mfma_f32_16x16x32_bf16 v[68:71], v[192:195], v[164:167], v[68:71]\n"
    "s_waitcnt lgkmcnt(1)\n"
    "v_mfma_f32_16x16x32_bf16 v[72:75], v[192:195], v[168:171], v[72:75]\n"
    "s_waitcnt lgkmcnt(0)\n"
    "v_mfma_f32_16x16x32_bf16 v[76:79], v[192:195], v[172:175], v[76:79]\n"
    "s_waitcnt vmcnt(2)\n"
    "v_mfma_f32_16x16x32_bf16 v[80:83], v[52:55], v[160:163], v[80:83]\n"
    "v_mfma_f32_16x16x32_bf16 v[84:87], v[52:55], v[164:167], v[84:87]\n"
    "v_mfma_f32_16x16x32_bf16 v[88:91], v[52:55], v[168:171], v[88:91]\n"
    "v_mfma_f32_16x16x32_bf16 v[92:95], v[52:55], v[172:175], v[92:95]\n"
    "s_waitcnt vmcnt(1)\n"
    "v_mfma_f32_16x16x32_bf16 v[96:99], v[56:59], v[160:163], v[96:99]\n"
    "v_mfma_f32_16x16x32_bf16 v[100:103], v[56:59], v[164:167], v[100:103]\n"
    "v_mfma_f32_16x16x32_bf16 v[104:107], v[56:59], v[168:171], v[104:107]\n"
    "v_mfma_f32_16x16x32_bf16 v[108:111], v[56:59], v[172:175], v[108:111]\n"
    "s_waitcnt vmcnt(0)\n"
    "v_mfma_f32_16x16x32_bf16 v[112:115], v[240:243], v[160:163], v[112:115]\n"
    "v_mfma_f32_16x16x32_bf16 v[116:119], v[240:243], v[164:167], v[116:119]\n"
    "v_mfma_f32_16x16x32_bf16 v[120:123], v[240:243], v[168:171], v[120:123]\n"
    "v_mfma_f32_16x16x32_bf16 v[124:127], v[240:243], v[172:175], v[124:127]\n"
    "v_cvt_pk_bf16_f32 v64, v64, v65\n"
    "v_cvt_pk_bf16_f32 v65, v66, v67\n"
    "v_cvt_pk_bf16_f32 v66, v80, v81\n"
    "v_cvt_pk_bf16_f32 v67, v82, v83\n"
    "s_nop 0\n"
    "v_permlane16_swap_b32 v64, v66\n"
    "v_permlane16_swap_b32 v65, v67\n"
    "buffer_store_dwordx4 v[64:67], v3, s[20:23], s51 offen sc1\n"
    "v_cvt_pk_bf16_f32 v96, v96, v97\n"
    "v_cvt_pk_bf16_f32 v97, v98, v99\n"
    "v_cvt_pk_bf16_f32 v98, v112, v113\n"
    "v_cvt_pk_bf16_f32 v99, v114, v115\n"
    "s_nop 0\n"
    "v_permlane16_swap_b32 v96, v98\n"
    "v_permlane16_swap_b32 v97, v99\n"
    "buffer_store_dwordx4 v[96:99], v3, s[20:23], s52 offen sc1\n"
    "v_cvt_pk_bf16_f32 v68, v68, v69\n"
    "v_cvt_pk_bf16_f32 v69, v70, v71\n"
    "v_cvt_pk_bf16_f32 v70, v84, v85\n"
    "v_cvt_pk_bf16_f32 v71, v86, v87\n"
    "s_nop 0\n"
    "v_permlane16_swap_b32 v68, v70\n"
    "v_permlane16_swap_b32 v69, v71\n"
    "buffer_store_dwordx4 v[68:71], v3, s[20:23], s51 offen offset:2304 sc1\n"
    "v_cvt_pk_bf16_f32 v100, v100, v101\n"
    "v_cvt_pk_bf16_f32 v101, v102, v103\n"
    "v_cvt_pk_bf16_f32 v102, v116, v117\n"
    "v_cvt_pk_bf16_f32 v103, v118, v119\n"
    "s_nop 0\n"
    "v_permlane16_swap_b32 v100, v102\n"
    "v_permlane16_swap_b32 v101, v103\n"
    "buffer_store_dwordx4 v[100:103], v3, s[20:23], s52 offen offset:2304 sc1\n"
    "s_add_u32 s51, s51, 4608\n"
    "s_add_u32 s52, s52, 4608\n"
    "v_cvt_pk_bf16_f32 v72, v72, v73\n"
    "v_cvt_pk_bf16_f32 v73, v74, v75\n"
    "v_cvt_pk_bf16_f32 v74, v88, v89\n"
    "v_cvt_pk_bf16_f32 v75, v90, v91\n"
    "s_nop 0\n"
    "v_permlane16_swap_b32 v72, v74\n"
    "v_permlane16_swap_b32 v73, v75\n"
    "buffer_store_dwordx4 v[72:75], v3, s[20:23], s51 offen sc1\n"
    "v_cvt_pk_bf16_f32 v104, v104, v105\n"
    "v_cvt_pk_bf16_f32 v105, v106, v107\n"
    "v_cvt_pk_bf16_f32 v106, v120, v121\n"
    "v_cvt_pk_bf16_f32 v107, v122, v123\n"
    "s_nop 0\n"
    "v_permlane16_swap_b32 v104, v106\n"
    "v_permlane16_swap_b32 v105, v107\n"
    "buffer_store_dwordx4 v[104:107], v3, s[20:23], s52 offen sc1\n"
    "v_cvt_pk_bf16_f32 v76, v76, v77\n"
    "v_cvt_pk_bf16_f32 v77, v78, v79\n"
    "v_cvt_pk_bf16_f32 v78, v92, v93\n"
    "v_cvt_pk_bf16_f32 v79, v94, v95\n"
    "s_nop 0\n"
    "v_permlane16_swap_b32 v76, v78\n"
    "v_permlane16_swap_b32 v77, v79\n"
    "buffer_store_dwordx4 v[76:79], v3, s[20:23], s51 offen offset:2304 sc1\n"
    "v_cvt_pk_bf16_f32 v108, v108, v109\n"
    "v_cvt_pk_bf16_f32 v109, v110, v111\n"
    "v_cvt_pk_bf16_f32 v110, v124, v125\n"
    "v_cvt_pk_bf16_f32 v111, v126, v127\n"
    "s_nop 0\n"
    "v_permlane16_swap_b32 v108, v110\n"
    "v_permlane16_swap_b32 v109, v111\n"
    "buffer_store_dwordx4 v[108:111], v3, s[20:23], s52 offen offset:2304 sc1\n"
    "s_setprio 0\n"
    "v_and_b32 v88, 15, v2\n"
    "v_lshrrev_b32 v89, 4, v2\n"
    "v_lshlrev_b32 v90, 4, v88\n"
    "s_mul_i32 s44, s36, 1024\n"
    "s_add_u32 s44, s44, 147456\n"
    "v_add_u32 v91, s44, v1\n"
    "v_lshlrev_b32 v92, 5, v89\n"
    "v_add_u32 v92, 162080, v92\n"
    "ds_read_b128 v[122:125], v91\n"
    "ds_read_b128 v[112:115], v92\n"
    "ds_read_b128 v[116:119], v92 offset:16\n"
    "s_mul_i32 s44, s39, 256\n"
    "s_add_u32 s44, s44, 155648\n"
    "v_lshl_add_u32 v91, v89, 2, v90\n"
    "v_add_u32 v91, s44, v91\n"
    "v_lshlrev_b32 v92, 3, v89\n"
    "v_add_u32 v92, 162336, v92\n"
    "ds_read_b32 v126, v91\n"
    "ds_read_b64 v[120:121], v92\n"
    "v_mul_u32_u24 v84, 9216, v89\n"
    "v_add_u32 v84, v84, v90\n"
    "s_mul_i32 s44, s39, 4096\n"
    "s_add_u32 s44, s44, 0\n"
    "v_lshl_add_u32 v81, v89, 9, v90\n"
    "v_add_u32 v81, s44, v81\n"
    "v_mul_u32_u24 v88, 1152, v88\n"
    "v_lshl_add_u32 v82, v89, 4, v88\n"
    "v_lshl_add_u32 v83, v89, 2, v88\n"
    "v_mov_b32 v85, 0x3f80\n"
    "v_mov_b32 v86, 0x3f800000\n"
    "s_lshl_b32 s44, s38, 2\n"
    "s_add_u32 s44, s44, s39\n"
    "s_mul_i32 s44, s44, 18432\n"
    "s_add_u32 s44, s44, 0\n"
    "s_mul_i32 s46, s37, 144\n"
    "s_add_u32 s46, s46, s44\n"
    "s_lshl_b32 s45, s38, 3\n"
    "s_add_u32 s45, s45, s37\n"
    "s_mul_i32 s45, s45, 73728\n"
    "s_add_u32 s45, s45, 27471872\n"
    "s_mul_i32 s44, s39, 2304\n"
    "s_add_u32 s44, s44, s45\n"
    "s_mov_b32 s45, s44\n"
    "s_waitcnt lgkmcnt(4)\n"
    "v_lshlrev_b32 v96, 16, v122\n"
    "v_and_b32 v97, 0xffff0000, v122\n"
    "v_lshlrev_b32 v98, 16, v123\n"
    "v_and_b32 v99, 0xffff0000, v123\n"
    "v_lshlrev_b32 v100, 16, v124\n"
    "v_and_b32 v101, 0xffff0000, v124\n"
    "v_lshlrev_b32 v102, 16, v125\n"
    "v_and_b32 v103, 0xffff0000, v125\n"
    "s_waitcnt lgkmcnt(1)\n"
    "v_lshlrev_b32 v104, 16, v126\n"
    "v_and_b32 v105, 0xffff0000, v126\n"
    "v_and_b32 v12, 3, v2\n"
    "v_add_u32 v12, s37, v12\n"
    "v_add_u32 v12, 4, v12\n"
    "v_and_b32 v12, 7, v12\n"
    "v_lshlrev_b32 v12, 2, v12\n"
    "v_and_b32 v13, 7, v2\n"
    "v_min_u32 v14, 2, v13\n"
    "v_add_u32 v14, s37, v14\n"
    "v_add_u32 v14, 1, v14\n"
    "v_and_b32 v14, 7, v14\n"
    "v_add_u32 v13, 8, v13\n"
    "v_and_b32 v15, 8, v2\n"
    "v_cmp_eq_u32 vcc, 0, v15\n"
    "s_nop 1\n"
    "v_cndmask_b32 v13, v13, v14, vcc\n"
    "v_lshlrev_b32 v10, 2, v13\n"
    "s_mul_i32 s44, s39, 2048\n"
    "s_add_u32 s44, s44, 16384\n"
    "v_add_u32 v13, s44, v1\n"
    "s_waitcnt vmcnt(0)\n"
    "buffer_store_dword v9, v5, s[20:23], s98 offen sc1\n"
    "s_barrier\n"
    "buffer_load_dword v8, v12, s[20:23], s99 offen sc1\n"
    "s_mov_b32 s60, 0x8000\n"
    ".Lk_sp_75:\n"
    "s_waitcnt vmcnt(0)\n"
    "v_cmp_ne_u32 vcc, s95, v8\n"
    "s_cbranch_vccz .Lk_spd_76\n"
    "s_sub_u32 s60, s60, 1\n"
    "s_cbranch_scc1 .Lk_spd_76\n"
    "s_sleep 4\n"
    "buffer_load_dword v8, v12, s[20:23], s99 offen sc1\n"
    "s_branch .Lk_sp_75\n"
    ".Lk_spd_76:\n"
    "s_add_u32 s47, s37, 4\n"
    "s_and_b32 s47, s47, 7\n"
    "s_mul_i32 s47, s47, 9216\n"
    "s_add_u32 s47, s47, s45\n"
    "buffer_load_dwordx4 v[28:31], v1, s[20:23], s47 offen sc1\n"
    "buffer_load_dwordx4 v[32:35], v1, s[20:23], s47 offen offset:1024 sc1\n"
    "s_add_u32 s47, s37, 5\n"
    "s_and_b32 s47, s47, 7\n"
    "s_mul_i32 s47, s47, 9216\n"
    "s_add_u32 s47, s47, s45\n"
    "buffer_load_dwordx4 v[36:39], v1, s[20:23], s47 offen sc1\n"
    "buffer_load_dwordx4 v[40:43], v1, s[20:23], s47 offen offset:1024 sc1\n"
    "s_waitcnt vmcnt(3)\n"
    "v_dot2_f32_bf16 v128, v28, v85, 0\n"
    "s_add_u32 s47, s37, 6\n"
    "s_and_b32 s47, s47, 7\n"
    "s_mul_i32 s47, s47, 9216\n"
    "s_add_u32 s47, s47, s45\n"
    "buffer_load_dwordx4 v[44:47], v1, s[20:23], s47 offen sc1\n"
    "v_dot2_f32_bf16 v129, v28, v86, 0\n"
    "v_dot2_f32_bf16 v130, v29, v85, 0\n"
    "v_dot2_f32_bf16 v131, v29, v86, 0\n"
    "v_dot2_f32_bf16 v132, v30, v85, 0\n"
    "v_dot2_f32_bf16 v133, v30, v86, 0\n"
    "v_dot2_f32_bf16 v134, v31, v85, 0\n"
    "v_dot2_f32_bf16 v135, v31, v86, 0\n"
    "s_waitcnt vmcnt(3)\n"
    "v_dot2_f32_bf16 v136, v32, v85, 0\n"
    "buffer_load_dwordx4 v[48:51], v1, s[20:23], s47 offen offset:1024 sc1\n"
    "v_dot2_f32_bf16 v137, v32, v86, 0\n"
    "v_dot2_f32_bf16 v138, v33, v85, 0\n"
    "v_dot2_f32_bf16 v139, v33, v86, 0\n"
    "v_dot2_f32_bf16 v140, v34, v85, 0\n"
    "v_dot2_f32_bf16 v141, v34, v86, 0\n"
    "v_dot2_f32_bf16 v142, v35, v85, 0\n"
    "v_dot2_f32_bf16 v143, v35, v86, 0\n"
    "s_waitcnt vmcnt(3)\n"
    "v_dot2_f32_bf16 v128, v36, v85, v128\n"
    "s_add_u32 s47, s37, 7\n"
    "s_and_b32 s47, s47, 7\n"
    "s_mul_i32 s47, s47, 9216\n"
    "s_add_u32 s47, s47, s45\n"
    "buffer_load_dwordx4 v[52:55], v1, s[20:23], s47 offen sc1\n"
    "v_dot2_f32_bf16 v129, v36, v86, v129\n"
    "v_dot2_f32_bf16 v130, v37, v85, v130\n"
    "v_dot2_f32_bf16 v131, v37, v86, v131\n"
    "v_dot2_f32_bf16 v132, v38, v85, v132\n"
    "v_dot2_f32_bf16 v133, v38, v86, v133\n"
    "v_dot2_f32_bf16 v134, v39, v85, v134\n"
    "v_dot2_f32_bf16 v135, v39, v86, v135\n"
    "s_waitcnt vmcnt(3)\n"
    "v_dot2_f32_bf16 v136, v40, v85, v136\n"
    "buffer_load_dwordx4 v[56:59], v1, s[20:23], s47 offen offset:1024 sc1\n"
    "v_dot2_f32_bf16 v137, v40, v86, v137\n"
    "v_dot2_f32_bf16 v138, v41, v85, v138\n"
    "v_dot2_f32_bf16 v139, v41, v86, v139\n"
    "v_dot2_f32_bf16 v140, v42, v85, v140\n"
    "v_dot2_f32_bf16 v141, v42, v86, v141\n"
    "v_dot2_f32_bf16 v142, v43, v85, v142\n"
    "v_dot2_f32_bf16 v143, v43, v86, v143\n"
    "s_waitcnt vmcnt(3)\n"
    "v_dot2_f32_bf16 v128, v44, v85, v128\n"
    "v_dot2_f32_bf16 v129, v44, v86, v129\n"
    "v_dot2_f32_bf16 v130, v45, v85, v130\n"
    "v_dot2_f32_bf16 v131, v45, v86, v131\n"
    "v_dot2_f32_bf16 v132, v46, v85, v132\n"
    "v_dot2_f32_bf16 v133, v46, v86, v133\n"
    "v_dot2_f32_bf16 v134, v47, v85, v134\n"
    "v_dot2_f32_bf16 v135, v47, v86, v135\n"
    "s_waitcnt vmcnt(2)\n"
    "v_dot2_f32_bf16 v136, v48, v85, v136\n"
    "v_dot2_f32_bf16 v137, v48, v86, v137\n"
    "v_dot2_f32_bf16 v138, v49, v85, v138\n"
    "v_dot2_f32_bf16 v139, v49, v86, v139\n"
    "v_dot2_f32_bf16 v140, v50, v85, v140\n"
    "v_dot2_f32_bf16 v141, v50, v86, v141\n"
    "v_dot2_f32_bf16 v142, v51, v85, v142\n"
    "v_dot2_f32_bf16 v143, v51, v86, v143\n"
    "s_waitcnt vmcnt(1)\n"
    "v_dot2_f32_bf16 v128, v52, v85, v128\n"
    "v_dot2_f32_bf16 v129, v52, v86, v129\n"
    "v_dot2_f32_bf16 v130, v53, v85, v130\n"
    "v_dot2_f32_bf16 v131, v53, v86, v131\n"
    "v_dot2_f32_bf16 v132, v54, v85, v132\n"
    "v_dot2_f32_bf16 v133, v54, v86, v133\n"
    "v_dot2_f32_bf16 v134, v55, v85, v134\n"
    "v_dot2_f32_bf16 v135, v55, v86, v135\n"
    "s_waitcnt vmcnt(0)\n"
    "v_dot2_f32_bf16 v136, v56, v85, v136\n"
    "v_dot2_f32_bf16 v137, v56, v86, v137\n"
    "v_dot2_f32_bf16 v138, v57, v85, v138\n"
    "v_dot2_f32_bf16 v139, v57, v86, v139\n"
    "v_dot2_f32_bf16 v140, v58, v85, v140\n"
    "v_dot2_f32_bf16 v141, v58, v86, v141\n"
    "v_dot2_f32_bf16 v142, v59, v85, v142\n"
    "v_dot2_f32_bf16 v143, v59, v86, v143\n"
    "ds_write_b128 v13, v[136:139]\n"
    "s_nop 1\n"
    "ds_write_b128 v13, v[140:143] offset:1024\n"
    "s_cmp_lg_u32 s36, 0\n"
    "s_cbranch_scc1 .Lk_es_77\n"
    "buffer_load_dword v8, v10, s[20:23], s99 offen sc1\n"
    ".Lk_es_77:\n"
    "s_cmp_lg_u32 s36, 0\n"
    "s_cbranch_scc1 .Lk_polld_79\n"
    "s_mov_b32 s60, 0x8000\n"
    ".Lk_poll_78:\n"
    "s_waitcnt vmcnt(0)\n"
    "v_cmp_ne_u32 vcc, s95, v8\n"
    "s_cbranch_vccz .Lk_polld_79\n"
    "s_sub_u32 s60, s60, 1\n"
    "s_cbranch_scc1 .Lk_polld_79\n"
    "s_sleep 1\n"
    "buffer_load_dword v8, v10, s[20:23], s99 offen sc1\n"
    "s_branch .Lk_poll_78\n"
    ".Lk_polld_79:\n"
    "s_waitcnt lgkmcnt(0)\n"
    "s_barrier\n"
    "s_setprio 2\n"
    "s_add_u32 s47, s45, 36864\n"
    "buffer_load_dwordx4 v[56:59], v84, s[20:23], s45 offen offset:2048 sc1\n"
    "buffer_load_dwordx4 v[60:63], v84, s[20:23], s47 offen offset:2048 sc1\n"
    "s_add_u32 s44, s37, 1\n"
    "s_and_b32 s44, s44, 7\n"
    "s_mul_i32 s44, s44, 9216\n"
    "s_add_u32 s44, s44, s45\n"
    "buffer_load_dwordx4 v[28:31], v1, s[20:23], s44 offen sc1\n"
    "s_add_u32 s44, s37, 2\n"
    "s_and_b32 s44, s44, 7\n"
    "s_mul_i32 s44, s44, 9216\n"
    "s_add_u32 s44, s44, s45\n"
    "buffer_load_dwordx4 v[32:35], v1, s[20:23], s44 offen sc1\n"
    "s_add_u32 s44, s37, 3\n"
    "s_and_b32 s44, s44, 7\n"
    "s_mul_i32 s44, s44, 9216\n"
    "s_add_u32 s44, s44, s45\n"
    "buffer_load_dwordx4 v[36:39], v1, s[20:23], s44 offen sc1\n"
    "ds_read_b128 v[64:67], v81\n"
    "ds_read_b128 v[68:71], v81 offset:256\n"
    "s_waitcnt lgkmcnt(1)\n"
    "v_pk_add_f32 v[64:65], v[64:65], v[128:129]\n"
    "v_pk_add_f32 v[66:67], v[66:67], v[130:131]\n"
    "s_waitcnt lgkmcnt(0)\n"
    "v_pk_add_f32 v[68:69], v[68:69], v[132:133]\n"
    "v_pk_add_f32 v[70:71], v[70:71], v[134:135]\n"
    "s_waitcnt vmcnt(4)\n"
    "v_dot2_f32_bf16 v72, v56, v85, 0\n"
    "v_dot2_f32_bf16 v73, v56, v86, 0\n"
    "v_dot2_f32_bf16 v74, v57, v85, 0\n"
    "v_dot2_f32_bf16 v75, v57, v86, 0\n"
    "v_dot2_f32_bf16 v76, v58, v85, 0\n"
    "v_dot2_f32_bf16 v77, v58, v86, 0\n"
    "v_dot2_f32_bf16 v78, v59, v85, 0\n"
    "v_dot2_f32_bf16 v79, v59, v86, 0\n"
    "s_waitcnt vmcnt(3)\n"
    "v_dot2_f32_bf16 v72, v60, v85, v72\n"
    "v_dot2_f32_bf16 v73, v60, v86, v73\n"
    "v_dot2_f32_bf16 v74, v61, v85, v74\n"
    "v_dot2_f32_bf16 v75, v61, v86, v75\n"
    "v_dot2_f32_bf16 v76, v62, v85, v76\n"
    "v_dot2_f32_bf16 v77, v62, v86, v77\n"
    "v_dot2_f32_bf16 v78, v63, v85, v78\n"
    "v_dot2_f32_bf16 v79, v63, v86, v79\n"
    "v_permlane16_swap_b32 v72, v74\n"
    "s_nop 0\n"
    "v_permlane16_swap_b32 v76, v78\n"
    "v_permlane16_swap_b32 v73, v75\n"
    "v_permlane16_swap_b32 v77, v79\n"
    "v_add_f32 v72, v72, v74\n"
    "v_add_f32 v76, v76, v78\n"
    "v_add_f32 v73, v73, v75\n"
    "v_add_f32 v77, v77, v79\n"
    "v_permlane32_swap_b32 v72, v76\n"
    "s_nop 0\n"
    "v_permlane32_swap_b32 v73, v77\n"
    "v_add_f32 v72, v72, v76\n"
    "v_add_f32 v73, v73, v77\n"
    "v_pk_fma_f32 v[72:73], v[72:73], v[120:121], v[104:105]\n"
    "v_cvt_pk_bf16_f32 v72, v72, v73\n"
    "buffer_store_dword v72, v83, s[32:35], s46 offen offset:128\n"
    "s_waitcnt vmcnt(3)\n"
    "v_dot2_f32_bf16 v64, v28, v85, v64\n"
    "v_dot2_f32_bf16 v65, v28, v86, v65\n"
    "v_dot2_f32_bf16 v66, v29, v85, v66\n"
    "v_dot2_f32_bf16 v67, v29, v86, v67\n"
    "v_dot2_f32_bf16 v68, v30, v85, v68\n"
    "v_dot2_f32_bf16 v69, v30, v86, v69\n"
    "v_dot2_f32_bf16 v70, v31, v85, v70\n"
    "v_dot2_f32_bf16 v71, v31, v86, v71\n"
    "s_waitcnt vmcnt(2)\n"
    "v_dot2_f32_bf16 v64, v32, v85, v64\n"
    "v_dot2_f32_bf16 v65, v32, v86, v65\n"
    "v_dot2_f32_bf16 v66, v33, v85, v66\n"
    "v_dot2_f32_bf16 v67, v33, v86, v67\n"
    "v_dot2_f32_bf16 v68, v34, v85, v68\n"
    "v_dot2_f32_bf16 v69, v34, v86, v69\n"
    "v_dot2_f32_bf16 v70, v35, v85, v70\n"
    "v_dot2_f32_bf16 v71, v35, v86, v71\n"
    "s_waitcnt vmcnt(1)\n"
    "v_dot2_f32_bf16 v64, v36, v85, v64\n"
    "v_dot2_f32_bf16 v65, v36, v86, v65\n"
    "v_dot2_f32_bf16 v66, v37, v85, v66\n"
    "v_dot2_f32_bf16 v67, v37, v86, v67\n"
    "v_dot2_f32_bf16 v68, v38, v85, v68\n"
    "v_dot2_f32_bf16 v69, v38, v86, v69\n"
    "v_dot2_f32_bf16 v70, v39, v85, v70\n"
    "v_dot2_f32_bf16 v71, v39, v86, v71\n"
    "v_pk_fma_f32 v[64:65], v[64:65], v[112:113], v[96:97]\n"
    "v_pk_fma_f32 v[66:67], v[66:67], v[114:115], v[98:99]\n"
    "v_pk_fma_f32 v[68:69], v[68:69], v[116:117], v[100:101]\n"
    "v_pk_fma_f32 v[70:71], v[70:71], v[118:119], v[102:103]\n"
    "v_cvt_pk_bf16_f32 v64, v64, v65\n"
    "v_cvt_pk_bf16_f32 v65, v66, v67\n"
    "v_cvt_pk_bf16_f32 v66, v68, v69\n"
    "v_cvt_pk_bf16_f32 v67, v70, v71\n"
    "buffer_store_dwordx4 v[64:67], v82, s[32:35], s46 offen\n"
    "s_branch .Lk_final_71\n"
    ".Lk_young_70:\n"
    "s_waitcnt vmcnt(0)\n"
    "s_barrier\n"
    "s_cmp_lg_u32 s36, 0\n"
    "s_cbranch_scc1 .Lk_pub_80\n"
    "buffer_store_dword v9, v5, s[20:23], s98 offen sc1\n"
    "buffer_load_dword v8, v10, s[20:23], s99 offen sc1\n"
    ".Lk_pub_80:\n"
    "s_mul_i32 s47, s37, 1327104\n"
    "s_add_u32 s47, s47, 13316096\n"
    "s_lshl_b32 s97, s37, 2\n"
    "s_add_u32 s97, s97, s39\n"
    "s_mul_i32 s97, s97, 18432\n"
    "s_add_u32 s97, s97, 663552\n"
    "s_add_u32 s97, s97, s47\n"
    "buffer_load_dwordx4 v[236:239], v1, s[20:23], s96 offen offset:3072 sc1\n"
    "buffer_load_dwordx4 v[240:243], v252, s[20:23], s96 offen offset:3072 sc1\n"
    "buffer_load_dwordx4 v[244:247], v253, s[20:23], s96 offen offset:3072 sc1\n"
    "buffer_load_dwordx4 v[248:251], v254, s[20:23], s96 offen offset:3072 sc1\n"
    "s_add_u32 s48, s37, s36\n"
    "s_add_u32 s48, s48, 1\n"
    "s_and_b32 s48, s48, 7\n"
    "s_lshl_b32 s49, s48, 7\n"
    "v_mov_b32 v14, s49\n"
    "v_lshlrev_b32 v15, 7, v2\n"
    "v_add_u32 v15, 32, v15\n"
    "s_cmp_eq_u32 s36, 7\n"
    "s_cselect_b64 vcc, -1, 0\n"
    "s_cselect_b32 s49, 8, 1\n"
    "v_cndmask_b32 v14, v14, v15, vcc\n"
    "v_cmp_gt_u32 vcc, s49, v2\n"
    "s_nop 1\n"
    "v_cndmask_b32 v5, v11, v14, vcc\n"
    "s_cmp_eq_u32 s36, 7\n"
    "s_cselect_b32 s58, 8, s48\n"
    "s_mul_i32 s58, s58, 73728\n"
    "s_add_u32 s58, s58, 663552\n"
    "s_add_u32 s58, s58, s47\n"
    "s_cmp_eq_u32 s36, 7\n"
    "s_cselect_b32 s48, 0, s48\n"
    "s_cselect_b32 s49, 2048, 0\n"
    "s_mov_b32 s47, 256\n"
    "s_cselect_b32 s47, 73728, s47\n"
    "s_lshl_b32 s51, s38, 3\n"
    "s_add_u32 s51, s51, s48\n"
    "s_lshl_b32 s51, s51, 3\n"
    "s_add_u32 s51, s51, s37\n"
    "s_mul_i32 s51, s51, 9216\n"
    "s_add_u32 s51, s51, 27471872\n"
    "s_add_u32 s51, s51, s49\n"
    "s_lshl_b32 s48, s47, 2\n"
    "s_add_u32 s52, s51, s48\n"
    "v_and_b32 v14, 15, v2\n"
    "v_lshrrev_b32 v15, 4, v2\n"
    "v_lshlrev_b32 v14, 4, v14\n"
    "v_and_b32 v16, 1, v15\n"
    "v_lshrrev_b32 v17, 1, v15\n"
    "v_lshl_add_u32 v16, v16, 1, v17\n"
    "v_mul_u32_u24 v16, s47, v16\n"
    "v_add_u32 v3, v16, v14\n"
    "s_mul_i32 s47, s36, 8192\n"
    "s_add_u32 s47, s47, 73728\n"
    "v_add_u32 v22, s47, v1\n"
    "v_add_u32 v24, 73728, v1\n"
    "v_add_u32 v25, 139264, v1\n"
    "s_mul_i32 s47, s39, 18432\n"
    "s_add_u32 s47, s47, 1024\n"
    "v_add_u32 v26, s47, v1\n"
    "v_lshlrev_b32 v15, 6, v15\n"
    "v_add_u32 v27, 156928, v15\n"
    "ds_read_b128 v[208:211], v27\n"
    "ds_read_b128 v[212:215], v27 offset:16\n"
    "ds_read_b128 v[216:219], v27 offset:32\n"
    "ds_read_b128 v[220:223], v27 offset:48\n"
    "s_mul_i32 s44, s38, 73728\n"
    "s_add_u32 s44, s44, 10694656\n"
    "s_add_u32 s44, s44, s47\n"
    "s_lshl_b32 s45, s38, 2\n"
    "s_add_u32 s45, s45, s39\n"
    "s_mul_i32 s45, s45, 2048\n"
    "s_add_u32 s45, s45, 13053952\n"
    "s_cmp_lg_u32 s36, 0\n"
    "s_cbranch_scc1 .Lk_polld_82\n"
    "s_mov_b32 s60, 0x8000\n"
    ".Lk_poll_81:\n"
    "s_waitcnt vmcnt(0)\n"
    "v_cmp_ne_u32 vcc, s95, v8\n"
    "s_cbranch_vccz .Lk_polld_82\n"
    "s_sub_u32 s60, s60, 1\n"
    "s_cbranch_scc1 .Lk_polld_82\n"
    "s_sleep 1\n"
    "buffer_load_dword v8, v10, s[20:23], s99 offen sc1\n"
    "s_branch .Lk_poll_81\n"
    ".Lk_polld_82:\n"
    "s_barrier\n"
    "s_setprio 3\n"
    "buffer_load_dwordx4 v[148:151], v1, s[20:23], s45 offen sc1\n"
    "buffer_load_dwordx4 v[152:155], v1, s[20:23], s45 offen offset:1024 sc1\n"
    "buffer_load_dwordx4 v[144:147], v1, s[20:23], s44 offen sc1\n"
    "s_setprio 2\n"
    "buffer_load_dwordx4 v[176:179], v1, s[20:23], s44 offen offset:2048 sc1\n"
    "s_add_u32 s44, s44, 4096\n"
    "buffer_load_dwordx4 v[180:183], v1, s[20:23], s44 offen sc1\n"
    "s_setprio 1\n"
    "buffer_load_dwordx4 v[184:187], v1, s[20:23], s44 offen offset:2048 sc1\n"
    "s_add_u32 s44, s44, 4096\n"
    "buffer_load_dwordx4 v[188:191], v1, s[20:23], s44 offen sc1\n"
    "buffer_load_dwordx4 v[192:195], v1, s[20:23], s44 offen offset:2048 sc1\n"
    "s_setprio 0\n"
    "s_add_u32 s44, s44, 4096\n"
    "buffer_load_dwordx4 v[196:199], v1, s[20:23], s44 offen sc1\n"
    "buffer_load_dwordx4 v[200:203], v1, s[20:23], s44 offen offset:2048 sc1\n"
    "s_add_u32 s44, s44, 4096\n"
    "buffer_load_dwordx4 v[204:207], v1, s[20:23], s44 offen sc1\n"
    "s_add_u32 s98, s98, 32768\n"
    "s_add_u32 s99, s99, 32768\n"
    "s_mul_i32 s98, s38, 1024\n"
    "s_add_u32 s98, s98, 131072\n"
    "s_lshl_b32 s48, s37, 2\n"
    "s_add_u32 s98, s98, s48\n"
    "s_setprio 1\n"
    "s_waitcnt vmcnt(10)\n"
    "v_pk_add_f32 v[148:149], v[148:149], v[150:151]\n"
    "s_waitcnt vmcnt(9)\n"
    "v_pk_add_f32 v[152:153], v[152:153], v[154:155]\n"
    "v_pk_add_f32 v[148:149], v[148:149], v[152:153]\n"
    "v_mov_b32 v151, 0x3ae38e39\n"
    "s_nop 0\n"
    "v_permlane16_swap_b32 v148, v149\n"
    "v_add_f32 v148, v148, v149\n"
    "v_mov_b32 v149, v148\n"
    "s_nop 1\n"
    "v_permlane32_swap_b32 v148, v149\n"
    "v_add_f32 v148, v148, v149\n"
    "v_mov_b32 v149, v148\n"
    "s_nop 1\n"
    "v_permlane16_swap_b32 v148, v149\n"
    "v_mul_f32 v148, v151, v148\n"
    "v_mul_f32 v150, v148, v148\n"
    "v_fma_f32 v149, v149, v151, -v150\n"
    "v_max_f32 v149, 0, v149\n"
    "v_add_f32 v149, 0x3727c5ac, v149\n"
    "v_rsq_f32 v12, v149\n"
    "s_nop 0\n"
    "v_mul_f32 v13, -v148, v12\n"
    "s_waitcnt vmcnt(8)\n"
    "v_lshlrev_b32 v14, 16, v144\n"
    "v_and_b32 v15, 0xffff0000, v144\n"
    "v_lshlrev_b32 v16, 16, v145\n"
    "v_and_b32 v17, 0xffff0000, v145\n"
    "v_fma_f32 v14, v14, v12, v13\n"
    "v_fma_f32 v15, v15, v12, v13\n"
    "v_fma_f32 v16, v16, v12, v13\n"
    "v_fma_f32 v17, v17, v12, v13\n"
    "s_waitcnt lgkmcnt(2)\n"
    "v_fma_f32 v14, v14, v208, v212\n"
    "v_fma_f32 v15, v15, v209, v213\n"
    "v_fma_f32 v16, v16, v210, v214\n"
    "v_fma_f32 v17, v17, v211, v215\n"
    "ds_read_b128 v[208:211], v27 offset:512\n"
    "ds_read_b128 v[212:215], v27 offset:528\n"
    "v_cvt_pk_bf16_f32 v144, v14, v15\n"
    "v_cvt_pk_bf16_f32 v145, v16, v17\n"
    "v_lshlrev_b32 v18, 16, v146\n"
    "v_and_b32 v19, 0xffff0000, v146\n"
    "v_lshlrev_b32 v20, 16, v147\n"
    "v_and_b32 v21, 0xffff0000, v147\n"
    "v_fma_f32 v18, v18, v12, v13\n"
    "v_fma_f32 v19, v19, v12, v13\n"
    "v_fma_f32 v20, v20, v12, v13\n"
    "v_fma_f32 v21, v21, v12, v13\n"
    "s_waitcnt lgkmcnt(2)\n"
    "v_fma_f32 v18, v18, v216, v220\n"
    "v_fma_f32 v19, v19, v217, v221\n"
    "v_fma_f32 v20, v20, v218, v222\n"
    "v_fma_f32 v21, v21, v219, v223\n"
    "ds_read_b128 v[216:219], v27 offset:544\n"
    "ds_read_b128 v[220:223], v27 offset:560\n"
    "v_cvt_pk_bf16_f32 v146, v18, v19\n"
    "v_cvt_pk_bf16_f32 v147, v20, v21\n"
    "ds_write_b128 v26, v[144:147]\n"
    "s_waitcnt lgkmcnt(0)\n"
    "s_barrier\n"
    "ds_read_b128 v[144:147], v1\n"
    "ds_read_b128 v[148:151], v1 offset:18432\n"
    "ds_read_b128 v[152:155], v7\n"
    "ds_read_b128 v[156:159], v7 offset:18432\n"
    "s_waitcnt vmcnt(7)\n"
    "v_lshlrev_b32 v14, 16, v176\n"
    "v_and_b32 v15, 0xffff0000, v176\n"
    "v_lshlrev_b32 v16, 16, v177\n"
    "v_and_b32 v17, 0xffff0000, v177\n"
    "v_fma_f32 v14, v14, v12, v13\n"
    "v_fma_f32 v15, v15, v12, v13\n"
    "v_fma_f32 v16, v16, v12, v13\n"
    "v_fma_f32 v17, v17, v12, v13\n"
    "v_fma_f32 v14, v14, v208, v212\n"
    "v_fma_f32 v15, v15, v209, v213\n"
    "v_fma_f32 v16, v16, v210, v214\n"
    "v_fma_f32 v17, v17, v211, v215\n"
    "ds_read_b128 v[208:211], v27 offset:1024\n"
    "ds_read_b128 v[212:215], v27 offset:1040\n"
    "v_cvt_pk_bf16_f32 v176, v14, v15\n"
    "v_cvt_pk_bf16_f32 v177, v16, v17\n"
    "v_lshlrev_b32 v18, 16, v178\n"
    "v_and_b32 v19, 0xffff0000, v178\n"
    "v_lshlrev_b32 v20, 16, v179\n"
    "v_and_b32 v21, 0xffff0000, v179\n"
    "v_fma_f32 v18, v18, v12, v13\n"
    "v_fma_f32 v19, v19, v12, v13\n"
    "v_fma_f32 v20, v20, v12, v13\n"
    "v_fma_f32 v21, v21, v12, v13\n"
    "v_fma_f32 v18, v18, v216, v220\n"
    "v_fma_f32 v19, v19, v217, v221\n"
    "v_fma_f32 v20, v20, v218, v222\n"
    "v_fma_f32 v21, v21, v219, v223\n"
    "ds_read_b128 v[216:219], v27 offset:1056\n"
    "ds_read_b128 v[220:223], v27 offset:1072\n"
    "v_cvt_pk_bf16_f32 v178, v18, v19\n"
    "v_cvt_pk_bf16_f32 v179, v20, v21\n"
    "ds_write_b128 v26, v[176:179] offset:2048\n"
    "s_waitcnt vmcnt(6)\n"
    "v_lshlrev_b32 v14, 16, v180\n"
    "v_and_b32 v15, 0xffff0000, v180\n"
    "v_lshlrev_b32 v16, 16, v181\n"
    "v_and_b32 v17, 0xffff0000, v181\n"
    "v_fma_f32 v14, v14, v12, v13\n"
    "v_fma_f32 v15, v15, v12, v13\n"
    "v_fma_f32 v16, v16, v12, v13\n"
    "v_fma_f32 v17, v17, v12, v13\n"
    "s_waitcnt lgkmcnt(3)\n"
    "v_fma_f32 v14, v14, v208, v212\n"
    "v_fma_f32 v15, v15, v209, v213\n"
    "v_fma_f32 v16, v16, v210, v214\n"
    "v_fma_f32 v17, v17, v211, v215\n"
    "ds_read_b128 v[208:211], v27 offset:1536\n"
    "ds_read_b128 v[212:215], v27 offset:1552\n"
    "v_cvt_pk_bf16_f32 v180, v14, v15\n"
    "v_cvt_pk_bf16_f32 v181, v16, v17\n"
    "v_lshlrev_b32 v18, 16, v182\n"
    "v_and_b32 v19, 0xffff0000, v182\n"
    "v_lshlrev_b32 v20, 16, v183\n"
    "v_and_b32 v21, 0xffff0000, v183\n"
    "v_fma_f32 v18, v18, v12, v13\n"
    "v_fma_f32 v19, v19, v12, v13\n"
    "v_fma_f32 v20, v20, v12, v13\n"
    "v_fma_f32 v21, v21, v12, v13\n"
    "s_waitcnt lgkmcnt(3)\n"
    "v_fma_f32 v18, v18, v216, v220\n"
    "v_fma_f32 v19, v19, v217, v221\n"
    "v_fma_f32 v20, v20, v218, v222\n"
    "v_fma_f32 v21, v21, v219, v223\n"
    "ds_read_b128 v[216:219], v27 offset:1568\n"
    "ds_read_b128 v[220:223], v27 offset:1584\n"
    "v_cvt_pk_bf16_f32 v182, v18, v19\n"
    "v_cvt_pk_bf16_f32 v183, v20, v21\n"
    "ds_write_b128 v26, v[180:183] offset:4096\n"
    "s_waitcnt lgkmcnt(0)\n"
    "s_barrier\n"
    "s_waitcnt vmcnt(5)\n"
    "v_lshlrev_b32 v14, 16, v184\n"
    "v_and_b32 v15, 0xffff0000, v184\n"
    "v_lshlrev_b32 v16, 16, v185\n"
    "v_and_b32 v17, 0xffff0000, v185\n"
    "v_fma_f32 v14, v14, v12, v13\n"
    "v_fma_f32 v15, v15, v12, v13\n"
    "v_fma_f32 v16, v16, v12, v13\n"
    "v_fma_f32 v17, v17, v12, v13\n"
    "v_fma_f32 v14, v14, v208, v212\n"
    "v_fma_f32 v15, v15, v209, v213\n"
    "v_fma_f32 v16, v16, v210, v214\n"
    "v_fma_f32 v17, v17, v211, v215\n"
    "ds_read_b128 v[208:211], v27 offset:2048\n"
    "ds_read_b128 v[212:215], v27 offset:2064\n"
    "v_cvt_pk_bf16_f32 v184, v14, v15\n"
    "v_cvt_pk_bf16_f32 v185, v16, v17\n"
    "v_lshlrev_b32 v18, 16, v186\n"
    "v_and_b32 v19, 0xffff0000, v186\n"
    "v_lshlrev_b32 v20, 16, v187\n"
    "v_and_b32 v21, 0xffff0000, v187\n"
    "v_fma_f32 v18, v18, v12, v13\n"
    "v_fma_f32 v19, v19, v12, v13\n"
    "v_fma_f32 v20, v20, v12, v13\n"
    "v_fma_f32 v21, v21, v12, v13\n"
    "v_fma_f32 v18, v18, v216, v220\n"
    "v_fma_f32 v19, v19, v217, v221\n"
    "v_fma_f32 v20, v20, v218, v222\n"
    "v_fma_f32 v21, v21, v219, v223\n"
    "ds_read_b128 v[216:219], v27 offset:2080\n"
    "ds_read_b128 v[220:223], v27 offset:2096\n"
    "v_cvt_pk_bf16_f32 v186, v18, v19\n"
    "v_cvt_pk_bf16_f32 v187, v20, v21\n"
    "ds_write_b128 v26, v[184:187] offset:6144\n"
    "s_waitcnt vmcnt(4)\n"
    "v_lshlrev_b32 v14, 16, v188\n"
    "v_and_b32 v15, 0xffff0000, v188\n"
    "v_lshlrev_b32 v16, 16, v189\n"
    "v_and_b32 v17, 0xffff0000, v189\n"
    "v_fma_f32 v14, v14, v12, v13\n"
    "v_fma_f32 v15, v15, v12, v13\n"
    "v_fma_f32 v16, v16, v12, v13\n"
    "v_fma_f32 v17, v17, v12, v13\n"
    "s_waitcnt lgkmcnt(3)\n"
    "v_fma_f32 v14, v14, v208, v212\n"
    "v_fma_f32 v15, v15, v209, v213\n"
    "v_fma_f32 v16, v16, v210, v214\n"
    "v_fma_f32 v17, v17, v211, v215\n"
    "ds_read_b128 v[208:211], v27 offset:2560\n"
    "ds_read_b128 v[212:215], v27 offset:2576\n"
    "v_cvt_pk_bf16_f32 v188, v14, v15\n"
    "v_cvt_pk_bf16_f32 v189, v16, v17\n"
    "v_lshlrev_b32 v18, 16, v190\n"
    "v_and_b32 v19, 0xffff0000, v190\n"
    "v_lshlrev_b32 v20, 16, v191\n"
    "v_and_b32 v21, 0xffff0000, v191\n"
    "v_fma_f32 v18, v18, v12, v13\n"
    "v_fma_f32 v19, v19, v12, v13\n"
    "v_fma_f32 v20, v20, v12, v13\n"
    "v_fma_f32 v21, v21, v12, v13\n"
    "s_waitcnt lgkmcnt(3)\n"
    "v_fma_f32 v18, v18, v216, v220\n"
    "v_fma_f32 v19, v19, v217, v221\n"
    "v_fma_f32 v20, v20, v218, v222\n"
    "v_fma_f32 v21, v21, v219, v223\n"
    "ds_read_b128 v[216:219], v27 offset:2592\n"
    "ds_read_b128 v[220:223], v27 offset:2608\n"
    "v_cvt_pk_bf16_f32 v190, v18, v19\n"
    "v_cvt_pk_bf16_f32 v191, v20, v21\n"
    "ds_write_b128 v26, v[188:191] offset:8192\n"
    "s_waitcnt vmcnt(3)\n"
    "v_lshlrev_b32 v14, 16, v192\n"
    "v_and_b32 v15, 0xffff0000, v192\n"
    "v_lshlrev_b32 v16, 16, v193\n"
    "v_and_b32 v17, 0xffff0000, v193\n"
    "v_fma_f32 v14, v14, v12, v13\n"
    "v_fma_f32 v15, v15, v12, v13\n"
    "v_fma_f32 v16, v16, v12, v13\n"
    "v_fma_f32 v17, v17, v12, v13\n"
    "s_waitcnt lgkmcnt(3)\n"
    "v_fma_f32 v14, v14, v208, v212\n"
    "v_fma_f32 v15, v15, v209, v213\n"
    "v_fma_f32 v16, v16, v210, v214\n"
    "v_fma_f32 v17, v17, v211, v215\n"
    "ds_read_b128 v[208:211], v27 offset:3072\n"
    "ds_read_b128 v[212:215], v27 offset:3088\n"
    "v_cvt_pk_bf16_f32 v192, v14, v15\n"
    "v_cvt_pk_bf16_f32 v193, v16, v17\n"
    "v_lshlrev_b32 v18, 16, v194\n"
    "v_and_b32 v19, 0xffff0000, v194\n"
    "v_lshlrev_b32 v20, 16, v195\n"
    "v_and_b32 v21, 0xffff0000, v195\n"
    "v_fma_f32 v18, v18, v12, v13\n"
    "v_fma_f32 v19, v19, v12, v13\n"
    "v_fma_f32 v20, v20, v12, v13\n"
    "v_fma_f32 v21, v21, v12, v13\n"
    "s_waitcnt lgkmcnt(3)\n"
    "v_fma_f32 v18, v18, v216, v220\n"
    "v_fma_f32 v19, v19, v217, v221\n"
    "v_fma_f32 v20, v20, v218, v222\n"
    "v_fma_f32 v21, v21, v219, v223\n"
    "ds_read_b128 v[216:219], v27 offset:3104\n"
    "ds_read_b128 v[220:223], v27 offset:3120\n"
    "v_cvt_pk_bf16_f32 v194, v18, v19\n"
    "v_cvt_pk_bf16_f32 v195, v20, v21\n"
    "ds_write_b128 v26, v[192:195] offset:10240\n"
    "s_waitcnt lgkmcnt(0)\n"
    "s_barrier\n"
    "s_waitcnt vmcnt(2)\n"
    "v_lshlrev_b32 v14, 16, v196\n"
    "v_and_b32 v15, 0xffff0000, v196\n"
    "v_lshlrev_b32 v16, 16, v197\n"
    "v_and_b32 v17, 0xffff0000, v197\n"
    "v_fma_f32 v14, v14, v12, v13\n"
    "v_fma_f32 v15, v15, v12, v13\n"
    "v_fma_f32 v16, v16, v12, v13\n"
    "v_fma_f32 v17, v17, v12, v13\n"
    "v_fma_f32 v14, v14, v208, v212\n"
    "v_fma_f32 v15, v15, v209, v213\n"
    "v_fma_f32 v16, v16, v210, v214\n"
    "v_fma_f32 v17, v17, v211, v215\n"
    "ds_read_b128 v[208:211], v27 offset:3584\n"
    "ds_read_b128 v[212:215], v27 offset:3600\n"
    "v_cvt_pk_bf16_f32 v196, v14, v15\n"
    "v_cvt_pk_bf16_f32 v197, v16, v17\n"
    "v_lshlrev_b32 v18, 16, v198\n"
    "v_and_b32 v19, 0xffff0000, v198\n"
    "v_lshlrev_b32 v20, 16, v199\n"
    "v_and_b32 v21, 0xffff0000, v199\n"
    "v_fma_f32 v18, v18, v12, v13\n"
    "v_fma_f32 v19, v19, v12, v13\n"
    "v_fma_f32 v20, v20, v12, v13\n"
    "v_fma_f32 v21, v21, v12, v13\n"
    "v_fma_f32 v18, v18, v216, v220\n"
    "v_fma_f32 v19, v19, v217, v221\n"
    "v_fma_f32 v20, v20, v218, v222\n"
    "v_fma_f32 v21, v21, v219, v223\n"
    "ds_read_b128 v[216:219], v27 offset:3616\n"
    "ds_read_b128 v[220:223], v27 offset:3632\n"
    "v_cvt_pk_bf16_f32 v198, v18, v19\n"
    "v_cvt_pk_bf16_f32 v199, v20, v21\n"
    "ds_write_b128 v26, v[196:199] offset:12288\n"
    "s_waitcnt vmcnt(1)\n"
    "v_lshlrev_b32 v14, 16, v200\n"
    "v_and_b32 v15, 0xffff0000, v200\n"
    "v_lshlrev_b32 v16, 16, v201\n"
    "v_and_b32 v17, 0xffff0000, v201\n"
    "v_fma_f32 v14, v14, v12, v13\n"
    "v_fma_f32 v15, v15, v12, v13\n"
    "v_fma_f32 v16, v16, v12, v13\n"
    "v_fma_f32 v17, v17, v12, v13\n"
    "s_waitcnt lgkmcnt(3)\n"
    "v_fma_f32 v14, v14, v208, v212\n"
    "v_fma_f32 v15, v15, v209, v213\n"
    "v_fma_f32 v16, v16, v210, v214\n"
    "v_fma_f32 v17, v17, v211, v215\n"
    "ds_read_b128 v[208:211], v27 offset:4096\n"
    "ds_read_b128 v[212:215], v27 offset:4112\n"
    "v_cvt_pk_bf16_f32 v200, v14, v15\n"
    "v_cvt_pk_bf16_f32 v201, v16, v17\n"
    "v_lshlrev_b32 v18, 16, v202\n"
    "v_and_b32 v19, 0xffff0000, v202\n"
    "v_lshlrev_b32 v20, 16, v203\n"
    "v_and_b32 v21, 0xffff0000, v203\n"
    "v_fma_f32 v18, v18, v12, v13\n"
    "v_fma_f32 v19, v19, v12, v13\n"
    "v_fma_f32 v20, v20, v12, v13\n"
    "v_fma_f32 v21, v21, v12, v13\n"
    "s_waitcnt lgkmcnt(3)\n"
    "v_fma_f32 v18, v18, v216, v220\n"
    "v_fma_f32 v19, v19, v217, v221\n"
    "v_fma_f32 v20, v20, v218, v222\n"
    "v_fma_f32 v21, v21, v219, v223\n"
    "ds_read_b128 v[216:219], v27 offset:4128\n"
    "ds_read_b128 v[220:223], v27 offset:4144\n"
    "v_cvt_pk_bf16_f32 v202, v18, v19\n"
    "v_cvt_pk_bf16_f32 v203, v20, v21\n"
    "ds_write_b128 v26, v[200:203] offset:14336\n"
    "s_waitcnt vmcnt(0)\n"
    "v_lshlrev_b32 v14, 16, v204\n"
    "v_and_b32 v15, 0xffff0000, v204\n"
    "v_lshlrev_b32 v16, 16, v205\n"
    "v_and_b32 v17, 0xffff0000, v205\n"
    "v_fma_f32 v14, v14, v12, v13\n"
    "v_fma_f32 v15, v15, v12, v13\n"
    "v_fma_f32 v16, v16, v12, v13\n"
    "v_fma_f32 v17, v17, v12, v13\n"
    "s_waitcnt lgkmcnt(3)\n"
    "v_fma_f32 v14, v14, v208, v212\n"
    "v_fma_f32 v15, v15, v209, v213\n"
    "v_fma_f32 v16, v16, v210, v214\n"
    "v_fma_f32 v17, v17, v211, v215\n"
    "v_cvt_pk_bf16_f32 v204, v14, v15\n"
    "v_cvt_pk_bf16_f32 v205, v16, v17\n"
    "v_lshlrev_b32 v18, 16, v206\n"
    "v_and_b32 v19, 0xffff0000, v206\n"
    "v_lshlrev_b32 v20, 16, v207\n"
    "v_and_b32 v21, 0xffff0000, v207\n"
    "v_fma_f32 v18, v18, v12, v13\n"
    "v_fma_f32 v19, v19, v12, v13\n"
    "v_fma_f32 v20, v20, v12, v13\n"
    "v_fma_f32 v21, v21, v12, v13\n"
    "s_waitcnt lgkmcnt(1)\n"
    "v_fma_f32 v18, v18, v216, v220\n"
    "v_fma_f32 v19, v19, v217, v221\n"
    "v_fma_f32 v20, v20, v218, v222\n"
    "v_fma_f32 v21, v21, v219, v223\n"
    "v_cvt_pk_bf16_f32 v206, v18, v19\n"
    "v_cvt_pk_bf16_f32 v207, v20, v21\n"
    "ds_write_b128 v26, v[204:207] offset:16384\n"
    "s_waitcnt lgkmcnt(0)\n"
    "s_barrier\n"
    "v_mfma_f32_16x16x32_bf16 v[64:67], v[28:31], v[144:147], 0\n"
    "ds_read_b128 v[160:163], v1 offset:1024\n"
    "v_mfma_f32_16x16x32_bf16 v[68:71], v[28:31], v[148:151], 0\n"
    "v_mfma_f32_16x16x32_bf16 v[72:75], v[28:31], v[152:155], 0\n"
    "v_mfma_f32_16x16x32_bf16 v[76:79], v[28:31], v[156:159], 0\n"
    "s_add_u32 s96, s96, 4096\n"
    "buffer_load_dwordx4 v[176:179], v1, s[20:23], s96 offen sc1\n"
    "v_mfma_f32_16x16x32_bf16 v[80:83], v[32:35], v[144:147], 0\n"
    "ds_read_b128 v[164:167], v1 offset:19456\n"
    "v_mfma_f32_16x16x32_bf16 v[84:87], v[32:35], v[148:151], 0\n"
    "v_mfma_f32_16x16x32_bf16 v[88:91], v[32:35], v[152:155], 0\n"
    "v_mfma_f32_16x16x32_bf16 v[92:95], v[32:35], v[156:159], 0\n"
    "buffer_load_dwordx4 v[180:183], v252, s[20:23], s96 offen sc1\n"
    "v_mfma_f32_16x16x32_bf16 v[96:99], v[36:39], v[144:147], 0\n"
    "ds_read_b128 v[168:171], v7 offset:1024\n"
    "v_mfma_f32_16x16x32_bf16 v[100:103], v[36:39], v[148:151], 0\n"
    "v_mfma_f32_16x16x32_bf16 v[104:107], v[36:39], v[152:155], 0\n"
    "v_mfma_f32_16x16x32_bf16 v[108:111], v[36:39], v[156:159], 0\n"
    "buffer_load_dwordx4 v[184:187], v253, s[20:23], s96 offen sc1\n"
    "v_mfma_f32_16x16x32_bf16 v[112:115], v[40:43], v[144:147], 0\n"
    "ds_read_b128 v[172:175], v7 offset:19456\n"
    "v_mfma_f32_16x16x32_bf16 v[116:119], v[40:43], v[148:151], 0\n"
    "v_mfma_f32_16x16x32_bf16 v[120:123], v[40:43], v[152:155], 0\n"
    "v_mfma_f32_16x16x32_bf16 v[124:127], v[40:43], v[156:159], 0\n"
    "buffer_load_dwordx4 v[188:191], v254, s[20:23], s96 offen sc1\n"
    "s_waitcnt lgkmcnt(3)\n"
    "v_mfma_f32_16x16x32_bf16 v[64:67], v[44:47], v[160:163], v[64:67]\n"
    "ds_read_b128 v[144:147], v1 offset:2048\n"
    "s_waitcnt lgkmcnt(3)\n"
    "v_mfma_f32_16x16x32_bf16 v[68:71], v[44:47], v[164:167], v[68:71]\n"
    "s_waitcnt lgkmcnt(2)\n"
    "v_mfma_f32_16x16x32_bf16 v[72:75], v[44:47], v[168:171], v[72:75]\n"
    "s_waitcnt lgkmcnt(1)\n"
    "v_mfma_f32_16x16x32_bf16 v[76:79], v[44:47], v[172:175], v[76:79]\n"
    "buffer_load_dwordx4 v[192:195], v1, s[20:23], s96 offen offset:1024 sc1\n"
    "v_mfma_f32_16x16x32_bf16 v[80:83], v[48:51], v[160:163], v[80:83]\n"
    "ds_read_b128 v[148:151], v1 offset:20480\n"
    "v_mfma_f32_16x16x32_bf16 v[84:87], v[48:51], v[164:167], v[84:87]\n"
    "v_mfma_f32_16x16x32_bf16 v[88:91], v[48:51], v[168:171], v[88:91]\n"
    "v_mfma_f32_16x16x32_bf16 v[92:95], v[48:51], v[172:175], v[92:95]\n"
    "buffer_load_dwordx4 v[196:199], v252, s[20:23], s96 offen offset:1024 sc1\n"
    "v_mfma_f32_16x16x32_bf16 v[96:99], v[52:55], v[160:163], v[96:99]\n"
    "ds_read_b128 v[152:155], v7 offset:2048\n"
    "v_mfma_f32_16x16x32_bf16 v[100:103], v[52:55], v[164:167], v[100:103]\n"
    "v_mfma_f32_16x16x32_bf16 v[104:107], v[52:55], v[168:171], v[104:107]\n"
    "v_mfma_f32_16x16x32_bf16 v[108:111], v[52:55], v[172:175], v[108:111]\n"
    "buffer_load_dwordx4 v[200:203], v253, s[20:23], s96 offen offset:1024 sc1\n"
    "v_mfma_f32_16x16x32_bf16 v[112:115], v[56:59], v[160:163], v[112:115]\n"
    "ds_read_b128 v[156:159], v7 offset:20480\n"
    "v_mfma_f32_16x16x32_bf16 v[116:119], v[56:59], v[164:167], v[116:119]\n"
    "v_mfma_f32_16x16x32_bf16 v[120:123], v[56:59], v[168:171], v[120:123]\n"
    "v_mfma_f32_16x16x32_bf16 v[124:127], v[56:59], v[172:175], v[124:127]\n"
    "buffer_load_dwordx4 v[204:207], v254, s[20:23], s96 offen offset:1024 sc1\n"
    "s_waitcnt lgkmcnt(3)\n"
    "v_mfma_f32_16x16x32_bf16 v[64:67], v[60:63], v[144:147], v[64:67]\n"
    "ds_read_b128 v[160:163], v1 offset:3072\n"
    "s_waitcnt lgkmcnt(3)\n"
    "v_mfma_f32_16x16x32_bf16 v[68:71], v[60:63], v[148:151], v[68:71]\n"
    "s_waitcnt lgkmcnt(2)\n"
    "v_mfma_f32_16x16x32_bf16 v[72:75], v[60:63], v[152:155], v[72:75]\n"
    "s_waitcnt lgkmcnt(1)\n"
    "v_mfma_f32_16x16x32_bf16 v[76:79], v[60:63], v[156:159], v[76:79]\n"
    "buffer_load_dwordx4 v[208:211], v1, s[20:23], s96 offen offset:2048 sc1\n"
    "v_mfma_f32_16x16x32_bf16 v[80:83], v[224:227], v[144:147], v[80:83]\n"
    "ds_read_b128 v[164:167], v1 offset:21504\n"
    "v_mfma_f32_16x16x32_bf16 v[84:87], v[224:227], v[148:151], v[84:87]\n"
    "v_mfma_f32_16x16x32_bf16 v[88:91], v[224:227], v[152:155], v[88:91]\n"
    "v_mfma_f32_16x16x32_bf16 v[92:95], v[224:227], v[156:159], v[92:95]\n"
    "buffer_load_dwordx4 v[212:215], v252, s[20:23], s96 offen offset:2048 sc1\n"
    "v_mfma_f32_16x16x32_bf16 v[96:99], v[228:231], v[144:147], v[96:99]\n"
    "ds_read_b128 v[168:171], v7 offset:3072\n"
    "v_mfma_f32_16x16x32_bf16 v[100:103], v[228:231], v[148:151], v[100:103]\n"
    "v_mfma_f32_16x16x32_bf16 v[104:107], v[228:231], v[152:155], v[104:107]\n"
    "v_mfma_f32_16x16x32_bf16 v[108:111], v[228:231], v[156:159], v[108:111]\n"
    "buffer_load_dwordx4 v[216:219], v253, s[20:23], s96 offen offset:2048 sc1\n"
    "v_mfma_f32_16x16x32_bf16 v[112:115], v[232:235], v[144:147], v[112:115]\n"
    "ds_read_b128 v[172:175], v7 offset:21504\n"
    "v_mfma_f32_16x16x32_bf16 v[116:119], v[232:235], v[148:151], v[116:119]\n"
    "v_mfma_f32_16x16x32_bf16 v[120:123], v[232:235], v[152:155], v[120:123]\n"
    "v_mfma_f32_16x16x32_bf16 v[124:127], v[232:235], v[156:159], v[124:127]\n"
    "buffer_load_dwordx4 v[220:223], v254, s[20:23], s96 offen offset:2048 sc1\n"
    "s_waitcnt lgkmcnt(3)\n"
    "v_mfma_f32_16x16x32_bf16 v[64:67], v[236:239], v[160:163], v[64:67]\n"
    "ds_read_b128 v[144:147], v1 offset:4096\n"
    "s_waitcnt lgkmcnt(3)\n"
    "v_mfma_f32_16x16x32_bf16 v[68:71], v[236:239], v[164:167], v[68:71]\n"
    "s_waitcnt lgkmcnt(2)\n"
    "v_mfma_f32_16x16x32_bf16 v[72:75], v[236:239], v[168:171], v[72:75]\n"
    "s_waitcnt lgkmcnt(1)\n"
    "v_mfma_f32_16x16x32_bf16 v[76:79], v[236:239], v[172:175], v[76:79]\n"
    "buffer_load_dwordx4 v[28:31], v1, s[20:23], s96 offen offset:3072 sc1\n"
    "v_mfma_f32_16x16x32_bf16 v[80:83], v[240:243], v[160:163], v[80:83]\n"
    "ds_read_b128 v[148:151], v1 offset:22528\n"
    "v_mfma_f32_16x16x32_bf16 v[84:87], v[240:243], v[164:167], v[84:87]\n"
    "v_mfma_f32_16x16x32_bf16 v[88:91], v[240:243], v[168:171], v[88:91]\n"
    "v_mfma_f32_16x16x32_bf16 v[92:95], v[240:243], v[172:175], v[92:95]\n"
    "buffer_load_dwordx4 v[32:35], v252, s[20:23], s96 offen offset:3072 sc1\n"
    "v_mfma_f32_16x16x32_bf16 v[96:99], v[244:247], v[160:163], v[96:99]\n"
    "ds_read_b128 v[152:155], v7 offset:4096\n"
    "v_mfma_f32_16x16x32_bf16 v[100:103], v[244:247], v[164:167], v[100:103]\n"
    "v_mfma_f32_16x16x32_bf16 v[104:107], v[244:247], v[168:171], v[104:107]\n"
    "v_mfma_f32_16x16x32_bf16 v[108:111], v[244:247], v[172:175], v[108:111]\n"
    "buffer_load_dwordx4 v[36:39], v253, s[20:23], s96 offen offset:3072 sc1\n"
    "v_mfma_f32_16x16x32_bf16 v[112:115], v[248:251], v[160:163], v[112:115]\n"
    "ds_read_b128 v[156:159], v7 offset:22528\n"
    "v_mfma_f32_16x16x32_bf16 v[116:119], v[248:251], v[164:167], v[116:119]\n"
    "v_mfma_f32_16x16x32_bf16 v[120:123], v[248:251], v[168:171], v[120:123]\n"
    "v_mfma_f32_16x16x32_bf16 v[124:127], v[248:251], v[172:175], v[124:127]\n"
    "buffer_load_dwordx4 v[40:43], v254, s[20:23], s96 offen offset:3072 sc1\n"
    "s_waitcnt vmcnt(15) lgkmcnt(3)\n"
    "v_mfma_f32_16x16x32_bf16 v[64:67], v[176:179], v[144:147], v[64:67]\n"
    "ds_read_b128 v[160:163], v1 offset:5120\n"
    "s_waitcnt lgkmcnt(3)\n"
    "v_mfma_f32_16x16x32_bf16 v[68:71], v[176:179], v[148:151], v[68:71]\n"
    "s_waitcnt lgkmcnt(2)\n"
    "v_mfma_f32_16x16x32_bf16 v[72:75], v[176:179], v[152:155], v[72:75]\n"
    "s_waitcnt lgkmcnt(1)\n"
    "v_mfma_f32_16x16x32_bf16 v[76:79], v[176:179], v[156:159], v[76:79]\n"
    "s_add_u32 s96, s96, 4096\n"
    "buffer_load_dwordx4 v[44:47], v1, s[20:23], s96 offen sc1\n"
    "buffer_load_dwordx4 v[48:51], v252, s[20:23], s96 offen sc1\n"
    "s_waitcnt vmcnt(16)\n"
    "v_mfma_f32_16x16x32_bf16 v[80:83], v[180:183], v[144:147], v[80:83]\n"
    "ds_read_b128 v[164:167], v1 offset:23552\n"
    "v_mfma_f32_16x16x32_bf16 v[84:87], v[180:183], v[148:151], v[84:87]\n"
    "v_mfma_f32_16x16x32_bf16 v[88:91], v[180:183], v[152:155], v[88:91]\n"
    "v_mfma_f32_16x16x32_bf16 v[92:95], v[180:183], v[156:159], v[92:95]\n"
    "buffer_load_dwordx4 v[52:55], v253, s[20:23], s96 offen sc1\n"
    "buffer_load_dwordx4 v[56:59], v254, s[20:23], s96 offen sc1\n"
    "s_waitcnt vmcnt(17)\n"
    "v_mfma_f32_16x16x32_bf16 v[96:99], v[184:187], v[144:147], v[96:99]\n"
    "ds_read_b128 v[168:171], v7 offset:5120\n"
    "v_mfma_f32_16x16x32_bf16 v[100:103], v[184:187], v[148:151], v[100:103]\n"
    "v_mfma_f32_16x16x32_bf16 v[104:107], v[184:187], v[152:155], v[104:107]\n"
    "v_mfma_f32_16x16x32_bf16 v[108:111], v[184:187], v[156:159], v[108:111]\n"
    "buffer_load_dwordx4 v[60:63], v1, s[20:23], s96 offen offset:1024 sc1\n"
    "buffer_load_dwordx4 v[224:227], v252, s[20:23], s96 offen offset:1024 sc1\n"
    "s_waitcnt vmcnt(18)\n"
    "v_mfma_f32_16x16x32_bf16 v[112:115], v[188:191], v[144:147], v[112:115]\n"
    "ds_read_b128 v[172:175], v7 offset:23552\n"
    "v_mfma_f32_16x16x32_bf16 v[116:119], v[188:191], v[148:151], v[116:119]\n"
    "v_mfma_f32_16x16x32_bf16 v[120:123], v[188:191], v[152:155], v[120:123]\n"
    "v_mfma_f32_16x16x32_bf16 v[124:127], v[188:191], v[156:159], v[124:127]\n"
    "buffer_load_dwordx4 v[228:231], v253, s[20:23], s96 offen offset:1024 sc1\n"
    "buffer_load_dwordx4 v[232:235], v254, s[20:23], s96 offen offset:1024 sc1\n"
    "s_waitcnt vmcnt(19) lgkmcnt(3)\n"
    "v_mfma_f32_16x16x32_bf16 v[64:67], v[192:195], v[160:163], v[64:67]\n"
    "ds_read_b128 v[144:147], v1 offset:6144\n"
    "s_waitcnt lgkmcnt(3)\n"
    "v_mfma_f32_16x16x32_bf16 v[68:71], v[192:195], v[164:167], v[68:71]\n"
    "s_waitcnt lgkmcnt(2)\n"
    "v_mfma_f32_16x16x32_bf16 v[72:75], v[192:195], v[168:171], v[72:75]\n"
    "s_waitcnt lgkmcnt(1)\n"
    "v_mfma_f32_16x16x32_bf16 v[76:79], v[192:195], v[172:175], v[76:79]\n"
    "buffer_load_dwordx4 v[236:239], v1, s[20:23], s96 offen offset:2048 sc1\n"
    "buffer_load_dwordx4 v[240:243], v252, s[20:23], s96 offen offset:2048 sc1\n"
    "s_waitcnt vmcnt(20)\n"
    "v_mfma_f32_16x16x32_bf16 v[80:83], v[196:199], v[160:163], v[80:83]\n"
    "ds_read_b128 v[148:151], v1 offset:24576\n"
    "v_mfma_f32_16x16x32_bf16 v[84:87], v[196:199], v[164:167], v[84:87]\n"
    "v_mfma_f32_16x16x32_bf16 v[88:91], v[196:199], v[168:171], v[88:91]\n"
    "v_mfma_f32_16x16x32_bf16 v[92:95], v[196:199], v[172:175], v[92:95]\n"
    "buffer_load_dwordx4 v[244:247], v253, s[20:23], s96 offen offset:2048 sc1\n"
    "buffer_load_dwordx4 v[248:251], v254, s[20:23], s96 offen offset:2048 sc1\n"
    "s_waitcnt vmcnt(21)\n"
    "v_mfma_f32_16x16x32_bf16 v[96:99], v[200:203], v[160:163], v[96:99]\n"
    "ds_read_b128 v[152:155], v7 offset:6144\n"
    "v_mfma_f32_16x16x32_bf16 v[100:103], v[200:203], v[164:167], v[100:103]\n"
    "v_mfma_f32_16x16x32_bf16 v[104:107], v[200:203], v[168:171], v[104:107]\n"
    "v_mfma_f32_16x16x32_bf16 v[108:111], v[200:203], v[172:175], v[108:111]\n"
    "buffer_load_dwordx4 v[176:179], v1, s[20:23], s96 offen offset:3072 sc1\n"
    "buffer_load_dwordx4 v[180:183], v252, s[20:23], s96 offen offset:3072 sc1\n"
    "s_waitcnt vmcnt(22)\n"
    "v_mfma_f32_16x16x32_bf16 v[112:115], v[204:207], v[160:163], v[112:115]\n"
    "ds_read_b128 v[156:159], v7 offset:24576\n"
    "v_mfma_f32_16x16x32_bf16 v[116:119], v[204:207], v[164:167], v[116:119]\n"
    "v_mfma_f32_16x16x32_bf16 v[120:123], v[204:207], v[168:171], v[120:123]\n"
    "v_mfma_f32_16x16x32_bf16 v[124:127], v[204:207], v[172:175], v[124:127]\n"
    "buffer_load_dwordx4 v[184:187], v253, s[20:23], s96 offen offset:3072 sc1\n"
    "buffer_load_dwordx4 v[188:191], v254, s[20:23], s96 offen offset:3072 sc1\n"
    "s_waitcnt vmcnt(23) lgkmcnt(3)\n"
    "v_mfma_f32_16x16x32_bf16 v[64:67], v[208:211], v[144:147], v[64:67]\n"
    "ds_read_b128 v[160:163], v1 offset:7168\n"
    "s_waitcnt lgkmcnt(3)\n"
    "v_mfma_f32_16x16x32_bf16 v[68:71], v[208:211], v[148:151], v[68:71]\n"
    "s_waitcnt lgkmcnt(2)\n"
    "v_mfma_f32_16x16x32_bf16 v[72:75], v[208:211], v[152:155], v[72:75]\n"
    "s_waitcnt lgkmcnt(1)\n"
    "v_mfma_f32_16x16x32_bf16 v[76:79], v[208:211], v[156:159], v[76:79]\n"
    "s_add_u32 s96, s96, 4096\n"
    "buffer_load_dwordx4 v[192:195], v1, s[20:23], s96 offen sc1\n"
    "buffer_load_dwordx4 v[196:199], v252, s[20:23], s96 offen sc1\n"
    "s_waitcnt vmcnt(24)\n"
    "v_mfma_f32_16x16x32_bf16 v[80:83], v[212:215], v[144:147], v[80:83]\n"
    "ds_read_b128 v[164:167], v1 offset:25600\n"
    "v_mfma_f32_16x16x32_bf16 v[84:87], v[212:215], v[148:151], v[84:87]\n"
    "v_mfma_f32_16x16x32_bf16 v[88:91], v[212:215], v[152:155], v[88:91]\n"
    "v_mfma_f32_16x16x32_bf16 v[92:95], v[212:215], v[156:159], v[92:95]\n"
    "buffer_load_dwordx4 v[200:203], v253, s[20:23], s96 offen sc1\n"
    "buffer_load_dwordx4 v[204:207], v254, s[20:23], s96 offen sc1\n"
    "s_waitcnt vmcnt(25)\n"
    "v_mfma_f32_16x16x32_bf16 v[96:99], v[216:219], v[144:147], v[96:99]\n"
    "ds_read_b128 v[168:171], v7 offset:7168\n"
    "v_mfma_f32_16x16x32_bf16 v[100:103], v[216:219], v[148:151], v[100:103]\n"
    "v_mfma_f32_16x16x32_bf16 v[104:107], v[216:219], v[152:155], v[104:107]\n"
    "v_mfma_f32_16x16x32_bf16 v[108:111], v[216:219], v[156:159], v[108:111]\n"
    "buffer_load_dwordx4 v[208:211], v1, s[20:23], s96 offen offset:1024 sc1\n"
    "buffer_load_dwordx4 v[212:215], v252, s[20:23], s96 offen offset:1024 sc1\n"
    "s_waitcnt vmcnt(26)\n"
    "v_mfma_f32_16x16x32_bf16 v[112:115], v[220:223], v[144:147], v[112:115]\n"
    "ds_read_b128 v[172:175], v7 offset:25600\n"
    "v_mfma_f32_16x16x32_bf16 v[116:119], v[220:223], v[148:151], v[116:119]\n"
    "v_mfma_f32_16x16x32_bf16 v[120:123], v[220:223], v[152:155], v[120:123]\n"
    "v_mfma_f32_16x16x32_bf16 v[124:127], v[220:223], v[156:159], v[124:127]\n"
    "buffer_load_dwordx4 v[216:219], v253, s[20:23], s96 offen offset:1024 sc1\n"
    "buffer_load_dwordx4 v[220:223], v254, s[20:23], s96 offen offset:1024 sc1\n"
    "s_waitcnt vmcnt(27) lgkmcnt(3)\n"
    "v_mfma_f32_16x16x32_bf16 v[64:67], v[28:31], v[160:163], v[64:67]\n"
    "ds_read_b128 v[144:147], v1 offset:8192\n"
    "s_waitcnt lgkmcnt(3)\n"
    "v_mfma_f32_16x16x32_bf16 v[68:71], v[28:31], v[164:167], v[68:71]\n"
    "s_waitcnt lgkmcnt(2)\n"
    "v_mfma_f32_16x16x32_bf16 v[72:75], v[28:31], v[168:171], v[72:75]\n"
    "s_waitcnt lgkmcnt(1)\n"
    "v_mfma_f32_16x16x32_bf16 v[76:79], v[28:31], v[172:175], v[76:79]\n"
    "buffer_load_dwordx4 v[28:31], v1, s[20:23], s96 offen offset:2048 sc1\n"
    "s_waitcnt vmcnt(27)\n"
    "v_mfma_f32_16x16x32_bf16 v[80:83], v[32:35], v[160:163], v[80:83]\n"
    "ds_read_b128 v[148:151], v1 offset:26624\n"
    "v_mfma_f32_16x16x32_bf16 v[84:87], v[32:35], v[164:167], v[84:87]\n"
    "v_mfma_f32_16x16x32_bf16 v[88:91], v[32:35], v[168:171], v[88:91]\n"
    "v_mfma_f32_16x16x32_bf16 v[92:95], v[32:35], v[172:175], v[92:95]\n"
    "buffer_load_dwordx4 v[32:35], v252, s[20:23], s96 offen offset:2048 sc1\n"
    "s_waitcnt vmcnt(27)\n"
    "v_mfma_f32_16x16x32_bf16 v[96:99], v[36:39], v[160:163], v[96:99]\n"
    "ds_read_b128 v[152:155], v7 offset:8192\n"
    "v_mfma_f32_16x16x32_bf16 v[100:103], v[36:39], v[164:167], v[100:103]\n"
    "v_mfma_f32_16x16x32_bf16 v[104:107], v[36:39], v[168:171], v[104:107]\n"
    "v_mfma_f32_16x16x32_bf16 v[108:111], v[36:39], v[172:175], v[108:111]\n"
    "buffer_load_dwordx4 v[36:39], v253, s[20:23], s96 offen offset:2048 sc1\n"
    "s_waitcnt vmcnt(27)\n"
    "v_mfma_f32_16x16x32_bf16 v[112:115], v[40:43], v[160:163], v[112:115]\n"
    "ds_read_b128 v[156:159], v7 offset:26624\n"
    "v_mfma_f32_16x16x32_bf16 v[116:119], v[40:43], v[164:167], v[116:119]\n"
    "v_mfma_f32_16x16x32_bf16 v[120:123], v[40:43], v[168:171], v[120:123]\n"
    "v_mfma_f32_16x16x32_bf16 v[124:127], v[40:43], v[172:175], v[124:127]\n"
    "buffer_load_dwordx4 v[40:43], v254, s[20:23], s96 offen offset:2048 sc1\n"
    "s_waitcnt vmcnt(27) lgkmcnt(3)\n"
    "v_mfma_f32_16x16x32_bf16 v[64:67], v[44:47], v[144:147], v[64:67]\n"
    "ds_read_b128 v[160:163], v1 offset:9216\n"
    "s_waitcnt lgkmcnt(3)\n"
    "v_mfma_f32_16x16x32_bf16 v[68:71], v[44:47], v[148:151], v[68:71]\n"
    "s_waitcnt lgkmcnt(2)\n"
    "v_mfma_f32_16x16x32_bf16 v[72:75], v[44:47], v[152:155], v[72:75]\n"
    "s_waitcnt lgkmcnt(1)\n"
    "v_mfma_f32_16x16x32_bf16 v[76:79], v[44:47], v[156:159], v[76:79]\n"
    "buffer_load_dwordx4 v[44:47], v1, s[20:23], s96 offen offset:3072 sc1\n"
    "s_waitcnt vmcnt(27)\n"
    "v_mfma_f32_16x16x32_bf16 v[80:83], v[48:51], v[144:147], v[80:83]\n"
    "ds_read_b128 v[164:167], v1 offset:27648\n"
    "v_mfma_f32_16x16x32_bf16 v[84:87], v[48:51], v[148:151], v[84:87]\n"
    "v_mfma_f32_16x16x32_bf16 v[88:91], v[48:51], v[152:155], v[88:91]\n"
    "v_mfma_f32_16x16x32_bf16 v[92:95], v[48:51], v[156:159], v[92:95]\n"
    "buffer_load_dwordx4 v[48:51], v252, s[20:23], s96 offen offset:3072 sc1\n"
    "s_waitcnt vmcnt(27)\n"
    "v_mfma_f32_16x16x32_bf16 v[96:99], v[52:55], v[144:147], v[96:99]\n"
    "ds_read_b128 v[168:171], v7 offset:9216\n"
    "v_mfma_f32_16x16x32_bf16 v[100:103], v[52:55], v[148:151], v[100:103]\n"
    "v_mfma_f32_16x16x32_bf16 v[104:107], v[52:55], v[152:155], v[104:107]\n"
    "v_mfma_f32_16x16x32_bf16 v[108:111], v[52:55], v[156:159], v[108:111]\n"
    "buffer_load_dwordx4 v[52:55], v253, s[20:23], s96 offen offset:3072 sc1\n"
    "s_waitcnt vmcnt(27)\n"
    "v_mfma_f32_16x16x32_bf16 v[112:115], v[56:59], v[144:147], v[112:115]\n"
    "ds_read_b128 v[172:175], v7 offset:27648\n"
    "v_mfma_f32_16x16x32_bf16 v[116:119], v[56:59], v[148:151], v[116:119]\n"
    "v_mfma_f32_16x16x32_bf16 v[120:123], v[56:59], v[152:155], v[120:123]\n"
    "v_mfma_f32_16x16x32_bf16 v[124:127], v[56:59], v[156:159], v[124:127]\n"
    "buffer_load_dwordx4 v[56:59], v254, s[20:23], s96 offen offset:3072 sc1\n"
    "s_waitcnt vmcnt(27) lgkmcnt(3)\n"
    "v_mfma_f32_16x16x32_bf16 v[64:67], v[60:63], v[160:163], v[64:67]\n"
    "ds_read_b128 v[144:147], v1 offset:10240\n"
    "s_waitcnt lgkmcnt(3)\n"
    "v_mfma_f32_16x16x32_bf16 v[68:71], v[60:63], v[164:167], v[68:71]\n"
    "s_waitcnt lgkmcnt(2)\n"
    "v_mfma_f32_16x16x32_bf16 v[72:75], v[60:63], v[168:171], v[72:75]\n"
    "s_waitcnt lgkmcnt(1)\n"
    "v_mfma_f32_16x16x32_bf16 v[76:79], v[60:63], v[172:175], v[76:79]\n"
    "s_add_u32 s96, s96, 4096\n"
    "buffer_load_dwordx4 v[60:63], v1, s[20:23], s96 offen sc1\n"
    "s_waitcnt vmcnt(27)\n"
    "v_mfma_f32_16x16x32_bf16 v[80:83], v[224:227], v[160:163], v[80:83]\n"
    "ds_read_b128 v[148:151], v1 offset:28672\n"
    "v_mfma_f32_16x16x32_bf16 v[84:87], v[224:227], v[164:167], v[84:87]\n"
    "v_mfma_f32_16x16x32_bf16 v[88:91], v[224:227], v[168:171], v[88:91]\n"
    "v_mfma_f32_16x16x32_bf16 v[92:95], v[224:227], v[172:175], v[92:95]\n"
    "buffer_load_dwordx4 v[224:227], v252, s[20:23], s96 offen sc1\n"
    "s_waitcnt vmcnt(27)\n"
    "v_mfma_f32_16x16x32_bf16 v[96:99], v[228:231], v[160:163], v[96:99]\n"
    "ds_read_b128 v[152:155], v7 offset:10240\n"
    "v_mfma_f32_16x16x32_bf16 v[100:103], v[228:231], v[164:167], v[100:103]\n"
    "v_mfma_f32_16x16x32_bf16 v[104:107], v[228:231], v[168:171], v[104:107]\n"
    "v_mfma_f32_16x16x32_bf16 v[108:111], v[228:231], v[172:175], v[108:111]\n"
    "buffer_load_dwordx4 v[228:231], v253, s[20:23], s96 offen sc1\n"
    "s_waitcnt vmcnt(27)\n"
    "v_mfma_f32_16x16x32_bf16 v[112:115], v[232:235], v[160:163], v[112:115]\n"
    "ds_read_b128 v[156:159], v7 offset:28672\n"
    "v_mfma_f32_16x16x32_bf16 v[116:119], v[232:235], v[164:167], v[116:119]\n"
    "v_mfma_f32_16x16x32_bf16 v[120:123], v[232:235], v[168:171], v[120:123]\n"
    "v_mfma_f32_16x16x32_bf16 v[124:127], v[232:235], v[172:175], v[124:127]\n"
    "buffer_load_dwordx4 v[232:235], v254, s[20:23], s96 offen sc1\n"
    "s_waitcnt vmcnt(27) lgkmcnt(3)\n"
    "v_mfma_f32_16x16x32_bf16 v[64:67], v[236:239], v[144:147], v[64:67]\n"
    "ds_read_b128 v[160:163], v1 offset:11264\n"
    "s_waitcnt lgkmcnt(3)\n"
    "v_mfma_f32_16x16x32_bf16 v[68:71], v[236:239], v[148:151], v[68:71]\n"
    "s_waitcnt lgkmcnt(2)\n"
    "v_mfma_f32_16x16x32_bf16 v[72:75], v[236:239], v[152:155], v[72:75]\n"
    "s_waitcnt lgkmcnt(1)\n"
    "v_mfma_f32_16x16x32_bf16 v[76:79], v[236:239], v[156:159], v[76:79]\n"
    "buffer_load_dwordx4 v[236:239], v1, s[20:23], s96 offen offset:1024 sc1\n"
    "s_waitcnt vmcnt(27)\n"
    "v_mfma_f32_16x16x32_bf16 v[80:83], v[240:243], v[144:147], v[80:83]\n"
    "ds_read_b128 v[164:167], v1 offset:29696\n"
    "v_mfma_f32_16x16x32_bf16 v[84:87], v[240:243], v[148:151], v[84:87]\n"
    "v_mfma_f32_16x16x32_bf16 v[88:91], v[240:243], v[152:155], v[88:91]\n"
    "v_mfma_f32_16x16x32_bf16 v[92:95], v[240:243], v[156:159], v[92:95]\n"
    "buffer_load_dwordx4 v[240:243], v252, s[20:23], s96 offen offset:1024 sc1\n"
    "s_waitcnt vmcnt(27)\n"
    "v_mfma_f32_16x16x32_bf16 v[96:99], v[244:247], v[144:147], v[96:99]\n"
    "ds_read_b128 v[168:171], v7 offset:11264\n"
    "v_mfma_f32_16x16x32_bf16 v[100:103], v[244:247], v[148:151], v[100:103]\n"
    "v_mfma_f32_16x16x32_bf16 v[104:107], v[244:247], v[152:155], v[104:107]\n"
    "v_mfma_f32_16x16x32_bf16 v[108:111], v[244:247], v[156:159], v[108:111]\n"
    "buffer_load_dwordx4 v[244:247], v253, s[20:23], s96 offen offset:1024 sc1\n"
    "s_waitcnt vmcnt(27)\n"
    "v_mfma_f32_16x16x32_bf16 v[112:115], v[248:251], v[144:147], v[112:115]\n"
    "ds_read_b128 v[172:175], v7 offset:29696\n"
    "v_mfma_f32_16x16x32_bf16 v[116:119], v[248:251], v[148:151], v[116:119]\n"
    "v_mfma_f32_16x16x32_bf16 v[120:123], v[248:251], v[152:155], v[120:123]\n"
    "v_mfma_f32_16x16x32_bf16 v[124:127], v[248:251], v[156:159], v[124:127]\n"
    "buffer_load_dwordx4 v[248:251], v254, s[20:23], s96 offen offset:1024 sc1\n"
    "s_waitcnt vmcnt(27) lgkmcnt(3)\n"
    "v_mfma_f32_16x16x32_bf16 v[64:67], v[176:179], v[160:163], v[64:67]\n"
    "ds_read_b128 v[144:147], v1 offset:12288\n"
    "s_waitcnt lgkmcnt(3)\n"
    "v_mfma_f32_16x16x32_bf16 v[68:71], v[176:179], v[164:167], v[68:71]\n"
    "s_waitcnt lgkmcnt(2)\n"
    "v_mfma_f32_16x16x32_bf16 v[72:75], v[176:179], v[168:171], v[72:75]\n"
    "s_waitcnt lgkmcnt(1)\n"
    "v_mfma_f32_16x16x32_bf16 v[76:79], v[176:179], v[172:175], v[76:79]\n"
    "buffer_load_dwordx4 v[176:179], v1, s[20:23], s58 offen sc1\n"
    "s_waitcnt vmcnt(27)\n"
    "v_mfma_f32_16x16x32_bf16 v[80:83], v[180:183], v[160:163], v[80:83]\n"
    "ds_read_b128 v[148:151], v1 offset:30720\n"
    "v_mfma_f32_16x16x32_bf16 v[84:87], v[180:183], v[164:167], v[84:87]\n"
    "v_mfma_f32_16x16x32_bf16 v[88:91], v[180:183], v[168:171], v[88:91]\n"
    "v_mfma_f32_16x16x32_bf16 v[92:95], v[180:183], v[172:175], v[92:95]\n"
    "buffer_load_dwordx4 v[180:183], v252, s[20:23], s58 offen sc1\n"
    "s_waitcnt vmcnt(27)\n"
    "v_mfma_f32_16x16x32_bf16 v[96:99], v[184:187], v[160:163], v[96:99]\n"
    "ds_read_b128 v[152:155], v7 offset:12288\n"
    "v_mfma_f32_16x16x32_bf16 v[100:103], v[184:187], v[164:167], v[100:103]\n"
    "v_mfma_f32_16x16x32_bf16 v[104:107], v[184:187], v[168:171], v[104:107]\n"
    "v_mfma_f32_16x16x32_bf16 v[108:111], v[184:187], v[172:175], v[108:111]\n"
    "buffer_load_dwordx4 v[184:187], v253, s[20:23], s58 offen sc1\n"
    "s_waitcnt vmcnt(27)\n"
    "v_mfma_f32_16x16x32_bf16 v[112:115], v[188:191], v[160:163], v[112:115]\n"
    "ds_read_b128 v[156:159], v7 offset:30720\n"
    "v_mfma_f32_16x16x32_bf16 v[116:119], v[188:191], v[164:167], v[116:119]\n"
    "v_mfma_f32_16x16x32_bf16 v[120:123], v[188:191], v[168:171], v[120:123]\n"
    "v_mfma_f32_16x16x32_bf16 v[124:127], v[188:191], v[172:175], v[124:127]\n"
    "buffer_load_dwordx4 v[188:191], v254, s[20:23], s58 offen sc1\n"
    "s_waitcnt vmcnt(27) lgkmcnt(3)\n"
    "v_mfma_f32_16x16x32_bf16 v[64:67], v[192:195], v[144:147], v[64:67]\n"
    "ds_read_b128 v[160:163], v1 offset:13312\n"
    "s_waitcnt lgkmcnt(3)\n"
    "v_mfma_f32_16x16x32_bf16 v[68:71], v[192:195], v[148:151], v[68:71]\n"
    "s_waitcnt lgkmcnt(2)\n"
    "v_mfma_f32_16x16x32_bf16 v[72:75], v[192:195], v[152:155], v[72:75]\n"
    "s_waitcnt lgkmcnt(1)\n"
    "v_mfma_f32_16x16x32_bf16 v[76:79], v[192:195], v[156:159], v[76:79]\n"
    "buffer_load_dwordx4 v[192:195], v1, s[20:23], s58 offen offset:1024 sc1\n"
    "s_waitcnt vmcnt(27)\n"
    "v_mfma_f32_16x16x32_bf16 v[80:83], v[196:199], v[144:147], v[80:83]\n"
    "ds_read_b128 v[164:167], v1 offset:31744\n"
    "v_mfma_f32_16x16x32_bf16 v[84:87], v[196:199], v[148:151], v[84:87]\n"
    "v_mfma_f32_16x16x32_bf16 v[88:91], v[196:199], v[152:155], v[88:91]\n"
    "v_mfma_f32_16x16x32_bf16 v[92:95], v[196:199], v[156:159], v[92:95]\n"
    "buffer_load_dwordx4 v[196:199], v252, s[20:23], s58 offen offset:1024 sc1\n"
    "s_waitcnt vmcnt(27)\n"
    "v_mfma_f32_16x16x32_bf16 v[96:99], v[200:203], v[144:147], v[96:99]\n"
    "ds_read_b128 v[168:171], v7 offset:13312\n"
    "v_mfma_f32_16x16x32_bf16 v[100:103], v[200:203], v[148:151], v[100:103]\n"
    "v_mfma_f32_16x16x32_bf16 v[104:107], v[200:203], v[152:155], v[104:107]\n"
    "v_mfma_f32_16x16x32_bf16 v[108:111], v[200:203], v[156:159], v[108:111]\n"
    "buffer_load_dwordx4 v[200:203], v253, s[20:23], s58 offen offset:1024 sc1\n"
    "s_waitcnt vmcnt(27)\n"
    "v_mfma_f32_16x16x32_bf16 v[112:115], v[204:207], v[144:147], v[112:115]\n"
    "ds_read_b128 v[172:175], v7 offset:31744\n"
    "v_mfma_f32_16x16x32_bf16 v[116:119], v[204:207], v[148:151], v[116:119]\n"
    "v_mfma_f32_16x16x32_bf16 v[120:123], v[204:207], v[152:155], v[120:123]\n"
    "v_mfma_f32_16x16x32_bf16 v[124:127], v[204:207], v[156:159], v[124:127]\n"
    "buffer_load_dwordx4 v[204:207], v254, s[20:23], s58 offen offset:1024 sc1\n"
    "s_waitcnt vmcnt(27) lgkmcnt(3)\n"
    "v_mfma_f32_16x16x32_bf16 v[64:67], v[208:211], v[160:163], v[64:67]\n"
    "ds_read_b128 v[144:147], v1 offset:14336\n"
    "s_waitcnt lgkmcnt(3)\n"
    "v_mfma_f32_16x16x32_bf16 v[68:71], v[208:211], v[164:167], v[68:71]\n"
    "s_waitcnt lgkmcnt(2)\n"
    "v_mfma_f32_16x16x32_bf16 v[72:75], v[208:211], v[168:171], v[72:75]\n"
    "s_waitcnt lgkmcnt(1)\n"
    "v_mfma_f32_16x16x32_bf16 v[76:79], v[208:211], v[172:175], v[76:79]\n"
    "buffer_load_dwordx4 v[208:211], v1, s[20:23], s58 offen offset:2048 sc1\n"
    "s_waitcnt vmcnt(27)\n"
    "v_mfma_f32_16x16x32_bf16 v[80:83], v[212:215], v[160:163], v[80:83]\n"
    "ds_read_b128 v[148:151], v1 offset:32768\n"
    "v_mfma_f32_16x16x32_bf16 v[84:87], v[212:215], v[164:167], v[84:87]\n"
    "v_mfma_f32_16x16x32_bf16 v[88:91], v[212:215], v[168:171], v[88:91]\n"
    "v_mfma_f32_16x16x32_bf16 v[92:95], v[212:215], v[172:175], v[92:95]\n"
    "buffer_load_dwordx4 v[212:215], v252, s[20:23], s58 offen offset:2048 sc1\n"
    "s_waitcnt vmcnt(27)\n"
    "v_mfma_f32_16x16x32_bf16 v[96:99], v[216:219], v[160:163], v[96:99]\n"
    "ds_read_b128 v[152:155], v7 offset:14336\n"
    "v_mfma_f32_16x16x32_bf16 v[100:103], v[216:219], v[164:167], v[100:103]\n"
    "v_mfma_f32_16x16x32_bf16 v[104:107], v[216:219], v[168:171], v[104:107]\n"
    "v_mfma_f32_16x16x32_bf16 v[108:111], v[216:219], v[172:175], v[108:111]\n"
    "buffer_load_dwordx4 v[216:219], v253, s[20:23], s58 offen offset:2048 sc1\n"
    "s_waitcnt vmcnt(27)\n"
    "v_mfma_f32_16x16x32_bf16 v[112:115], v[220:223], v[160:163], v[112:115]\n"
    "ds_read_b128 v[156:159], v7 offset:32768\n"
    "v_mfma_f32_16x16x32_bf16 v[116:119], v[220:223], v[164:167], v[116:119]\n"
    "v_mfma_f32_16x16x32_bf16 v[120:123], v[220:223], v[168:171], v[120:123]\n"
    "v_mfma_f32_16x16x32_bf16 v[124:127], v[220:223], v[172:175], v[124:127]\n"
    "buffer_load_dwordx4 v[220:223], v254, s[20:23], s58 offen offset:2048 sc1\n"
    "s_lshl_b32 s45, s38, 2\n"
    "s_add_u32 s45, s45, s39\n"
    "s_mul_i32 s45, s45, 18432\n"
    "s_add_u32 s45, s45, 10695680\n"
    "s_mul_i32 s46, s37, 2304\n"
    "s_add_u32 s46, s46, s45\n"
    "s_mul_i32 s45, s36, 1024\n"
    "s_add_u32 s45, s45, 147456\n"
    "s_mov_b32 m0, s45\n"
    "s_nop 0\n"
    "buffer_load_dwordx4 v1, s[20:23], s46 offen sc1 lds\n"
    "s_waitcnt vmcnt(28) lgkmcnt(3)\n"
    "v_mfma_f32_16x16x32_bf16 v[64:67], v[28:31], v[144:147], v[64:67]\n"
    "ds_read_b128 v[160:163], v1 offset:15360\n"
    "s_waitcnt lgkmcnt(3)\n"
    "v_mfma_f32_16x16x32_bf16 v[68:71], v[28:31], v[148:151], v[68:71]\n"
    "s_waitcnt lgkmcnt(2)\n"
    "v_mfma_f32_16x16x32_bf16 v[72:75], v[28:31], v[152:155], v[72:75]\n"
    "s_waitcnt lgkmcnt(1)\n"
    "v_mfma_f32_16x16x32_bf16 v[76:79], v[28:31], v[156:159], v[76:79]\n"
    "buffer_load_dwordx4 v[28:31], v1, s[20:23], s58 offen offset:3072 sc1\n"
    "s_waitcnt vmcnt(28)\n"
    "v_mfma_f32_16x16x32_bf16 v[80:83], v[32:35], v[144:147], v[80:83]\n"
    "ds_read_b128 v[164:167], v1 offset:33792\n"
    "v_mfma_f32_16x16x32_bf16 v[84:87], v[32:35], v[148:151], v[84:87]\n"
    "v_mfma_f32_16x16x32_bf16 v[88:91], v[32:35], v[152:155], v[88:91]\n"
    "v_mfma_f32_16x16x32_bf16 v[92:95], v[32:35], v[156:159], v[92:95]\n"
    "buffer_load_dwordx4 v[32:35], v252, s[20:23], s58 offen offset:3072 sc1\n"
    "s_waitcnt vmcnt(28)\n"
    "v_mfma_f32_16x16x32_bf16 v[96:99], v[36:39], v[144:147], v[96:99]\n"
    "ds_read_b128 v[168:171], v7 offset:15360\n"
    "v_mfma_f32_16x16x32_bf16 v[100:103], v[36:39], v[148:151], v[100:103]\n"
    "v_mfma_f32_16x16x32_bf16 v[104:107], v[36:39], v[152:155], v[104:107]\n"
    "v_mfma_f32_16x16x32_bf16 v[108:111], v[36:39], v[156:159], v[108:111]\n"
    "buffer_load_dwordx4 v[36:39], v253, s[20:23], s58 offen offset:3072 sc1\n"
    "s_waitcnt vmcnt(28)\n"
    "v_mfma_f32_16x16x32_bf16 v[112:115], v[40:43], v[144:147], v[112:115]\n"
    "ds_read_b128 v[172:175], v7 offset:33792\n"
    "v_mfma_f32_16x16x32_bf16 v[116:119], v[40:43], v[148:151], v[116:119]\n"
    "v_mfma_f32_16x16x32_bf16 v[120:123], v[40:43], v[152:155], v[120:123]\n"
    "v_mfma_f32_16x16x32_bf16 v[124:127], v[40:43], v[156:159], v[124:127]\n"
    "buffer_load_dwordx4 v[40:43], v254, s[20:23], s58 offen offset:3072 sc1\n"
    "s_waitcnt vmcnt(28) lgkmcnt(3)\n"
    "v_mfma_f32_16x16x32_bf16 v[64:67], v[44:47], v[160:163], v[64:67]\n"
    "ds_read_b128 v[144:147], v1 offset:16384\n"
    "s_waitcnt lgkmcnt(3)\n"
    "v_mfma_f32_16x16x32_bf16 v[68:71], v[44:47], v[164:167], v[68:71]\n"
    "s_waitcnt lgkmcnt(2)\n"
    "v_mfma_f32_16x16x32_bf16 v[72:75], v[44:47], v[168:171], v[72:75]\n"
    "s_waitcnt lgkmcnt(1)\n"
    "v_mfma_f32_16x16x32_bf16 v[76:79], v[44:47], v[172:175], v[76:79]\n"
    "s_add_u32 s58, s58, 4096\n"
    "buffer_load_dwordx4 v[44:47], v1, s[20:23], s58 offen sc1\n"
    "s_waitcnt vmcnt(28)\n"
    "v_mfma_f32_16x16x32_bf16 v[80:83], v[48:51], v[160:163], v[80:83]\n"
    "ds_read_b128 v[148:151], v1 offset:34816\n"
    "v_mfma_f32_16x16x32_bf16 v[84:87], v[48:51], v[164:167], v[84:87]\n"
    "v_mfma_f32_16x16x32_bf16 v[88:91], v[48:51], v[168:171], v[88:91]\n"
    "v_mfma_f32_16x16x32_bf16 v[92:95], v[48:51], v[172:175], v[92:95]\n"
    "buffer_load_dwordx4 v[48:51], v252, s[20:23], s58 offen sc1\n"
    "s_waitcnt vmcnt(28)\n"
    "v_mfma_f32_16x16x32_bf16 v[96:99], v[52:55], v[160:163], v[96:99]\n"
    "ds_read_b128 v[152:155], v7 offset:16384\n"
    "v_mfma_f32_16x16x32_bf16 v[100:103], v[52:55], v[164:167], v[100:103]\n"
    "v_mfma_f32_16x16x32_bf16 v[104:107], v[52:55], v[168:171], v[104:107]\n"
    "v_mfma_f32_16x16x32_bf16 v[108:111], v[52:55], v[172:175], v[108:111]\n"
    "buffer_load_dwordx4 v[52:55], v253, s[20:23], s58 offen sc1\n"
    "s_waitcnt vmcnt(28)\n"
    "v_mfma_f32_16x16x32_bf16 v[112:115], v[56:59], v[160:163], v[112:115]\n"
    "ds_read_b128 v[156:159], v7 offset:34816\n"
    "v_mfma_f32_16x16x32_bf16 v[116:119], v[56:59], v[164:167], v[116:119]\n"
    "v_mfma_f32_16x16x32_bf16 v[120:123], v[56:59], v[168:171], v[120:123]\n"
    "v_mfma_f32_16x16x32_bf16 v[124:127], v[56:59], v[172:175], v[124:127]\n"
    "buffer_load_dwordx4 v[56:59], v254, s[20:23], s58 offen sc1\n"
    "s_waitcnt vmcnt(28) lgkmcnt(3)\n"
    "v_mfma_f32_16x16x32_bf16 v[64:67], v[60:63], v[144:147], v[64:67]\n"
    "ds_read_b128 v[160:163], v1 offset:17408\n"
    "s_waitcnt lgkmcnt(3)\n"
    "v_mfma_f32_16x16x32_bf16 v[68:71], v[60:63], v[148:151], v[68:71]\n"
    "s_waitcnt lgkmcnt(2)\n"
    "v_mfma_f32_16x16x32_bf16 v[72:75], v[60:63], v[152:155], v[72:75]\n"
    "s_waitcnt lgkmcnt(1)\n"
    "v_mfma_f32_16x16x32_bf16 v[76:79], v[60:63], v[156:159], v[76:79]\n"
    "buffer_load_dwordx4 v[60:63], v1, s[20:23], s58 offen offset:1024 sc1\n"
    "s_waitcnt vmcnt(28)\n"
    "v_mfma_f32_16x16x32_bf16 v[80:83], v[224:227], v[144:147], v[80:83]\n"
    "ds_read_b128 v[164:167], v1 offset:35840\n"
    "v_mfma_f32_16x16x32_bf16 v[84:87], v[224:227], v[148:151], v[84:87]\n"
    "v_mfma_f32_16x16x32_bf16 v[88:91], v[224:227], v[152:155], v[88:91]\n"
    "v_mfma_f32_16x16x32_bf16 v[92:95], v[224:227], v[156:159], v[92:95]\n"
    "buffer_load_dwordx4 v[224:227], v252, s[20:23], s58 offen offset:1024 sc1\n"
    "s_waitcnt vmcnt(28)\n"
    "v_mfma_f32_16x16x32_bf16 v[96:99], v[228:231], v[144:147], v[96:99]\n"
    "ds_read_b128 v[168:171], v7 offset:17408\n"
    "v_mfma_f32_16x16x32_bf16 v[100:103], v[228:231], v[148:151], v[100:103]\n"
    "v_mfma_f32_16x16x32_bf16 v[104:107], v[228:231], v[152:155], v[104:107]\n"
    "v_mfma_f32_16x16x32_bf16 v[108:111], v[228:231], v[156:159], v[108:111]\n"
    "buffer_load_dwordx4 v[228:231], v253, s[20:23], s58 offen offset:1024 sc1\n"
    "s_waitcnt vmcnt(28)\n"
    "v_mfma_f32_16x16x32_bf16 v[112:115], v[232:235], v[144:147], v[112:115]\n"
    "ds_read_b128 v[172:175], v7 offset:35840\n"
    "v_mfma_f32_16x16x32_bf16 v[116:119], v[232:235], v[148:151], v[116:119]\n"
    "v_mfma_f32_16x16x32_bf16 v[120:123], v[232:235], v[152:155], v[120:123]\n"
    "v_mfma_f32_16x16x32_bf16 v[124:127], v[232:235], v[156:159], v[124:127]\n"
    "buffer_load_dwordx4 v[232:235], v254, s[20:23], s58 offen offset:1024 sc1\n"
    "s_waitcnt vmcnt(28) lgkmcnt(3)\n"
    "v_mfma_f32_16x16x32_bf16 v[64:67], v[236:239], v[160:163], v[64:67]\n"
    "s_waitcnt lgkmcnt(2)\n"
    "v_mfma_f32_16x16x32_bf16 v[68:71], v[236:239], v[164:167], v[68:71]\n"
    "s_waitcnt lgkmcnt(1)\n"
    "v_mfma_f32_16x16x32_bf16 v[72:75], v[236:239], v[168:171], v[72:75]\n"
    "s_waitcnt lgkmcnt(0)\n"
    "v_mfma_f32_16x16x32_bf16 v[76:79], v[236:239], v[172:175], v[76:79]\n"
    "buffer_load_dwordx4 v[236:239], v1, s[20:23], s58 offen offset:2048 sc1\n"
    "s_waitcnt vmcnt(28)\n"
    "v_mfma_f32_16x16x32_bf16 v[80:83], v[240:243], v[160:163], v[80:83]\n"
    "v_mfma_f32_16x16x32_bf16 v[84:87], v[240:243], v[164:167], v[84:87]\n"
    "v_mfma_f32_16x16x32_bf16 v[88:91], v[240:243], v[168:171], v[88:91]\n"
    "v_mfma_f32_16x16x32_bf16 v[92:95], v[240:243], v[172:175], v[92:95]\n"
    "buffer_load_dwordx4 v[240:243], v252, s[20:23], s58 offen offset:2048 sc1\n"
    "s_waitcnt vmcnt(28)\n"
    "v_mfma_f32_16x16x32_bf16 v[96:99], v[244:247], v[160:163], v[96:99]\n"
    "v_mfma_f32_16x16x32_bf16 v[100:103], v[244:247], v[164:167], v[100:103]\n"
    "v_mfma_f32_16x16x32_bf16 v[104:107], v[244:247], v[168:171], v[104:107]\n"
    "v_mfma_f32_16x16x32_bf16 v[108:111], v[244:247], v[172:175], v[108:111]\n"
    "buffer_load_dwordx4 v[244:247], v253, s[20:23], s58 offen offset:2048 sc1\n"
    "s_waitcnt vmcnt(28)\n"
    "v_mfma_f32_16x16x32_bf16 v[112:115], v[248:251], v[160:163], v[112:115]\n"
    "v_mfma_f32_16x16x32_bf16 v[116:119], v[248:251], v[164:167], v[116:119]\n"
    "v_mfma_f32_16x16x32_bf16 v[120:123], v[248:251], v[168:171], v[120:123]\n"
    "v_mfma_f32_16x16x32_bf16 v[124:127], v[248:251], v[172:175], v[124:127]\n"
    "buffer_load_dwordx4 v[248:251], v254, s[20:23], s58 offen offset:2048 sc1\n"
    "s_barrier\n"
    "s_setprio 0\n"
    "ds_read_b128 v[144:147], v24\n"
    "ds_read_b128 v[148:151], v24 offset:1024\n"
    "ds_read_b128 v[152:155], v24 offset:2048\n"
    "ds_read_b128 v[156:159], v24 offset:3072\n"
    "v_exp_f32 v14, v64\n"
    "v_exp_f32 v15, v65\n"
    "v_exp_f32 v16, v66\n"
    "v_exp_f32 v17, v67\n"
    "v_add_f32 v14, 1.0, v14\n"
    "v_add_f32 v15, 1.0, v15\n"
    "v_add_f32 v16, 1.0, v16\n"
    "v_add_f32 v17, 1.0, v17\n"
    "v_rcp_f32 v14, v14\n"
    "v_rcp_f32 v15, v15\n"
    "v_rcp_f32 v16, v16\n"
    "v_rcp_f32 v17, v17\n"
    "v_mul_f32 v64, v64, v14\n"
    "v_mul_f32 v65, v65, v15\n"
    "v_mul_f32 v66, v66, v16\n"
    "v_mul_f32 v67, v67, v17\n"
    "v_cvt_pk_bf16_f32 v64, v64, v65\n"
    "v_cvt_pk_bf16_f32 v65, v66, v67\n"
    "v_exp_f32 v18, v80\n"
    "v_exp_f32 v19, v81\n"
    "v_exp_f32 v20, v82\n"
    "v_exp_f32 v21, v83\n"
    "v_add_f32 v18, 1.0, v18\n"
    "v_add_f32 v19, 1.0, v19\n"
    "v_add_f32 v20, 1.0, v20\n"
    "v_add_f32 v21, 1.0, v21\n"
    "v_rcp_f32 v18, v18\n"
    "v_rcp_f32 v19, v19\n"
    "v_rcp_f32 v20, v20\n"
    "v_rcp_f32 v21, v21\n"
    "v_mul_f32 v80, v80, v18\n"
    "v_mul_f32 v81, v81, v19\n"
    "v_mul_f32 v82, v82, v20\n"
    "v_mul_f32 v83, v83, v21\n"
    "v_cvt_pk_bf16_f32 v66, v80, v81\n"
    "v_cvt_pk_bf16_f32 v67, v82, v83\n"
    "ds_write_b128 v22, v[64:67]\n"
    "v_exp_f32 v14, v68\n"
    "v_exp_f32 v15, v69\n"
    "v_exp_f32 v16, v70\n"
    "v_exp_f32 v17, v71\n"
    "v_add_f32 v14, 1.0, v14\n"
    "v_add_f32 v15, 1.0, v15\n"
    "v_add_f32 v16, 1.0, v16\n"
    "v_add_f32 v17, 1.0, v17\n"
    "v_rcp_f32 v14, v14\n"
    "v_rcp_f32 v15, v15\n"
    "v_rcp_f32 v16, v16\n"
    "v_rcp_f32 v17, v17\n"
    "v_mul_f32 v68, v68, v14\n"
    "v_mul_f32 v69, v69, v15\n"
    "v_mul_f32 v70, v70, v16\n"
    "v_mul_f32 v71, v71, v17\n"
    "v_cvt_pk_bf16_f32 v68, v68, v69\n"
    "v_cvt_pk_bf16_f32 v69, v70, v71\n"
    "v_exp_f32 v18, v84\n"
    "v_exp_f32 v19, v85\n"
    "v_exp_f32 v20, v86\n"
    "v_exp_f32 v21, v87\n"
    "v_add_f32 v18, 1.0, v18\n"
    "v_add_f32 v19, 1.0, v19\n"
    "v_add_f32 v20, 1.0, v20\n"
    "v_add_f32 v21, 1.0, v21\n"
    "v_rcp_f32 v18, v18\n"
    "v_rcp_f32 v19, v19\n"
    "v_rcp_f32 v20, v20\n"
    "v_rcp_f32 v21, v21\n"
    "v_mul_f32 v84, v84, v18\n"
    "v_mul_f32 v85, v85, v19\n"
    "v_mul_f32 v86, v86, v20\n"
    "v_mul_f32 v87, v87, v21\n"
    "v_cvt_pk_bf16_f32 v70, v84, v85\n"
    "v_cvt_pk_bf16_f32 v71, v86, v87\n"
    "ds_write_b128 v22, v[68:71] offset:1024\n"
    "v_exp_f32 v14, v72\n"
    "v_exp_f32 v15, v73\n"
    "v_exp_f32 v16, v74\n"
    "v_exp_f32 v17, v75\n"
    "v_add_f32 v14, 1.0, v14\n"
    "v_add_f32 v15, 1.0, v15\n"
    "v_add_f32 v16, 1.0, v16\n"
    "v_add_f32 v17, 1.0, v17\n"
    "v_rcp_f32 v14, v14\n"
    "v_rcp_f32 v15, v15\n"
    "v_rcp_f32 v16, v16\n"
    "v_rcp_f32 v17, v17\n"
    "v_mul_f32 v72, v72, v14\n"
    "v_mul_f32 v73, v73, v15\n"
    "v_mul_f32 v74, v74, v16\n"
    "v_mul_f32 v75, v75, v17\n"
    "v_cvt_pk_bf16_f32 v72, v72, v73\n"
    "v_cvt_pk_bf16_f32 v73, v74, v75\n"
    "v_exp_f32 v18, v88\n"
    "v_exp_f32 v19, v89\n"
    "v_exp_f32 v20, v90\n"
    "v_exp_f32 v21, v91\n"
    "v_add_f32 v18, 1.0, v18\n"
    "v_add_f32 v19, 1.0, v19\n"
    "v_add_f32 v20, 1.0, v20\n"
    "v_add_f32 v21, 1.0, v21\n"
    "v_rcp_f32 v18, v18\n"
    "v_rcp_f32 v19, v19\n"
    "v_rcp_f32 v20, v20\n"
    "v_rcp_f32 v21, v21\n"
    "v_mul_f32 v88, v88, v18\n"
    "v_mul_f32 v89, v89, v19\n"
    "v_mul_f32 v90, v90, v20\n"
    "v_mul_f32 v91, v91, v21\n"
    "v_cvt_pk_bf16_f32 v74, v88, v89\n"
    "v_cvt_pk_bf16_f32 v75, v90, v91\n"
    "ds_write_b128 v22, v[72:75] offset:2048\n"
    "v_exp_f32 v14, v76\n"
    "v_exp_f32 v15, v77\n"
    "v_exp_f32 v16, v78\n"
    "v_exp_f32 v17, v79\n"
    "v_add_f32 v14, 1.0, v14\n"
    "v_add_f32 v15, 1.0, v15\n"
    "v_add_f32 v16, 1.0, v16\n"
    "v_add_f32 v17, 1.0, v17\n"
    "v_rcp_f32 v14, v14\n"
    "v_rcp_f32 v15, v15\n"
    "v_rcp_f32 v16, v16\n"
    "v_rcp_f32 v17, v17\n"
    "v_mul_f32 v76, v76, v14\n"
    "v_mul_f32 v77, v77, v15\n"
    "v_mul_f32 v78, v78, v16\n"
    "v_mul_f32 v79, v79, v17\n"
    "v_cvt_pk_bf16_f32 v76, v76, v77\n"
    "v_cvt_pk_bf16_f32 v77, v78, v79\n"
    "v_exp_f32 v18, v92\n"
    "v_exp_f32 v19, v93\n"
    "v_exp_f32 v20, v94\n"
    "v_exp_f32 v21, v95\n"
    "v_add_f32 v18, 1.0, v18\n"
    "v_add_f32 v19, 1.0, v19\n"
    "v_add_f32 v20, 1.0, v20\n"
    "v_add_f32 v21, 1.0, v21\n"
    "v_rcp_f32 v18, v18\n"
    "v_rcp_f32 v19, v19\n"
    "v_rcp_f32 v20, v20\n"
    "v_rcp_f32 v21, v21\n"
    "v_mul_f32 v92, v92, v18\n"
    "v_mul_f32 v93, v93, v19\n"
    "v_mul_f32 v94, v94, v20\n"
    "v_mul_f32 v95, v95, v21\n"
    "v_cvt_pk_bf16_f32 v78, v92, v93\n"
    "v_cvt_pk_bf16_f32 v79, v94, v95\n"
    "ds_write_b128 v22, v[76:79] offset:3072\n"
    "v_exp_f32 v14, v96\n"
    "v_exp_f32 v15, v97\n"
    "v_exp_f32 v16, v98\n"
    "v_exp_f32 v17, v99\n"
    "v_add_f32 v14, 1.0, v14\n"
    "v_add_f32 v15, 1.0, v15\n"
    "v_add_f32 v16, 1.0, v16\n"
    "v_add_f32 v17, 1.0, v17\n"
    "v_rcp_f32 v14, v14\n"
    "v_rcp_f32 v15, v15\n"
    "v_rcp_f32 v16, v16\n"
    "v_rcp_f32 v17, v17\n"
    "v_mul_f32 v96, v96, v14\n"
    "v_mul_f32 v97, v97, v15\n"
    "v_mul_f32 v98, v98, v16\n"
    "v_mul_f32 v99, v99, v17\n"
    "v_cvt_pk_bf16_f32 v96, v96, v97\n"
    "v_cvt_pk_bf16_f32 v97, v98, v99\n"
    "v_exp_f32 v18, v112\n"
    "v_exp_f32 v19, v113\n"
    "v_exp_f32 v20, v114\n"
    "v_exp_f32 v21, v115\n"
    "v_add_f32 v18, 1.0, v18\n"
    "v_add_f32 v19, 1.0, v19\n"
    "v_add_f32 v20, 1.0, v20\n"
    "v_add_f32 v21, 1.0, v21\n"
    "v_rcp_f32 v18, v18\n"
    "v_rcp_f32 v19, v19\n"
    "v_rcp_f32 v20, v20\n"
    "v_rcp_f32 v21, v21\n"
    "v_mul_f32 v112, v112, v18\n"
    "v_mul_f32 v113, v113, v19\n"
    "v_mul_f32 v114, v114, v20\n"
    "v_mul_f32 v115, v115, v21\n"
    "v_cvt_pk_bf16_f32 v98, v112, v113\n"
    "v_cvt_pk_bf16_f32 v99, v114, v115\n"
    "ds_write_b128 v22, v[96:99] offset:4096\n"
    "v_exp_f32 v14, v100\n"
    "v_exp_f32 v15, v101\n"
    "v_exp_f32 v16, v102\n"
    "v_exp_f32 v17, v103\n"
    "v_add_f32 v14, 1.0, v14\n"
    "v_add_f32 v15, 1.0, v15\n"
    "v_add_f32 v16, 1.0, v16\n"
    "v_add_f32 v17, 1.0, v17\n"
    "v_rcp_f32 v14, v14\n"
    "v_rcp_f32 v15, v15\n"
    "v_rcp_f32 v16, v16\n"
    "v_rcp_f32 v17, v17\n"
    "v_mul_f32 v100, v100, v14\n"
    "v_mul_f32 v101, v101, v15\n"
    "v_mul_f32 v102, v102, v16\n"
    "v_mul_f32 v103, v103, v17\n"
    "v_cvt_pk_bf16_f32 v100, v100, v101\n"
    "v_cvt_pk_bf16_f32 v101, v102, v103\n"
    "v_exp_f32 v18, v116\n"
    "v_exp_f32 v19, v117\n"
    "v_exp_f32 v20, v118\n"
    "v_exp_f32 v21, v119\n"
    "v_add_f32 v18, 1.0, v18\n"
    "v_add_f32 v19, 1.0, v19\n"
    "v_add_f32 v20, 1.0, v20\n"
    "v_add_f32 v21, 1.0, v21\n"
    "v_rcp_f32 v18, v18\n"
    "v_rcp_f32 v19, v19\n"
    "v_rcp_f32 v20, v20\n"
    "v_rcp_f32 v21, v21\n"
    "v_mul_f32 v116, v116, v18\n"
    "v_mul_f32 v117, v117, v19\n"
    "v_mul_f32 v118, v118, v20\n"
    "v_mul_f32 v119, v119, v21\n"
    "v_cvt_pk_bf16_f32 v102, v116, v117\n"
    "v_cvt_pk_bf16_f32 v103, v118, v119\n"
    "ds_write_b128 v22, v[100:103] offset:5120\n"
    "v_exp_f32 v14, v104\n"
    "v_exp_f32 v15, v105\n"
    "v_exp_f32 v16, v106\n"
    "v_exp_f32 v17, v107\n"
    "v_add_f32 v14, 1.0, v14\n"
    "v_add_f32 v15, 1.0, v15\n"
    "v_add_f32 v16, 1.0, v16\n"
    "v_add_f32 v17, 1.0, v17\n"
    "v_rcp_f32 v14, v14\n"
    "v_rcp_f32 v15, v15\n"
    "v_rcp_f32 v16, v16\n"
    "v_rcp_f32 v17, v17\n"
    "v_mul_f32 v104, v104, v14\n"
    "v_mul_f32 v105, v105, v15\n"
    "v_mul_f32 v106, v106, v16\n"
    "v_mul_f32 v107, v107, v17\n"
    "v_cvt_pk_bf16_f32 v104, v104, v105\n"
    "v_cvt_pk_bf16_f32 v105, v106, v107\n"
    "v_exp_f32 v18, v120\n"
    "v_exp_f32 v19, v121\n"
    "v_exp_f32 v20, v122\n"
    "v_exp_f32 v21, v123\n"
    "v_add_f32 v18, 1.0, v18\n"
    "v_add_f32 v19, 1.0, v19\n"
    "v_add_f32 v20, 1.0, v20\n"
    "v_add_f32 v21, 1.0, v21\n"
    "v_rcp_f32 v18, v18\n"
    "v_rcp_f32 v19, v19\n"
    "v_rcp_f32 v20, v20\n"
    "v_rcp_f32 v21, v21\n"
    "v_mul_f32 v120, v120, v18\n"
    "v_mul_f32 v121, v121, v19\n"
    "v_mul_f32 v122, v122, v20\n"
    "v_mul_f32 v123, v123, v21\n"
    "v_cvt_pk_bf16_f32 v106, v120, v121\n"
    "v_cvt_pk_bf16_f32 v107, v122, v123\n"
    "ds_write_b128 v22, v[104:107] offset:6144\n"
    "v_exp_f32 v14, v108\n"
    "v_exp_f32 v15, v109\n"
    "v_exp_f32 v16, v110\n"
    "v_exp_f32 v17, v111\n"
    "v_add_f32 v14, 1.0, v14\n"
    "v_add_f32 v15, 1.0, v15\n"
    "v_add_f32 v16, 1.0, v16\n"
    "v_add_f32 v17, 1.0, v17\n"
    "v_rcp_f32 v14, v14\n"
    "v_rcp_f32 v15, v15\n"
    "v_rcp_f32 v16, v16\n"
    "v_rcp_f32 v17, v17\n"
    "v_mul_f32 v108, v108, v14\n"
    "v_mul_f32 v109, v109, v15\n"
    "v_mul_f32 v110, v110, v16\n"
    "v_mul_f32 v111, v111, v17\n"
    "v_cvt_pk_bf16_f32 v108, v108, v109\n"
    "v_cvt_pk_bf16_f32 v109, v110, v111\n"
    "v_exp_f32 v18, v124\n"
    "v_exp_f32 v19, v125\n"
    "v_exp_f32 v20, v126\n"
    "v_exp_f32 v21, v127\n"
    "v_add_f32 v18, 1.0, v18\n"
    "v_add_f32 v19, 1.0, v19\n"
    "v_add_f32 v20, 1.0, v20\n"
    "v_add_f32 v21, 1.0, v21\n"
    "v_rcp_f32 v18, v18\n"
    "v_rcp_f32 v19, v19\n"
    "v_rcp_f32 v20, v20\n"
    "v_rcp_f32 v21, v21\n"
    "v_mul_f32 v124, v124, v18\n"
    "v_mul_f32 v125, v125, v19\n"
    "v_mul_f32 v126, v126, v20\n"
    "v_mul_f32 v127, v127, v21\n"
    "v_cvt_pk_bf16_f32 v110, v124, v125\n"
    "v_cvt_pk_bf16_f32 v111, v126, v127\n"
    "ds_write_b128 v22, v[108:111] offset:7168\n"
    "s_setprio 1\n"
    "s_waitcnt lgkmcnt(0)\n"
    "s_barrier\n"
    "s_waitcnt vmcnt(28)\n"
    "v_mfma_f32_16x16x32_bf16 v[64:67], v[176:179], v[144:147], 0\n"
    "ds_read_b128 v[160:163], v24 offset:4096\n"
    "v_mfma_f32_16x16x32_bf16 v[68:71], v[176:179], v[148:151], 0\n"
    "v_mfma_f32_16x16x32_bf16 v[72:75], v[176:179], v[152:155], 0\n"
    "v_mfma_f32_16x16x32_bf16 v[76:79], v[176:179], v[156:159], 0\n"
    "buffer_load_dwordx4 v[176:179], v1, s[20:23], s58 offen offset:3072 sc1\n"
    "s_waitcnt vmcnt(28)\n"
    "v_mfma_f32_16x16x32_bf16 v[80:83], v[180:183], v[144:147], 0\n"
    "ds_read_b128 v[164:167], v24 offset:5120\n"
    "v_mfma_f32_16x16x32_bf16 v[84:87], v[180:183], v[148:151], 0\n"
    "v_mfma_f32_16x16x32_bf16 v[88:91], v[180:183], v[152:155], 0\n"
    "v_mfma_f32_16x16x32_bf16 v[92:95], v[180:183], v[156:159], 0\n"
    "buffer_load_dwordx4 v[180:183], v252, s[20:23], s58 offen offset:3072 sc1\n"
    "s_waitcnt vmcnt(28)\n"
    "v_mfma_f32_16x16x32_bf16 v[96:99], v[184:187], v[144:147], 0\n"
    "ds_read_b128 v[168:171], v24 offset:6144\n"
    "v_mfma_f32_16x16x32_bf16 v[100:103], v[184:187], v[148:151], 0\n"
    "v_mfma_f32_16x16x32_bf16 v[104:107], v[184:187], v[152:155], 0\n"
    "v_mfma_f32_16x16x32_bf16 v[108:111], v[184:187], v[156:159], 0\n"
    "buffer_load_dwordx4 v[184:187], v253, s[20:23], s58 offen offset:3072 sc1\n"
    "s_waitcnt vmcnt(28)\n"
    "v_mfma_f32_16x16x32_bf16 v[112:115], v[188:191], v[144:147], 0\n"
    "ds_read_b128 v[172:175], v24 offset:7168\n"
    "v_mfma_f32_16x16x32_bf16 v[116:119], v[188:191], v[148:151], 0\n"
    "v_mfma_f32_16x16x32_bf16 v[120:123], v[188:191], v[152:155], 0\n"
    "v_mfma_f32_16x16x32_bf16 v[124:127], v[188:191], v[156:159], 0\n"
    "buffer_load_dwordx4 v[188:191], v254, s[20:23], s58 offen offset:3072 sc1\n"
    "s_waitcnt vmcnt(28) lgkmcnt(3)\n"
    "v_mfma_f32_16x16x32_bf16 v[64:67], v[192:195], v[160:163], v[64:67]\n"
    "ds_read_b128 v[144:147], v24 offset:8192\n"
    "s_waitcnt lgkmcnt(3)\n"
    "v_mfma_f32_16x16x32_bf16 v[68:71], v[192:195], v[164:167], v[68:71]\n"
    "s_waitcnt lgkmcnt(2)\n"
    "v_mfma_f32_16x16x32_bf16 v[72:75], v[192:195], v[168:171], v[72:75]\n"
    "s_waitcnt lgkmcnt(1)\n"
    "v_mfma_f32_16x16x32_bf16 v[76:79], v[192:195], v[172:175], v[76:79]\n"
    "s_add_u32 s58, s58, 12288\n"
    "buffer_load_dwordx4 v[192:195], v1, s[20:23], s58 offen sc1\n"
    "s_waitcnt vmcnt(28)\n"
    "v_mfma_f32_16x16x32_bf16 v[80:83], v[196:199], v[160:163], v[80:83]\n"
    "ds_read_b128 v[148:151], v24 offset:9216\n"
    "v_mfma_f32_16x16x32_bf16 v[84:87], v[196:199], v[164:167], v[84:87]\n"
    "v_mfma_f32_16x16x32_bf16 v[88:91], v[196:199], v[168:171], v[88:91]\n"
    "v_mfma_f32_16x16x32_bf16 v[92:95], v[196:199], v[172:175], v[92:95]\n"
    "buffer_load_dwordx4 v[196:199], v252, s[20:23], s58 offen sc1\n"
    "s_waitcnt vmcnt(28)\n"
    "v_mfma_f32_16x16x32_bf16 v[96:99], v[200:203], v[160:163], v[96:99]\n"
    "ds_read_b128 v[152:155], v24 offset:10240\n"
    "v_mfma_f32_16x16x32_bf16 v[100:103], v[200:203], v[164:167], v[100:103]\n"
    "v_mfma_f32_16x16x32_bf16 v[104:107], v[200:203], v[168:171], v[104:107]\n"
    "v_mfma_f32_16x16x32_bf16 v[108:111], v[200:203], v[172:175], v[108:111]\n"
    "buffer_load_dwordx4 v[200:203], v253, s[20:23], s58 offen sc1\n"
    "s_waitcnt vmcnt(28)\n"
    "v_mfma_f32_16x16x32_bf16 v[112:115], v[204:207], v[160:163], v[112:115]\n"
    "ds_read_b128 v[156:159], v24 offset:11264\n"
    "v_mfma_f32_16x16x32_bf16 v[116:119], v[204:207], v[164:167], v[116:119]\n"
    "v_mfma_f32_16x16x32_bf16 v[120:123], v[204:207], v[168:171], v[120:123]\n"
    "v_mfma_f32_16x16x32_bf16 v[124:127], v[204:207], v[172:175], v[124:127]\n"
    "buffer_load_dwordx4 v[204:207], v254, s[20:23], s58 offen sc1\n"
    "s_waitcnt vmcnt(28) lgkmcnt(3)\n"
    "v_mfma_f32_16x16x32_bf16 v[64:67], v[208:211], v[144:147], v[64:67]\n"
    "ds_read_b128 v[160:163], v24 offset:12288\n"
    "s_waitcnt lgkmcnt(3)\n"
    "v_mfma_f32_16x16x32_bf16 v[68:71], v[208:211], v[148:151], v[68:71]\n"
    "s_waitcnt lgkmcnt(2)\n"
    "v_mfma_f32_16x16x32_bf16 v[72:75], v[208:211], v[152:155], v[72:75]\n"
    "s_waitcnt lgkmcnt(1)\n"
    "v_mfma_f32_16x16x32_bf16 v[76:79], v[208:211], v[156:159], v[76:79]\n"
    "buffer_load_dwordx4 v[208:211], v1, s[20:23], s58 offen offset:1024 sc1\n"
    "s_waitcnt vmcnt(28)\n"
    "v_mfma_f32_16x16x32_bf16 v[80:83], v[212:215], v[144:147], v[80:83]\n"
    "ds_read_b128 v[164:167], v24 offset:13312\n"
    "v_mfma_f32_16x16x32_bf16 v[84:87], v[212:215], v[148:151], v[84:87]\n"
    "v_mfma_f32_16x16x32_bf16 v[88:91], v[212:215], v[152:155], v[88:91]\n"
    "v_mfma_f32_16x16x32_bf16 v[92:95], v[212:215], v[156:159], v[92:95]\n"
    "buffer_load_dwordx4 v[212:215], v252, s[20:23], s58 offen offset:1024 sc1\n"
    "s_waitcnt vmcnt(28)\n"
    "v_mfma_f32_16x16x32_bf16 v[96:99], v[216:219], v[144:147], v[96:99]\n"
    "ds_read_b128 v[168:171], v24 offset:14336\n"
    "v_mfma_f32_16x16x32_bf16 v[100:103], v[216:219], v[148:151], v[100:103]\n"
    "v_mfma_f32_16x16x32_bf16 v[104:107], v[216:219], v[152:155], v[104:107]\n"
    "v_mfma_f32_16x16x32_bf16 v[108:111], v[216:219], v[156:159], v[108:111]\n"
    "buffer_load_dwordx4 v[216:219], v253, s[20:23], s58 offen offset:1024 sc1\n"
    "s_waitcnt vmcnt(28)\n"
    "v_mfma_f32_16x16x32_bf16 v[112:115], v[220:223], v[144:147], v[112:115]\n"
    "ds_read_b128 v[172:175], v24 offset:15360\n"
    "v_mfma_f32_16x16x32_bf16 v[116:119], v[220:223], v[148:151], v[116:119]\n"
    "v_mfma_f32_16x16x32_bf16 v[120:123], v[220:223], v[152:155], v[120:123]\n"
    "v_mfma_f32_16x16x32_bf16 v[124:127], v[220:223], v[156:159], v[124:127]\n"
    "buffer_load_dwordx4 v[220:223], v254, s[20:23], s58 offen offset:1024 sc1\n"
    "s_waitcnt vmcnt(27) lgkmcnt(3)\n"
    "v_mfma_f32_16x16x32_bf16 v[64:67], v[28:31], v[160:163], v[64:67]\n"
    "ds_read_b128 v[144:147], v24 offset:16384\n"
    "s_waitcnt lgkmcnt(3)\n"
    "v_mfma_f32_16x16x32_bf16 v[68:71], v[28:31], v[164:167], v[68:71]\n"
    "s_waitcnt lgkmcnt(2)\n"
    "v_mfma_f32_16x16x32_bf16 v[72:75], v[28:31], v[168:171], v[72:75]\n"
    "s_waitcnt lgkmcnt(1)\n"
    "v_mfma_f32_16x16x32_bf16 v[76:79], v[28:31], v[172:175], v[76:79]\n"
    "s_sub_u32 s58, s58, 8192\n"
    "buffer_load_dwordx4 v[28:31], v1, s[20:23], s58 offen sc1\n"
    "s_waitcnt vmcnt(27)\n"
    "v_mfma_f32_16x16x32_bf16 v[80:83], v[32:35], v[160:163], v[80:83]\n"
    "ds_read_b128 v[148:151], v24 offset:17408\n"
    "v_mfma_f32_16x16x32_bf16 v[84:87], v[32:35], v[164:167], v[84:87]\n"
    "v_mfma_f32_16x16x32_bf16 v[88:91], v[32:35], v[168:171], v[88:91]\n"
    "v_mfma_f32_16x16x32_bf16 v[92:95], v[32:35], v[172:175], v[92:95]\n"
    "buffer_load_dwordx4 v[32:35], v252, s[20:23], s58 offen sc1\n"
    "s_waitcnt vmcnt(27)\n"
    "v_mfma_f32_16x16x32_bf16 v[96:99], v[36:39], v[160:163], v[96:99]\n"
    "ds_read_b128 v[152:155], v24 offset:18432\n"
    "v_mfma_f32_16x16x32_bf16 v[100:103], v[36:39], v[164:167], v[100:103]\n"
    "v_mfma_f32_16x16x32_bf16 v[104:107], v[36:39], v[168:171], v[104:107]\n"
    "v_mfma_f32_16x16x32_bf16 v[108:111], v[36:39], v[172:175], v[108:111]\n"
    "buffer_load_dwordx4 v[36:39], v253, s[20:23], s58 offen sc1\n"
    "s_waitcnt vmcnt(27)\n"
    "v_mfma_f32_16x16x32_bf16 v[112:115], v[40:43], v[160:163], v[112:115]\n"
    "ds_read_b128 v[156:159], v24 offset:19456\n"
    "v_mfma_f32_16x16x32_bf16 v[116:119], v[40:43], v[164:167], v[116:119]\n"
    "v_mfma_f32_16x16x32_bf16 v[120:123], v[40:43], v[168:171], v[120:123]\n"
    "v_mfma_f32_16x16x32_bf16 v[124:127], v[40:43], v[172:175], v[124:127]\n"
    "buffer_load_dwordx4 v[40:43], v254, s[20:23], s58 offen sc1\n"
    "s_waitcnt vmcnt(27) lgkmcnt(3)\n"
    "v_mfma_f32_16x16x32_bf16 v[64:67], v[44:47], v[144:147], v[64:67]\n"
    "ds_read_b128 v[160:163], v24 offset:20480\n"
    "s_waitcnt lgkmcnt(3)\n"
    "v_mfma_f32_16x16x32_bf16 v[68:71], v[44:47], v[148:151], v[68:71]\n"
    "s_waitcnt lgkmcnt(2)\n"
    "v_mfma_f32_16x16x32_bf16 v[72:75], v[44:47], v[152:155], v[72:75]\n"
    "s_waitcnt lgkmcnt(1)\n"
    "v_mfma_f32_16x16x32_bf16 v[76:79], v[44:47], v[156:159], v[76:79]\n"
    "buffer_load_dwordx4 v[44:47], v1, s[20:23], s58 offen offset:1024 sc1\n"
    "s_waitcnt vmcnt(27)\n"
    "v_mfma_f32_16x16x32_bf16 v[80:83], v[48:51], v[144:147], v[80:83]\n"
    "ds_read_b128 v[164:167], v24 offset:21504\n"
    "v_mfma_f32_16x16x32_bf16 v[84:87], v[48:51], v[148:151], v[84:87]\n"
    "v_mfma_f32_16x16x32_bf16 v[88:91], v[48:51], v[152:155], v[88:91]\n"
    "v_mfma_f32_16x16x32_bf16 v[92:95], v[48:51], v[156:159], v[92:95]\n"
    "buffer_load_dwordx4 v[48:51], v252, s[20:23], s58 offen offset:1024 sc1\n"
    "s_waitcnt vmcnt(27)\n"
    "v_mfma_f32_16x16x32_bf16 v[96:99], v[52:55], v[144:147], v[96:99]\n"
    "ds_read_b128 v[168:171], v24 offset:22528\n"
    "v_mfma_f32_16x16x32_bf16 v[100:103], v[52:55], v[148:151], v[100:103]\n"
    "v_mfma_f32_16x16x32_bf16 v[104:107], v[52:55], v[152:155], v[104:107]\n"
    "v_mfma_f32_16x16x32_bf16 v[108:111], v[52:55], v[156:159], v[108:111]\n"
    "buffer_load_dwordx4 v[52:55], v253, s[20:23], s58 offen offset:1024 sc1\n"
    "s_waitcnt vmcnt(27)\n"
    "v_mfma_f32_16x16x32_bf16 v[112:115], v[56:59], v[144:147], v[112:115]\n"
    "ds_read_b128 v[172:175], v24 offset:23552\n"
    "v_mfma_f32_16x16x32_bf16 v[116:119], v[56:59], v[148:151], v[116:119]\n"
    "v_mfma_f32_16x16x32_bf16 v[120:123], v[56:59], v[152:155], v[120:123]\n"
    "v_mfma_f32_16x16x32_bf16 v[124:127], v[56:59], v[156:159], v[124:127]\n"
    "buffer_load_dwordx4 v[56:59], v254, s[20:23], s58 offen offset:1024 sc1\n"
    "s_waitcnt vmcnt(27) lgkmcnt(3)\n"
    "v_mfma_f32_16x16x32_bf16 v[64:67], v[60:63], v[160:163], v[64:67]\n"
    "ds_read_b128 v[144:147], v24 offset:24576\n"
    "s_waitcnt lgkmcnt(3)\n"
    "v_mfma_f32_16x16x32_bf16 v[68:71], v[60:63], v[164:167], v[68:71]\n"
    "s_waitcnt lgkmcnt(2)\n"
    "v_mfma_f32_16x16x32_bf16 v[72:75], v[60:63], v[168:171], v[72:75]\n"
    "s_waitcnt lgkmcnt(1)\n"
    "v_mfma_f32_16x16x32_bf16 v[76:79], v[60:63], v[172:175], v[76:79]\n"
    "buffer_load_dwordx4 v[60:63], v1, s[20:23], s58 offen offset:2048 sc1\n"
    "s_waitcnt vmcnt(27)\n"
    "v_mfma_f32_16x16x32_bf16 v[80:83], v[224:227], v[160:163], v[80:83]\n"
    "ds_read_b128 v[148:151], v24 offset:25600\n"
    "v_mfma_f32_16x16x32_bf16 v[84:87], v[224:227], v[164:167], v[84:87]\n"
    "v_mfma_f32_16x16x32_bf16 v[88:91], v[224:227], v[168:171], v[88:91]\n"
    "v_mfma_f32_16x16x32_bf16 v[92:95], v[224:227], v[172:175], v[92:95]\n"
    "buffer_load_dwordx4 v[224:227], v252, s[20:23], s58 offen offset:2048 sc1\n"
    "s_waitcnt vmcnt(27)\n"
    "v_mfma_f32_16x16x32_bf16 v[96:99], v[228:231], v[160:163], v[96:99]\n"
    "ds_read_b128 v[152:155], v24 offset:26624\n"
    "v_mfma_f32_16x16x32_bf16 v[100:103], v[228:231], v[164:167], v[100:103]\n"
    "v_mfma_f32_16x16x32_bf16 v[104:107], v[228:231], v[168:171], v[104:107]\n"
    "v_mfma_f32_16x16x32_bf16 v[108:111], v[228:231], v[172:175], v[108:111]\n"
    "buffer_load_dwordx4 v[228:231], v253, s[20:23], s58 offen offset:2048 sc1\n"
    "s_waitcnt vmcnt(27)\n"
    "v_mfma_f32_16x16x32_bf16 v[112:115], v[232:235], v[160:163], v[112:115]\n"
    "ds_read_b128 v[156:159], v24 offset:27648\n"
    "v_mfma_f32_16x16x32_bf16 v[116:119], v[232:235], v[164:167], v[116:119]\n"
    "v_mfma_f32_16x16x32_bf16 v[120:123], v[232:235], v[168:171], v[120:123]\n"
    "v_mfma_f32_16x16x32_bf16 v[124:127], v[232:235], v[172:175], v[124:127]\n"
    "buffer_load_dwordx4 v[232:235], v254, s[20:23], s58 offen offset:2048 sc1\n"
    "s_waitcnt vmcnt(27) lgkmcnt(3)\n"
    "v_mfma_f32_16x16x32_bf16 v[64:67], v[236:239], v[144:147], v[64:67]\n"
    "ds_read_b128 v[160:163], v24 offset:28672\n"
    "s_waitcnt lgkmcnt(3)\n"
    "v_mfma_f32_16x16x32_bf16 v[68:71], v[236:239], v[148:151], v[68:71]\n"
    "s_waitcnt lgkmcnt(2)\n"
    "v_mfma_f32_16x16x32_bf16 v[72:75], v[236:239], v[152:155], v[72:75]\n"
    "s_waitcnt lgkmcnt(1)\n"
    "v_mfma_f32_16x16x32_bf16 v[76:79], v[236:239], v[156:159], v[76:79]\n"
    "buffer_load_dwordx4 v[236:239], v1, s[20:23], s58 offen offset:3072 sc1\n"
    "s_waitcnt vmcnt(27)\n"
    "v_mfma_f32_16x16x32_bf16 v[80:83], v[240:243], v[144:147], v[80:83]\n"
    "ds_read_b128 v[164:167], v24 offset:29696\n"
    "v_mfma_f32_16x16x32_bf16 v[84:87], v[240:243], v[148:151], v[84:87]\n"
    "v_mfma_f32_16x16x32_bf16 v[88:91], v[240:243], v[152:155], v[88:91]\n"
    "v_mfma_f32_16x16x32_bf16 v[92:95], v[240:243], v[156:159], v[92:95]\n"
    "buffer_load_dwordx4 v[240:243], v252, s[20:23], s58 offen offset:3072 sc1\n"
    "s_waitcnt vmcnt(27)\n"
    "v_mfma_f32_16x16x32_bf16 v[96:99], v[244:247], v[144:147], v[96:99]\n"
    "ds_read_b128 v[168:171], v24 offset:30720\n"
    "v_mfma_f32_16x16x32_bf16 v[100:103], v[244:247], v[148:151], v[100:103]\n"
    "v_mfma_f32_16x16x32_bf16 v[104:107], v[244:247], v[152:155], v[104:107]\n"
    "v_mfma_f32_16x16x32_bf16 v[108:111], v[244:247], v[156:159], v[108:111]\n"
    "buffer_load_dwordx4 v[244:247], v253, s[20:23], s58 offen offset:3072 sc1\n"
    "s_waitcnt vmcnt(27)\n"
    "v_mfma_f32_16x16x32_bf16 v[112:115], v[248:251], v[144:147], v[112:115]\n"
    "ds_read_b128 v[172:175], v24 offset:31744\n"
    "v_mfma_f32_16x16x32_bf16 v[116:119], v[248:251], v[148:151], v[116:119]\n"
    "v_mfma_f32_16x16x32_bf16 v[120:123], v[248:251], v[152:155], v[120:123]\n"
    "v_mfma_f32_16x16x32_bf16 v[124:127], v[248:251], v[156:159], v[124:127]\n"
    "buffer_load_dwordx4 v[248:251], v254, s[20:23], s58 offen offset:3072 sc1\n"
    "s_waitcnt vmcnt(27) lgkmcnt(3)\n"
    "v_mfma_f32_16x16x32_bf16 v[64:67], v[176:179], v[160:163], v[64:67]\n"
    "ds_read_b128 v[144:147], v25\n"
    "s_waitcnt lgkmcnt(3)\n"
    "v_mfma_f32_16x16x32_bf16 v[68:71], v[176:179], v[164:167], v[68:71]\n"
    "s_waitcnt lgkmcnt(2)\n"
    "v_mfma_f32_16x16x32_bf16 v[72:75], v[176:179], v[168:171], v[72:75]\n"
    "s_waitcnt lgkmcnt(1)\n"
    "v_mfma_f32_16x16x32_bf16 v[76:79], v[176:179], v[172:175], v[76:79]\n"
    "s_add_u32 s58, s58, 4096\n"
    "buffer_load_dwordx4 v[176:179], v1, s[20:23], s58 offen sc1\n"
    "s_waitcnt vmcnt(27)\n"
    "v_mfma_f32_16x16x32_bf16 v[80:83], v[180:183], v[160:163], v[80:83]\n"
    "ds_read_b128 v[148:151], v25 offset:1024\n"
    "v_mfma_f32_16x16x32_bf16 v[84:87], v[180:183], v[164:167], v[84:87]\n"
    "v_mfma_f32_16x16x32_bf16 v[88:91], v[180:183], v[168:171], v[88:91]\n"
    "v_mfma_f32_16x16x32_bf16 v[92:95], v[180:183], v[172:175], v[92:95]\n"
    "buffer_load_dwordx4 v[180:183], v252, s[20:23], s58 offen sc1\n"
    "s_waitcnt vmcnt(27)\n"
    "v_mfma_f32_16x16x32_bf16 v[96:99], v[184:187], v[160:163], v[96:99]\n"
    "ds_read_b128 v[152:155], v25 offset:2048\n"
    "v_mfma_f32_16x16x32_bf16 v[100:103], v[184:187], v[164:167], v[100:103]\n"
    "v_mfma_f32_16x16x32_bf16 v[104:107], v[184:187], v[168:171], v[104:107]\n"
    "v_mfma_f32_16x16x32_bf16 v[108:111], v[184:187], v[172:175], v[108:111]\n"
    "buffer_load_dwordx4 v[184:187], v253, s[20:23], s58 offen sc1\n"
    "s_waitcnt vmcnt(27)\n"
    "v_mfma_f32_16x16x32_bf16 v[112:115], v[188:191], v[160:163], v[112:115]\n"
    "ds_read_b128 v[156:159], v25 offset:3072\n"
    "v_mfma_f32_16x16x32_bf16 v[116:119], v[188:191], v[164:167], v[116:119]\n"
    "v_mfma_f32_16x16x32_bf16 v[120:123], v[188:191], v[168:171], v[120:123]\n"
    "v_mfma_f32_16x16x32_bf16 v[124:127], v[188:191], v[172:175], v[124:127]\n"
    "buffer_load_dwordx4 v[188:191], v254, s[20:23], s58 offen sc1\n"
    "s_waitcnt vmcnt(27) lgkmcnt(3)\n"
    "v_mfma_f32_16x16x32_bf16 v[64:67], v[192:195], v[144:147], v[64:67]\n"
    "ds_read_b128 v[160:163], v25 offset:4096\n"
    "s_waitcnt lgkmcnt(3)\n"
    "v_mfma_f32_16x16x32_bf16 v[68:71], v[192:195], v[148:151], v[68:71]\n"
    "s_waitcnt lgkmcnt(2)\n"
    "v_mfma_f32_16x16x32_bf16 v[72:75], v[192:195], v[152:155], v[72:75]\n"
    "s_waitcnt lgkmcnt(1)\n"
    "v_mfma_f32_16x16x32_bf16 v[76:79], v[192:195], v[156:159], v[76:79]\n"
    "buffer_load_dwordx4 v[192:195], v1, s[20:23], s58 offen offset:1024 sc1\n"
    "s_waitcnt vmcnt(27)\n"
    "v_mfma_f32_16x16x32_bf16 v[80:83], v[196:199], v[144:147], v[80:83]\n"
    "ds_read_b128 v[164:167], v25 offset:5120\n"
    "v_mfma_f32_16x16x32_bf16 v[84:87], v[196:199], v[148:151], v[84:87]\n"
    "v_mfma_f32_16x16x32_bf16 v[88:91], v[196:199], v[152:155], v[88:91]\n"
    "v_mfma_f32_16x16x32_bf16 v[92:95], v[196:199], v[156:159], v[92:95]\n"
    "buffer_load_dwordx4 v[196:199], v252, s[20:23], s58 offen offset:1024 sc1\n"
    "s_waitcnt vmcnt(27)\n"
    "v_mfma_f32_16x16x32_bf16 v[96:99], v[200:203], v[144:147], v[96:99]\n"
    "ds_read_b128 v[168:171], v25 offset:6144\n"
    "v_mfma_f32_16x16x32_bf16 v[100:103], v[200:203], v[148:151], v[100:103]\n"
    "v_mfma_f32_16x16x32_bf16 v[104:107], v[200:203], v[152:155], v[104:107]\n"
    "v_mfma_f32_16x16x32_bf16 v[108:111], v[200:203], v[156:159], v[108:111]\n"
    "buffer_load_dwordx4 v[200:203], v253, s[20:23], s58 offen offset:1024 sc1\n"
    "s_waitcnt vmcnt(27)\n"
    "v_mfma_f32_16x16x32_bf16 v[112:115], v[204:207], v[144:147], v[112:115]\n"
    "ds_read_b128 v[172:175], v25 offset:7168\n"
    "v_mfma_f32_16x16x32_bf16 v[116:119], v[204:207], v[148:151], v[116:119]\n"
    "v_mfma_f32_16x16x32_bf16 v[120:123], v[204:207], v[152:155], v[120:123]\n"
    "v_mfma_f32_16x16x32_bf16 v[124:127], v[204:207], v[156:159], v[124:127]\n"
    "buffer_load_dwordx4 v[204:207], v254, s[20:23], s58 offen offset:1024 sc1\n"
    "s_waitcnt vmcnt(27) lgkmcnt(3)\n"
    "v_mfma_f32_16x16x32_bf16 v[64:67], v[208:211], v[160:163], v[64:67]\n"
    "ds_read_b128 v[144:147], v24 offset:32768\n"
    "s_waitcnt lgkmcnt(3)\n"
    "v_mfma_f32_16x16x32_bf16 v[68:71], v[208:211], v[164:167], v[68:71]\n"
    "s_waitcnt lgkmcnt(2)\n"
    "v_mfma_f32_16x16x32_bf16 v[72:75], v[208:211], v[168:171], v[72:75]\n"
    "s_waitcnt lgkmcnt(1)\n"
    "v_mfma_f32_16x16x32_bf16 v[76:79], v[208:211], v[172:175], v[76:79]\n"
    "buffer_load_dwordx4 v[208:211], v1, s[20:23], s58 offen offset:2048 sc1\n"
    "s_waitcnt vmcnt(27)\n"
    "v_mfma_f32_16x16x32_bf16 v[80:83], v[212:215], v[160:163], v[80:83]\n"
    "ds_read_b128 v[148:151], v24 offset:33792\n"
    "v_mfma_f32_16x16x32_bf16 v[84:87], v[212:215], v[164:167], v[84:87]\n"
    "v_mfma_f32_16x16x32_bf16 v[88:91], v[212:215], v[168:171], v[88:91]\n"
    "v_mfma_f32_16x16x32_bf16 v[92:95], v[212:215], v[172:175], v[92:95]\n"
    "buffer_load_dwordx4 v[212:215], v252, s[20:23], s58 offen offset:2048 sc1\n"
    "s_waitcnt vmcnt(27)\n"
    "v_mfma_f32_16x16x32_bf16 v[96:99], v[216:219], v[160:163], v[96:99]\n"
    "ds_read_b128 v[152:155], v24 offset:34816\n"
    "v_mfma_f32_16x16x32_bf16 v[100:103], v[216:219], v[164:167], v[100:103]\n"
    "v_mfma_f32_16x16x32_bf16 v[104:107], v[216:219], v[168:171], v[104:107]\n"
    "v_mfma_f32_16x16x32_bf16 v[108:111], v[216:219], v[172:175], v[108:111]\n"
    "buffer_load_dwordx4 v[216:219], v253, s[20:23], s58 offen offset:2048 sc1\n"
    "s_waitcnt vmcnt(27)\n"
    "v_mfma_f32_16x16x32_bf16 v[112:115], v[220:223], v[160:163], v[112:115]\n"
    "ds_read_b128 v[156:159], v24 offset:35840\n"
    "v_mfma_f32_16x16x32_bf16 v[116:119], v[220:223], v[164:167], v[116:119]\n"
    "v_mfma_f32_16x16x32_bf16 v[120:123], v[220:223], v[168:171], v[120:123]\n"
    "v_mfma_f32_16x16x32_bf16 v[124:127], v[220:223], v[172:175], v[124:127]\n"
    "buffer_load_dwordx4 v[220:223], v254, s[20:23], s58 offen offset:2048 sc1\n"
    "s_waitcnt vmcnt(27) lgkmcnt(3)\n"
    "v_mfma_f32_16x16x32_bf16 v[64:67], v[28:31], v[144:147], v[64:67]\n"
    "ds_read_b128 v[160:163], v24 offset:36864\n"
    "s_waitcnt lgkmcnt(3)\n"
    "v_mfma_f32_16x16x32_bf16 v[68:71], v[28:31], v[148:151], v[68:71]\n"
    "s_waitcnt lgkmcnt(2)\n"
    "v_mfma_f32_16x16x32_bf16 v[72:75], v[28:31], v[152:155], v[72:75]\n"
    "s_waitcnt lgkmcnt(1)\n"
    "v_mfma_f32_16x16x32_bf16 v[76:79], v[28:31], v[156:159], v[76:79]\n"
    "buffer_load_dwordx4 v[28:31], v1, s[20:23], s58 offen offset:3072 sc1\n"
    "s_waitcnt vmcnt(27)\n"
    "v_mfma_f32_16x16x32_bf16 v[80:83], v[32:35], v[144:147], v[80:83]\n"
    "ds_read_b128 v[164:167], v24 offset:37888\n"
    "v_mfma_f32_16x16x32_bf16 v[84:87], v[32:35], v[148:151], v[84:87]\n"
    "v_mfma_f32_16x16x32_bf16 v[88:91], v[32:35], v[152:155], v[88:91]\n"
    "v_mfma_f32_16x16x32_bf16 v[92:95], v[32:35], v[156:159], v[92:95]\n"
    "buffer_load_dwordx4 v[32:35], v252, s[20:23], s58 offen offset:3072 sc1\n"
    "s_waitcnt vmcnt(27)\n"
    "v_mfma_f32_16x16x32_bf16 v[96:99], v[36:39], v[144:147], v[96:99]\n"
    "ds_read_b128 v[168:171], v24 offset:38912\n"
    "v_mfma_f32_16x16x32_bf16 v[100:103], v[36:39], v[148:151], v[100:103]\n"
    "v_mfma_f32_16x16x32_bf16 v[104:107], v[36:39], v[152:155], v[104:107]\n"
    "v_mfma_f32_16x16x32_bf16 v[108:111], v[36:39], v[156:159], v[108:111]\n"
    "buffer_load_dwordx4 v[36:39], v253, s[20:23], s58 offen offset:3072 sc1\n"
    "s_waitcnt vmcnt(27)\n"
    "v_mfma_f32_16x16x32_bf16 v[112:115], v[40:43], v[144:147], v[112:115]\n"
    "ds_read_b128 v[172:175], v24 offset:39936\n"
    "v_mfma_f32_16x16x32_bf16 v[116:119], v[40:43], v[148:151], v[116:119]\n"
    "v_mfma_f32_16x16x32_bf16 v[120:123], v[40:43], v[152:155], v[120:123]\n"
    "v_mfma_f32_16x16x32_bf16 v[124:127], v[40:43], v[156:159], v[124:127]\n"
    "buffer_load_dwordx4 v[40:43], v254, s[20:23], s58 offen offset:3072 sc1\n"
    "s_waitcnt vmcnt(27) lgkmcnt(3)\n"
    "v_mfma_f32_16x16x32_bf16 v[64:67], v[44:47], v[160:163], v[64:67]\n"
    "ds_read_b128 v[144:147], v24 offset:40960\n"
    "s_waitcnt lgkmcnt(3)\n"
    "v_mfma_f32_16x16x32_bf16 v[68:71], v[44:47], v[164:167], v[68:71]\n"
    "s_waitcnt lgkmcnt(2)\n"
    "v_mfma_f32_16x16x32_bf16 v[72:75], v[44:47], v[168:171], v[72:75]\n"
    "s_waitcnt lgkmcnt(1)\n"
    "v_mfma_f32_16x16x32_bf16 v[76:79], v[44:47], v[172:175], v[76:79]\n"
    "buffer_load_dwordx4 v[44:47], v1, s[20:23], s97 offen sc1\n"
    "s_waitcnt vmcnt(27)\n"
    "v_mfma_f32_16x16x32_bf16 v[80:83], v[48:51], v[160:163], v[80:83]\n"
    "ds_read_b128 v[148:151], v24 offset:41984\n"
    "v_mfma_f32_16x16x32_bf16 v[84:87], v[48:51], v[164:167], v[84:87]\n"
    "v_mfma_f32_16x16x32_bf16 v[88:91], v[48:51], v[168:171], v[88:91]\n"
    "v_mfma_f32_16x16x32_bf16 v[92:95], v[48:51], v[172:175], v[92:95]\n"
    "buffer_load_dwordx4 v[48:51], v1, s[20:23], s97 offen offset:1024 sc1\n"
    "s_waitcnt vmcnt(27)\n"
    "v_mfma_f32_16x16x32_bf16 v[96:99], v[52:55], v[160:163], v[96:99]\n"
    "ds_read_b128 v[152:155], v24 offset:43008\n"
    "v_mfma_f32_16x16x32_bf16 v[100:103], v[52:55], v[164:167], v[100:103]\n"
    "v_mfma_f32_16x16x32_bf16 v[104:107], v[52:55], v[168:171], v[104:107]\n"
    "v_mfma_f32_16x16x32_bf16 v[108:111], v[52:55], v[172:175], v[108:111]\n"
    "buffer_load_dwordx4 v[52:55], v1, s[20:23], s97 offen offset:2048 sc1\n"
    "s_waitcnt vmcnt(27)\n"
    "v_mfma_f32_16x16x32_bf16 v[112:115], v[56:59], v[160:163], v[112:115]\n"
    "ds_read_b128 v[156:159], v24 offset:44032\n"
    "v_mfma_f32_16x16x32_bf16 v[116:119], v[56:59], v[164:167], v[116:119]\n"
    "v_mfma_f32_16x16x32_bf16 v[120:123], v[56:59], v[168:171], v[120:123]\n"
    "v_mfma_f32_16x16x32_bf16 v[124:127], v[56:59], v[172:175], v[124:127]\n"
    "buffer_load_dwordx4 v[56:59], v1, s[20:23], s97 offen offset:3072 sc1\n"
    "s_waitcnt vmcnt(27) lgkmcnt(3)\n"
    "v_mfma_f32_16x16x32_bf16 v[64:67], v[60:63], v[144:147], v[64:67]\n"
    "ds_read_b128 v[160:163], v24 offset:45056\n"
    "s_waitcnt lgkmcnt(3)\n"
    "v_mfma_f32_16x16x32_bf16 v[68:71], v[60:63], v[148:151], v[68:71]\n"
    "s_waitcnt lgkmcnt(2)\n"
    "v_mfma_f32_16x16x32_bf16 v[72:75], v[60:63], v[152:155], v[72:75]\n"
    "s_waitcnt lgkmcnt(1)\n"
    "v_mfma_f32_16x16x32_bf16 v[76:79], v[60:63], v[156:159], v[76:79]\n"
    "s_add_u32 s97, s97, 4096\n"
    "buffer_load_dwordx4 v[60:63], v1, s[20:23], s97 offen sc1\n"
    "s_waitcnt vmcnt(27)\n"
    "v_mfma_f32_16x16x32_bf16 v[80:83], v[224:227], v[144:147], v[80:83]\n"
    "ds_read_b128 v[164:167], v24 offset:46080\n"
    "v_mfma_f32_16x16x32_bf16 v[84:87], v[224:227], v[148:151], v[84:87]\n"
    "v_mfma_f32_16x16x32_bf16 v[88:91], v[224:227], v[152:155], v[88:91]\n"
    "v_mfma_f32_16x16x32_bf16 v[92:95], v[224:227], v[156:159], v[92:95]\n"
    "buffer_load_dwordx4 v[224:227], v1, s[20:23], s97 offen offset:1024 sc1\n"
    "s_waitcnt vmcnt(27)\n"
    "v_mfma_f32_16x16x32_bf16 v[96:99], v[228:231], v[144:147], v[96:99]\n"
    "ds_read_b128 v[168:171], v24 offset:47104\n"
    "v_mfma_f32_16x16x32_bf16 v[100:103], v[228:231], v[148:151], v[100:103]\n"
    "v_mfma_f32_16x16x32_bf16 v[104:107], v[228:231], v[152:155], v[104:107]\n"
    "v_mfma_f32_16x16x32_bf16 v[108:111], v[228:231], v[156:159], v[108:111]\n"
    "buffer_load_dwordx4 v[228:231], v1, s[20:23], s97 offen offset:2048 sc1\n"
    "s_waitcnt vmcnt(27)\n"
    "v_mfma_f32_16x16x32_bf16 v[112:115], v[232:235], v[144:147], v[112:115]\n"
    "ds_read_b128 v[172:175], v24 offset:48128\n"
    "v_mfma_f32_16x16x32_bf16 v[116:119], v[232:235], v[148:151], v[116:119]\n"
    "v_mfma_f32_16x16x32_bf16 v[120:123], v[232:235], v[152:155], v[120:123]\n"
    "v_mfma_f32_16x16x32_bf16 v[124:127], v[232:235], v[156:159], v[124:127]\n"
    "buffer_load_dwordx4 v[232:235], v1, s[20:23], s97 offen offset:3072 sc1\n"
    "s_barrier\n"
    "s_waitcnt vmcnt(27) lgkmcnt(3)\n"
    "v_mfma_f32_16x16x32_bf16 v[64:67], v[236:239], v[160:163], v[64:67]\n"
    "ds_read_b128 v[144:147], v24 offset:49152\n"
    "s_waitcnt lgkmcnt(3)\n"
    "v_mfma_f32_16x16x32_bf16 v[68:71], v[236:239], v[164:167], v[68:71]\n"
    "s_waitcnt lgkmcnt(2)\n"
    "v_mfma_f32_16x16x32_bf16 v[72:75], v[236:239], v[168:171], v[72:75]\n"
    "s_waitcnt lgkmcnt(1)\n"
    "v_mfma_f32_16x16x32_bf16 v[76:79], v[236:239], v[172:175], v[76:79]\n"
    "s_add_u32 s97, s97, 4096\n"
    "buffer_load_dwordx4 v[236:239], v1, s[20:23], s97 offen sc1\n"
    "s_waitcnt vmcnt(27)\n"
    "v_mfma_f32_16x16x32_bf16 v[80:83], v[240:243], v[160:163], v[80:83]\n"
    "ds_read_b128 v[148:151], v24 offset:50176\n"
    "v_mfma_f32_16x16x32_bf16 v[84:87], v[240:243], v[164:167], v[84:87]\n"
    "v_mfma_f32_16x16x32_bf16 v[88:91], v[240:243], v[168:171], v[88:91]\n"
    "v_mfma_f32_16x16x32_bf16 v[92:95], v[240:243], v[172:175], v[92:95]\n"
    "buffer_load_dwordx4 v[240:243], v1, s[20:23], s97 offen offset:1024 sc1\n"
    "s_waitcnt vmcnt(27)\n"
    "v_mfma_f32_16x16x32_bf16 v[96:99], v[244:247], v[160:163], v[96:99]\n"
    "ds_read_b128 v[152:155], v24 offset:51200\n"
    "v_mfma_f32_16x16x32_bf16 v[100:103], v[244:247], v[164:167], v[100:103]\n"
    "v_mfma_f32_16x16x32_bf16 v[104:107], v[244:247], v[168:171], v[104:107]\n"
    "v_mfma_f32_16x16x32_bf16 v[108:111], v[244:247], v[172:175], v[108:111]\n"
    "buffer_load_dwordx4 v[244:247], v1, s[20:23], s97 offen offset:2048 sc1\n"
    "s_waitcnt vmcnt(27)\n"
    "v_mfma_f32_16x16x32_bf16 v[112:115], v[248:251], v[160:163], v[112:115]\n"
    "ds_read_b128 v[156:159], v24 offset:52224\n"
    "v_mfma_f32_16x16x32_bf16 v[116:119], v[248:251], v[164:167], v[116:119]\n"
    "v_mfma_f32_16x16x32_bf16 v[120:123], v[248:251], v[168:171], v[120:123]\n"
    "v_mfma_f32_16x16x32_bf16 v[124:127], v[248:251], v[172:175], v[124:127]\n"
    "buffer_load_dwordx4 v[248:251], v1, s[20:23], s97 offen offset:3072 sc1\n"
    "s_waitcnt vmcnt(27) lgkmcnt(3)\n"
    "v_mfma_f32_16x16x32_bf16 v[64:67], v[176:179], v[144:147], v[64:67]\n"
    "ds_read_b128 v[160:163], v24 offset:53248\n"
    "s_waitcnt lgkmcnt(3)\n"
    "v_mfma_f32_16x16x32_bf16 v[68:71], v[176:179], v[148:151], v[68:71]\n"
    "s_waitcnt lgkmcnt(2)\n"
    "v_mfma_f32_16x16x32_bf16 v[72:75], v[176:179], v[152:155], v[72:75]\n"
    "s_waitcnt lgkmcnt(1)\n"
    "v_mfma_f32_16x16x32_bf16 v[76:79], v[176:179], v[156:159], v[76:79]\n"
    "s_add_u32 s97, s97, 4096\n"
    "buffer_load_dwordx4 v[176:179], v1, s[20:23], s97 offen sc1\n"
    "s_waitcnt vmcnt(27)\n"
    "v_mfma_f32_16x16x32_bf16 v[80:83], v[180:183], v[144:147], v[80:83]\n"
    "ds_read_b128 v[164:167], v24 offset:54272\n"
    "v_mfma_f32_16x16x32_bf16 v[84:87], v[180:183], v[148:151], v[84:87]\n"
    "v_mfma_f32_16x16x32_bf16 v[88:91], v[180:183], v[152:155], v[88:91]\n"
    "v_mfma_f32_16x16x32_bf16 v[92:95], v[180:183], v[156:159], v[92:95]\n"
    "buffer_load_dwordx4 v[180:183], v1, s[20:23], s97 offen offset:1024 sc1\n"
    "s_waitcnt vmcnt(27)\n"
    "v_mfma_f32_16x16x32_bf16 v[96:99], v[184:187], v[144:147], v[96:99]\n"
    "ds_read_b128 v[168:171], v24 offset:55296\n"
    "v_mfma_f32_16x16x32_bf16 v[100:103], v[184:187], v[148:151], v[100:103]\n"
    "v_mfma_f32_16x16x32_bf16 v[104:107], v[184:187], v[152:155], v[104:107]\n"
    "v_mfma_f32_16x16x32_bf16 v[108:111], v[184:187], v[156:159], v[108:111]\n"
    "buffer_load_dwordx4 v[184:187], v1, s[20:23], s97 offen offset:2048 sc1\n"
    "s_waitcnt vmcnt(27)\n"
    "v_mfma_f32_16x16x32_bf16 v[112:115], v[188:191], v[144:147], v[112:115]\n"
    "ds_read_b128 v[172:175], v24 offset:56320\n"
    "v_mfma_f32_16x16x32_bf16 v[116:119], v[188:191], v[148:151], v[116:119]\n"
    "v_mfma_f32_16x16x32_bf16 v[120:123], v[188:191], v[152:155], v[120:123]\n"
    "v_mfma_f32_16x16x32_bf16 v[124:127], v[188:191], v[156:159], v[124:127]\n"
    "buffer_load_dwordx4 v[188:191], v1, s[20:23], s97 offen offset:3072 sc1\n"
    "s_waitcnt vmcnt(27) lgkmcnt(3)\n"
    "v_mfma_f32_16x16x32_bf16 v[64:67], v[192:195], v[160:163], v[64:67]\n"
    "ds_read_b128 v[144:147], v24 offset:57344\n"
    "s_waitcnt lgkmcnt(3)\n"
    "v_mfma_f32_16x16x32_bf16 v[68:71], v[192:195], v[164:167], v[68:71]\n"
    "s_waitcnt lgkmcnt(2)\n"
    "v_mfma_f32_16x16x32_bf16 v[72:75], v[192:195], v[168:171], v[72:75]\n"
    "s_waitcnt lgkmcnt(1)\n"
    "v_mfma_f32_16x16x32_bf16 v[76:79], v[192:195], v[172:175], v[76:79]\n"
    "s_add_u32 s97, s97, 4096\n"
    "buffer_load_dwordx4 v[192:195], v1, s[20:23], s97 offen sc1\n"
    "s_waitcnt vmcnt(27)\n"
    "v_mfma_f32_16x16x32_bf16 v[80:83], v[196:199], v[160:163], v[80:83]\n"
    "ds_read_b128 v[148:151], v24 offset:58368\n"
    "v_mfma_f32_16x16x32_bf16 v[84:87], v[196:199], v[164:167], v[84:87]\n"
    "v_mfma_f32_16x16x32_bf16 v[88:91], v[196:199], v[168:171], v[88:91]\n"
    "v_mfma_f32_16x16x32_bf16 v[92:95], v[196:199], v[172:175], v[92:95]\n"
    "buffer_load_dwordx4 v[196:199], v1, s[20:23], s97 offen offset:1024 sc1\n"
    "s_waitcnt vmcnt(27)\n"
    "v_mfma_f32_16x16x32_bf16 v[96:99], v[200:203], v[160:163], v[96:99]\n"
    "ds_read_b128 v[152:155], v24 offset:59392\n"
    "v_mfma_f32_16x16x32_bf16 v[100:103], v[200:203], v[164:167], v[100:103]\n"
    "v_mfma_f32_16x16x32_bf16 v[104:107], v[200:203], v[168:171], v[104:107]\n"
    "v_mfma_f32_16x16x32_bf16 v[108:111], v[200:203], v[172:175], v[108:111]\n"
    "s_waitcnt vmcnt(26)\n"
    "v_mfma_f32_16x16x32_bf16 v[112:115], v[204:207], v[160:163], v[112:115]\n"
    "ds_read_b128 v[156:159], v24 offset:60416\n"
    "v_mfma_f32_16x16x32_bf16 v[116:119], v[204:207], v[164:167], v[116:119]\n"
    "v_mfma_f32_16x16x32_bf16 v[120:123], v[204:207], v[168:171], v[120:123]\n"
    "v_mfma_f32_16x16x32_bf16 v[124:127], v[204:207], v[172:175], v[124:127]\n"
    "s_waitcnt vmcnt(25) lgkmcnt(3)\n"
    "v_mfma_f32_16x16x32_bf16 v[64:67], v[208:211], v[144:147], v[64:67]\n"
    "ds_read_b128 v[160:163], v24 offset:61440\n"
    "s_waitcnt lgkmcnt(3)\n"
    "v_mfma_f32_16x16x32_bf16 v[68:71], v[208:211], v[148:151], v[68:71]\n"
    "s_waitcnt lgkmcnt(2)\n"
    "v_mfma_f32_16x16x32_bf16 v[72:75], v[208:211], v[152:155], v[72:75]\n"
    "s_waitcnt lgkmcnt(1)\n"
    "v_mfma_f32_16x16x32_bf16 v[76:79], v[208:211], v[156:159], v[76:79]\n"
    "s_waitcnt vmcnt(24)\n"
    "v_mfma_f32_16x16x32_bf16 v[80:83], v[212:215], v[144:147], v[80:83]\n"
    "ds_read_b128 v[164:167], v24 offset:62464\n"
    "v_mfma_f32_16x16x32_bf16 v[84:87], v[212:215], v[148:151], v[84:87]\n"
    "v_mfma_f32_16x16x32_bf16 v[88:91], v[212:215], v[152:155], v[88:91]\n"
    "v_mfma_f32_16x16x32_bf16 v[92:95], v[212:215], v[156:159], v[92:95]\n"
    "s_waitcnt vmcnt(23)\n"
    "v_mfma_f32_16x16x32_bf16 v[96:99], v[216:219], v[144:147], v[96:99]\n"
    "ds_read_b128 v[168:171], v24 offset:63488\n"
    "v_mfma_f32_16x16x32_bf16 v[100:103], v[216:219], v[148:151], v[100:103]\n"
    "v_mfma_f32_16x16x32_bf16 v[104:107], v[216:219], v[152:155], v[104:107]\n"
    "v_mfma_f32_16x16x32_bf16 v[108:111], v[216:219], v[156:159], v[108:111]\n"
    "s_waitcnt vmcnt(22)\n"
    "v_mfma_f32_16x16x32_bf16 v[112:115], v[220:223], v[144:147], v[112:115]\n"
    "ds_read_b128 v[172:175], v24 offset:64512\n"
    "v_mfma_f32_16x16x32_bf16 v[116:119], v[220:223], v[148:151], v[116:119]\n"
    "v_mfma_f32_16x16x32_bf16 v[120:123], v[220:223], v[152:155], v[120:123]\n"
    "v_mfma_f32_16x16x32_bf16 v[124:127], v[220:223], v[156:159], v[124:127]\n"
    "s_waitcnt vmcnt(21) lgkmcnt(3)\n"
    "v_mfma_f32_16x16x32_bf16 v[64:67], v[28:31], v[160:163], v[64:67]\n"
    "ds_read_b128 v[144:147], v24\n"
    "s_waitcnt lgkmcnt(3)\n"
    "v_mfma_f32_16x16x32_bf16 v[68:71], v[28:31], v[164:167], v[68:71]\n"
    "s_waitcnt lgkmcnt(2)\n"
    "v_mfma_f32_16x16x32_bf16 v[72:75], v[28:31], v[168:171], v[72:75]\n"
    "s_waitcnt lgkmcnt(1)\n"
    "v_mfma_f32_16x16x32_bf16 v[76:79], v[28:31], v[172:175], v[76:79]\n"
    "s_waitcnt vmcnt(20)\n"
    "v_mfma_f32_16x16x32_bf16 v[80:83], v[32:35], v[160:163], v[80:83]\n"
    "ds_read_b128 v[148:151], v24 offset:1024\n"
    "v_mfma_f32_16x16x32_bf16 v[84:87], v[32:35], v[164:167], v[84:87]\n"
    "v_mfma_f32_16x16x32_bf16 v[88:91], v[32:35], v[168:171], v[88:91]\n"
    "v_mfma_f32_16x16x32_bf16 v[92:95], v[32:35], v[172:175], v[92:95]\n"
    "s_waitcnt vmcnt(19)\n"
    "v_mfma_f32_16x16x32_bf16 v[96:99], v[36:39], v[160:163], v[96:99]\n"
    "ds_read_b128 v[152:155], v24 offset:2048\n"
    "v_mfma_f32_16x16x32_bf16 v[100:103], v[36:39], v[164:167], v[100:103]\n"
    "v_mfma_f32_16x16x32_bf16 v[104:107], v[36:39], v[168:171], v[104:107]\n"
    "v_mfma_f32_16x16x32_bf16 v[108:111], v[36:39], v[172:175], v[108:111]\n"
    "s_waitcnt vmcnt(18)\n"
    "v_mfma_f32_16x16x32_bf16 v[112:115], v[40:43], v[160:163], v[112:115]\n"
    "ds_read_b128 v[156:159], v24 offset:3072\n"
    "v_mfma_f32_16x16x32_bf16 v[116:119], v[40:43], v[164:167], v[116:119]\n"
    "v_mfma_f32_16x16x32_bf16 v[120:123], v[40:43], v[168:171], v[120:123]\n"
    "v_mfma_f32_16x16x32_bf16 v[124:127], v[40:43], v[172:175], v[124:127]\n"
    "ds_read_b128 v[220:223], v24 offset:4096\n"
    "ds_read_b128 v[216:219], v24 offset:5120\n"
    "ds_read_b128 v[212:215], v24 offset:6144\n"
    "ds_read_b128 v[208:211], v24 offset:7168\n"
    "ds_read_b128 v[204:207], v24 offset:8192\n"
    "ds_read_b128 v[200:203], v24 offset:9216\n"
    "ds_read_b128 v[14:17], v24 offset:10240\n"
    "ds_read_b128 v[18:21], v24 offset:11264\n"
    "s_waitcnt vmcnt(17) lgkmcnt(11)\n"
    "v_mfma_f32_16x16x32_bf16 v[128:131], v[44:47], v[144:147], 0\n"
    "ds_read_b128 v[160:163], v24 offset:12288\n"
    "s_waitcnt lgkmcnt(11)\n"
    "v_mfma_f32_16x16x32_bf16 v[132:135], v[44:47], v[148:151], 0\n"
    "ds_read_b128 v[164:167], v24 offset:13312\n"
    "v_cvt_pk_bf16_f32 v64, v64, v65\n"
    "v_cvt_pk_bf16_f32 v65, v66, v67\n"
    "v_cvt_pk_bf16_f32 v66, v80, v81\n"
    "v_cvt_pk_bf16_f32 v67, v82, v83\n"
    "s_nop 0\n"
    "v_permlane16_swap_b32 v64, v66\n"
    "v_permlane16_swap_b32 v65, v67\n"
    "buffer_store_dwordx4 v[64:67], v3, s[20:23], s51 offen sc1\n"
    "s_waitcnt lgkmcnt(11)\n"
    "v_mfma_f32_16x16x32_bf16 v[136:139], v[44:47], v[152:155], 0\n"
    "ds_read_b128 v[168:171], v24 offset:14336\n"
    "s_waitcnt lgkmcnt(11)\n"
    "v_mfma_f32_16x16x32_bf16 v[140:143], v[44:47], v[156:159], 0\n"
    "ds_read_b128 v[172:175], v24 offset:15360\n"
    "v_cvt_pk_bf16_f32 v96, v96, v97\n"
    "v_cvt_pk_bf16_f32 v97, v98, v99\n"
    "v_cvt_pk_bf16_f32 v98, v112, v113\n"
    "v_cvt_pk_bf16_f32 v99, v114, v115\n"
    "s_nop 0\n"
    "v_permlane16_swap_b32 v96, v98\n"
    "v_permlane16_swap_b32 v97, v99\n"
    "buffer_store_dwordx4 v[96:99], v3, s[20:23], s52 offen sc1\n"
    "s_waitcnt vmcnt(18) lgkmcnt(11)\n"
    "v_mfma_f32_16x16x32_bf16 v[128:131], v[48:51], v[220:223], v[128:131]\n"
    "ds_read_b128 v[144:147], v24 offset:16384\n"
    "s_waitcnt lgkmcnt(11)\n"
    "v_mfma_f32_16x16x32_bf16 v[132:135], v[48:51], v[216:219], v[132:135]\n"
    "ds_read_b128 v[148:151], v24 offset:17408\n"
    "s_waitcnt lgkmcnt(11)\n"
    "v_mfma_f32_16x16x32_bf16 v[136:139], v[48:51], v[212:215], v[136:139]\n"
    "ds_read_b128 v[152:155], v24 offset:18432\n"
    "v_cvt_pk_bf16_f32 v68, v68, v69\n"
    "v_cvt_pk_bf16_f32 v69, v70, v71\n"
    "v_cvt_pk_bf16_f32 v70, v84, v85\n"
    "v_cvt_pk_bf16_f32 v71, v86, v87\n"
    "s_nop 0\n"
    "v_permlane16_swap_b32 v68, v70\n"
    "v_permlane16_swap_b32 v69, v71\n"
    "buffer_store_dwordx4 v[68:71], v3, s[20:23], s51 offen offset:2304 sc1\n"
    "s_waitcnt lgkmcnt(11)\n"
    "v_mfma_f32_16x16x32_bf16 v[140:143], v[48:51], v[208:211], v[140:143]\n"
    "ds_read_b128 v[156:159], v24 offset:19456\n"
    "s_waitcnt vmcnt(18) lgkmcnt(11)\n"
    "v_mfma_f32_16x16x32_bf16 v[128:131], v[52:55], v[204:207], v[128:131]\n"
    "ds_read_b128 v[220:223], v24 offset:20480\n"
    "v_cvt_pk_bf16_f32 v100, v100, v101\n"
    "v_cvt_pk_bf16_f32 v101, v102, v103\n"
    "v_cvt_pk_bf16_f32 v102, v116, v117\n"
    "v_cvt_pk_bf16_f32 v103, v118, v119\n"
    "s_nop 0\n"
    "v_permlane16_swap_b32 v100, v102\n"
    "v_permlane16_swap_b32 v101, v103\n"
    "buffer_store_dwordx4 v[100:103], v3, s[20:23], s52 offen offset:2304 sc1\n"
    "s_waitcnt lgkmcnt(11)\n"
    "v_mfma_f32_16x16x32_bf16 v[132:135], v[52:55], v[200:203], v[132:135]\n"
    "ds_read_b128 v[216:219], v24 offset:21504\n"
    "s_waitcnt lgkmcnt(11)\n"
    "v_mfma_f32_16x16x32_bf16 v[136:139], v[52:55], v[14:17], v[136:139]\n"
    "ds_read_b128 v[212:215], v24 offset:22528\n"
    "s_waitcnt lgkmcnt(11)\n"
    "v_mfma_f32_16x16x32_bf16 v[140:143], v[52:55], v[18:21], v[140:143]\n"
    "ds_read_b128 v[208:211], v24 offset:23552\n"
    "s_add_u32 s51, s51, 4608\n"
    "s_add_u32 s52, s52, 4608\n"
    "s_waitcnt vmcnt(18) lgkmcnt(11)\n"
    "v_mfma_f32_16x16x32_bf16 v[128:131], v[56:59], v[160:163], v[128:131]\n"
    "ds_read_b128 v[204:207], v24 offset:24576\n"
    "s_waitcnt lgkmcnt(11)\n"
    "v_mfma_f32_16x16x32_bf16 v[132:135], v[56:59], v[164:167], v[132:135]\n"
    "ds_read_b128 v[200:203], v24 offset:25600\n"
    "v_cvt_pk_bf16_f32 v72, v72, v73\n"
    "v_cvt_pk_bf16_f32 v73, v74, v75\n"
    "v_cvt_pk_bf16_f32 v74, v88, v89\n"
    "v_cvt_pk_bf16_f32 v75, v90, v91\n"
    "s_nop 0\n"
    "v_permlane16_swap_b32 v72, v74\n"
    "v_permlane16_swap_b32 v73, v75\n"
    "buffer_store_dwordx4 v[72:75], v3, s[20:23], s51 offen sc1\n"
    "s_waitcnt lgkmcnt(11)\n"
    "v_mfma_f32_16x16x32_bf16 v[136:139], v[56:59], v[168:171], v[136:139]\n"
    "ds_read_b128 v[14:17], v24 offset:26624\n"
    "s_waitcnt lgkmcnt(11)\n"
    "v_mfma_f32_16x16x32_bf16 v[140:143], v[56:59], v[172:175], v[140:143]\n"
    "ds_read_b128 v[18:21], v24 offset:27648\n"
    "s_waitcnt vmcnt(18) lgkmcnt(11)\n"
    "v_mfma_f32_16x16x32_bf16 v[128:131], v[60:63], v[144:147], v[128:131]\n"
    "ds_read_b128 v[160:163], v24 offset:28672\n"
    "v_cvt_pk_bf16_f32 v104, v104, v105\n"
    "v_cvt_pk_bf16_f32 v105, v106, v107\n"
    "v_cvt_pk_bf16_f32 v106, v120, v121\n"
    "v_cvt_pk_bf16_f32 v107, v122, v123\n"
    "s_nop 0\n"
    "v_permlane16_swap_b32 v104, v106\n"
    "v_permlane16_swap_b32 v105, v107\n"
    "buffer_store_dwordx4 v[104:107], v3, s[20:23], s52 offen sc1\n"
    "s_waitcnt lgkmcnt(11)\n"
    "v_mfma_f32_16x16x32_bf16 v[132:135], v[60:63], v[148:151], v[132:135]\n"
    "ds_read_b128 v[164:167], v24 offset:29696\n"
    "s_waitcnt lgkmcnt(11)\n"
    "v_mfma_f32_16x16x32_bf16 v[136:139], v[60:63], v[152:155], v[136:139]\n"
    "ds_read_b128 v[168:171], v24 offset:30720\n"
    "v_cvt_pk_bf16_f32 v76, v76, v77\n"
    "v_cvt_pk_bf16_f32 v77, v78, v79\n"
    "v_cvt_pk_bf16_f32 v78, v92, v93\n"
    "v_cvt_pk_bf16_f32 v79, v94, v95\n"
    "s_nop 0\n"
    "v_permlane16_swap_b32 v76, v78\n"
    "v_permlane16_swap_b32 v77, v79\n"
    "buffer_store_dwordx4 v[76:79], v3, s[20:23], s51 offen offset:2304 sc1\n"
    "s_waitcnt lgkmcnt(11)\n"
    "v_mfma_f32_16x16x32_bf16 v[140:143], v[60:63], v[156:159], v[140:143]\n"
    "ds_read_b128 v[172:175], v24 offset:31744\n"
    "s_waitcnt vmcnt(19) lgkmcnt(11)\n"
    "v_mfma_f32_16x16x32_bf16 v[128:131], v[224:227], v[220:223], v[128:131]\n"
    "ds_read_b128 v[144:147], v24 offset:32768\n"
    "s_waitcnt lgkmcnt(11)\n"
    "v_mfma_f32_16x16x32_bf16 v[132:135], v[224:227], v[216:219], v[132:135]\n"
    "ds_read_b128 v[148:151], v24 offset:33792\n"
    "v_cvt_pk_bf16_f32 v108, v108, v109\n"
    "v_cvt_pk_bf16_f32 v109, v110, v111\n"
    "v_cvt_pk_bf16_f32 v110, v124, v125\n"
    "v_cvt_pk_bf16_f32 v111, v126, v127\n"
    "s_nop 0\n"
    "v_permlane16_swap_b32 v108, v110\n"
    "v_permlane16_swap_b32 v109, v111\n"
    "buffer_store_dwordx4 v[108:111], v3, s[20:23], s52 offen offset:2304 sc1\n"
    "s_waitcnt lgkmcnt(11)\n"
    "v_mfma_f32_16x16x32_bf16 v[136:139], v[224:227], v[212:215], v[136:139]\n"
    "ds_read_b128 v[152:155], v24 offset:34816\n"
    "s_waitcnt lgkmcnt(11)\n"
    "v_mfma_f32_16x16x32_bf16 v[140:143], v[224:227], v[208:211], v[140:143]\n"
    "ds_read_b128 v[156:159], v24 offset:35840\n"
    "s_waitcnt vmcnt(19) lgkmcnt(11)\n"
    "v_mfma_f32_16x16x32_bf16 v[128:131], v[228:231], v[204:207], v[128:131]\n"
    "ds_read_b128 v[220:223], v24 offset:36864\n"
    "s_waitcnt lgkmcnt(11)\n"
    "v_mfma_f32_16x16x32_bf16 v[132:135], v[228:231], v[200:203], v[132:135]\n"
    "ds_read_b128 v[216:219], v24 offset:37888\n"
    "s_waitcnt lgkmcnt(11)\n"
    "v_mfma_f32_16x16x32_bf16 v[136:139], v[228:231], v[14:17], v[136:139]\n"
    "ds_read_b128 v[212:215], v24 offset:38912\n"
    "s_waitcnt lgkmcnt(11)\n"
    "v_mfma_f32_16x16x32_bf16 v[140:143], v[228:231], v[18:21], v[140:143]\n"
    "ds_read_b128 v[208:211], v24 offset:39936\n"
    "v_and_b32 v88, 15, v2\n"
    "v_lshrrev_b32 v89, 4, v2\n"
    "v_lshlrev_b32 v90, 4, v88\n"
    "s_mul_i32 s44, s36, 1024\n"
    "s_add_u32 s44, s44, 147456\n"
    "v_add_u32 v91, s44, v1\n"
    "v_lshlrev_b32 v92, 5, v89\n"
    "v_add_u32 v92, 162208, v92\n"
    "ds_read_b128 v[122:125], v91\n"
    "ds_read_b128 v[112:115], v92\n"
    "ds_read_b128 v[116:119], v92 offset:16\n"
    "s_lshl_b32 s44, s39, 10\n"
    "v_add_u32 v80, s44, v1\n"
    "s_mul_i32 s44, s39, 4096\n"
    "s_add_u32 s44, s44, 2048\n"
    "v_lshl_add_u32 v81, v89, 9, v90\n"
    "v_add_u32 v81, s44, v81\n"
    "v_mul_u32_u24 v88, 1152, v88\n"
    "v_lshl_add_u32 v82, v89, 4, v88\n"
    "s_mul_i32 s44, s39, 2048\n"
    "s_add_u32 s44, s44, 16384\n"
    "v_add_u32 v83, s44, v1\n"
    "v_mov_b32 v85, 0x3f80\n"
    "v_mov_b32 v86, 0x3f800000\n"
    "s_lshl_b32 s44, s38, 2\n"
    "s_add_u32 s44, s44, s39\n"
    "s_mul_i32 s44, s44, 18432\n"
    "s_add_u32 s44, s44, 64\n"
    "s_mul_i32 s46, s37, 144\n"
    "s_add_u32 s46, s46, s44\n"
    "s_lshl_b32 s45, s38, 3\n"
    "s_add_u32 s45, s45, s37\n"
    "s_mul_i32 s45, s45, 73728\n"
    "s_add_u32 s45, s45, 27472896\n"
    "s_mul_i32 s44, s39, 2304\n"
    "s_add_u32 s44, s44, s45\n"
    "s_mov_b32 s45, s44\n"
    "s_waitcnt lgkmcnt(2)\n"
    "v_lshlrev_b32 v96, 16, v122\n"
    "v_and_b32 v97, 0xffff0000, v122\n"
    "v_lshlrev_b32 v98, 16, v123\n"
    "v_and_b32 v99, 0xffff0000, v123\n"
    "v_lshlrev_b32 v100, 16, v124\n"
    "v_and_b32 v101, 0xffff0000, v124\n"
    "v_lshlrev_b32 v102, 16, v125\n"
    "v_and_b32 v103, 0xffff0000, v125\n"
    "s_waitcnt vmcnt(18)\n"
    "v_mfma_f32_16x16x32_bf16 v[128:131], v[232:235], v[160:163], v[128:131]\n"
    "ds_read_b128 v[204:207], v24 offset:40960\n"
    "v_mfma_f32_16x16x32_bf16 v[132:135], v[232:235], v[164:167], v[132:135]\n"
    "ds_read_b128 v[200:203], v24 offset:41984\n"
    "v_mfma_f32_16x16x32_bf16 v[136:139], v[232:235], v[168:171], v[136:139]\n"
    "ds_read_b128 v[14:17], v24 offset:43008\n"
    "v_mfma_f32_16x16x32_bf16 v[140:143], v[232:235], v[172:175], v[140:143]\n"
    "ds_read_b128 v[18:21], v24 offset:44032\n"
    "s_waitcnt vmcnt(17)\n"
    "v_mfma_f32_16x16x32_bf16 v[128:131], v[236:239], v[144:147], v[128:131]\n"
    "ds_read_b128 v[160:163], v24 offset:45056\n"
    "v_mfma_f32_16x16x32_bf16 v[132:135], v[236:239], v[148:151], v[132:135]\n"
    "ds_read_b128 v[164:167], v24 offset:46080\n"
    "v_mfma_f32_16x16x32_bf16 v[136:139], v[236:239], v[152:155], v[136:139]\n"
    "ds_read_b128 v[168:171], v24 offset:47104\n"
    "v_mfma_f32_16x16x32_bf16 v[140:143], v[236:239], v[156:159], v[140:143]\n"
    "ds_read_b128 v[172:175], v24 offset:48128\n"
    "s_waitcnt vmcnt(16)\n"
    "v_mfma_f32_16x16x32_bf16 v[128:131], v[240:243], v[220:223], v[128:131]\n"
    "ds_read_b128 v[144:147], v24 offset:49152\n"
    "v_mfma_f32_16x16x32_bf16 v[132:135], v[240:243], v[216:219], v[132:135]\n"
    "ds_read_b128 v[148:151], v24 offset:50176\n"
    "v_mfma_f32_16x16x32_bf16 v[136:139], v[240:243], v[212:215], v[136:139]\n"
    "ds_read_b128 v[152:155], v24 offset:51200\n"
    "v_mfma_f32_16x16x32_bf16 v[140:143], v[240:243], v[208:211], v[140:143]\n"
    "ds_read_b128 v[156:159], v24 offset:52224\n"
    "s_waitcnt vmcnt(0)\n"
    "buffer_store_dword v9, v5, s[20:23], s98 offen sc1\n"
    "s_waitcnt lgkmcnt(11)\n"
    "v_mfma_f32_16x16x32_bf16 v[128:131], v[244:247], v[204:207], v[128:131]\n"
    "ds_read_b128 v[220:223], v24 offset:53248\n"
    "s_waitcnt lgkmcnt(11)\n"
    "v_mfma_f32_16x16x32_bf16 v[132:135], v[244:247], v[200:203], v[132:135]\n"
    "ds_read_b128 v[216:219], v24 offset:54272\n"
    "s_waitcnt lgkmcnt(11)\n"
    "v_mfma_f32_16x16x32_bf16 v[136:139], v[244:247], v[14:17], v[136:139]\n"
    "ds_read_b128 v[212:215], v24 offset:55296\n"
    "s_waitcnt lgkmcnt(11)\n"
    "v_mfma_f32_16x16x32_bf16 v[140:143], v[244:247], v[18:21], v[140:143]\n"
    "ds_read_b128 v[208:211], v24 offset:56320\n"
    "s_waitcnt lgkmcnt(11)\n"
    "v_mfma_f32_16x16x32_bf16 v[128:131], v[248:251], v[160:163], v[128:131]\n"
    "ds_read_b128 v[204:207], v24 offset:57344\n"
    "s_waitcnt lgkmcnt(11)\n"
    "v_mfma_f32_16x16x32_bf16 v[132:135], v[248:251], v[164:167], v[132:135]\n"
    "ds_read_b128 v[200:203], v24 offset:58368\n"
    "s_waitcnt lgkmcnt(11)\n"
    "v_mfma_f32_16x16x32_bf16 v[136:139], v[248:251], v[168:171], v[136:139]\n"
    "ds_read_b128 v[14:17], v24 offset:59392\n"
    "s_waitcnt lgkmcnt(11)\n"
    "v_mfma_f32_16x16x32_bf16 v[140:143], v[248:251], v[172:175], v[140:143]\n"
    "ds_read_b128 v[18:21], v24 offset:60416\n"
    "s_waitcnt lgkmcnt(11)\n"
    "v_mfma_f32_16x16x32_bf16 v[128:131], v[176:179], v[144:147], v[128:131]\n"
    "ds_read_b128 v[160:163], v24 offset:61440\n"
    "s_waitcnt lgkmcnt(11)\n"
    "v_mfma_f32_16x16x32_bf16 v[132:135], v[176:179], v[148:151], v[132:135]\n"
    "ds_read_b128 v[164:167], v24 offset:62464\n"
    "s_waitcnt lgkmcnt(11)\n"
    "v_mfma_f32_16x16x32_bf16 v[136:139], v[176:179], v[152:155], v[136:139]\n"
    "ds_read_b128 v[168:171], v24 offset:63488\n"
    "s_waitcnt lgkmcnt(11)\n"
    "v_mfma_f32_16x16x32_bf16 v[140:143], v[176:179], v[156:159], v[140:143]\n"
    "ds_read_b128 v[172:175], v24 offset:64512\n"
    "s_waitcnt lgkmcnt(11)\n"
    "v_mfma_f32_16x16x32_bf16 v[128:131], v[180:183], v[220:223], v[128:131]\n"
    "ds_read_b128 v[144:147], v25\n"
    "s_waitcnt lgkmcnt(11)\n"
    "v_mfma_f32_16x16x32_bf16 v[132:135], v[180:183], v[216:219], v[132:135]\n"
    "ds_read_b128 v[148:151], v25 offset:1024\n"
    "s_waitcnt lgkmcnt(11)\n"
    "v_mfma_f32_16x16x32_bf16 v[136:139], v[180:183], v[212:215], v[136:139]\n"
    "ds_read_b128 v[152:155], v25 offset:2048\n"
    "s_waitcnt lgkmcnt(11)\n"
    "v_mfma_f32_16x16x32_bf16 v[140:143], v[180:183], v[208:211], v[140:143]\n"
    "ds_read_b128 v[156:159], v25 offset:3072\n"
    "s_waitcnt lgkmcnt(11)\n"
    "v_mfma_f32_16x16x32_bf16 v[128:131], v[184:187], v[204:207], v[128:131]\n"
    "ds_read_b128 v[220:223], v25 offset:4096\n"
    "s_waitcnt lgkmcnt(11)\n"
    "v_mfma_f32_16x16x32_bf16 v[132:135], v[184:187], v[200:203], v[132:135]\n"
    "ds_read_b128 v[216:219], v25 offset:5120\n"
    "s_waitcnt lgkmcnt(11)\n"
    "v_mfma_f32_16x16x32_bf16 v[136:139], v[184:187], v[14:17], v[136:139]\n"
    "ds_read_b128 v[212:215], v25 offset:6144\n"
    "s_waitcnt lgkmcnt(11)\n"
    "v_mfma_f32_16x16x32_bf16 v[140:143], v[184:187], v[18:21], v[140:143]\n"
    "ds_read_b128 v[208:211], v25 offset:7168\n"
    "s_waitcnt lgkmcnt(11)\n"
    "v_mfma_f32_16x16x32_bf16 v[128:131], v[188:191], v[160:163], v[128:131]\n"
    "s_waitcnt lgkmcnt(10)\n"
    "v_mfma_f32_16x16x32_bf16 v[132:135], v[188:191], v[164:167], v[132:135]\n"
    "s_waitcnt lgkmcnt(9)\n"
    "v_mfma_f32_16x16x32_bf16 v[136:139], v[188:191], v[168:171], v[136:139]\n"
    "s_waitcnt lgkmcnt(8)\n"
    "v_mfma_f32_16x16x32_bf16 v[140:143], v[188:191], v[172:175], v[140:143]\n"
    "s_waitcnt lgkmcnt(7)\n"
    "v_mfma_f32_16x16x32_bf16 v[128:131], v[192:195], v[144:147], v[128:131]\n"
    "s_waitcnt lgkmcnt(6)\n"
    "v_mfma_f32_16x16x32_bf16 v[132:135], v[192:195], v[148:151], v[132:135]\n"
    "s_waitcnt lgkmcnt(5)\n"
    "v_mfma_f32_16x16x32_bf16 v[136:139], v[192:195], v[152:155], v[136:139]\n"
    "s_waitcnt lgkmcnt(4)\n"
    "v_mfma_f32_16x16x32_bf16 v[140:143], v[192:195], v[156:159], v[140:143]\n"
    "s_waitcnt lgkmcnt(3)\n"
    "v_mfma_f32_16x16x32_bf16 v[128:131], v[196:199], v[220:223], v[128:131]\n"
    "s_waitcnt lgkmcnt(2)\n"
    "v_mfma_f32_16x16x32_bf16 v[132:135], v[196:199], v[216:219], v[132:135]\n"
    "s_waitcnt lgkmcnt(1)\n"
    "v_mfma_f32_16x16x32_bf16 v[136:139], v[196:199], v[212:215], v[136:139]\n"
    "s_waitcnt lgkmcnt(0)\n"
    "v_mfma_f32_16x16x32_bf16 v[140:143], v[196:199], v[208:211], v[140:143]\n"
    "s_nop 1\n"
    "ds_write_b128 v80, v[128:131]\n"
    "s_nop 0\n"
    "ds_write_b128 v80, v[132:135] offset:4096\n"
    "s_nop 0\n"
    "ds_write_b128 v80, v[136:139] offset:8192\n"
    "s_nop 0\n"
    "ds_write_b128 v80, v[140:143] offset:12288\n"
    "s_cmp_lg_u32 s36, 0\n"
    "s_cbranch_scc1 .Lk_polld_84\n"
    "s_mov_b32 s60, 0x8000\n"
    ".Lk_poll_83:\n"
    "s_waitcnt vmcnt(0)\n"
    "v_cmp_ne_u32 vcc, s95, v8\n"
    "s_cbranch_vccz .Lk_polld_84\n"
    "s_sub_u32 s60, s60, 1\n"
    "s_cbranch_scc1 .Lk_polld_84\n"
    "s_sleep 1\n"
    "buffer_load_dword v8, v10, s[20:23], s99 offen sc1\n"
    "s_branch .Lk_poll_83\n"
    ".Lk_polld_84:\n"
    "s_waitcnt lgkmcnt(0)\n"
    "s_barrier\n"
    "s_add_u32 s44, s37, 1\n"
    "s_and_b32 s44, s44, 7\n"
    "s_mul_i32 s44, s44, 9216\n"
    "s_add_u32 s44, s44, s45\n"
    "buffer_load_dwordx4 v[28:31], v1, s[20:23], s44 offen sc1\n"
    "s_add_u32 s44, s37, 2\n"
    "s_and_b32 s44, s44, 7\n"
    "s_mul_i32 s44, s44, 9216\n"
    "s_add_u32 s44, s44, s45\n"
    "buffer_load_dwordx4 v[32:35], v1, s[20:23], s44 offen sc1\n"
    "s_add_u32 s44, s37, 3\n"
    "s_and_b32 s44, s44, 7\n"
    "s_mul_i32 s44, s44, 9216\n"
    "s_add_u32 s44, s44, s45\n"
    "buffer_load_dwordx4 v[36:39], v1, s[20:23], s44 offen sc1\n"
    "ds_read_b128 v[64:67], v81\n"
    "ds_read_b128 v[68:71], v81 offset:256\n"
    "ds_read_b128 v[128:131], v83\n"
    "ds_read_b128 v[132:135], v83 offset:1024\n"
    "s_waitcnt lgkmcnt(1)\n"
    "v_pk_add_f32 v[64:65], v[64:65], v[128:129]\n"
    "v_pk_add_f32 v[66:67], v[66:67], v[130:131]\n"
    "s_waitcnt lgkmcnt(0)\n"
    "v_pk_add_f32 v[68:69], v[68:69], v[132:133]\n"
    "v_pk_add_f32 v[70:71], v[70:71], v[134:135]\n"
    "s_waitcnt vmcnt(2)\n"
    "v_dot2_f32_bf16 v64, v28, v85, v64\n"
    "v_dot2_f32_bf16 v65, v28, v86, v65\n"
    "v_dot2_f32_bf16 v66, v29, v85, v66\n"
    "v_dot2_f32_bf16 v67, v29, v86, v67\n"
    "v_dot2_f32_bf16 v68, v30, v85, v68\n"
    "v_dot2_f32_bf16 v69, v30, v86, v69\n"
    "v_dot2_f32_bf16 v70, v31, v85, v70\n"
    "v_dot2_f32_bf16 v71, v31, v86, v71\n"
    "s_waitcnt vmcnt(1)\n"
    "v_dot2_f32_bf16 v64, v32, v85, v64\n"
    "v_dot2_f32_bf16 v65, v32, v86, v65\n"
    "v_dot2_f32_bf16 v66, v33, v85, v66\n"
    "v_dot2_f32_bf16 v67, v33, v86, v67\n"
    "v_dot2_f32_bf16 v68, v34, v85, v68\n"
    "v_dot2_f32_bf16 v69, v34, v86, v69\n"
    "v_dot2_f32_bf16 v70, v35, v85, v70\n"
    "v_dot2_f32_bf16 v71, v35, v86, v71\n"
    "s_waitcnt vmcnt(0)\n"
    "v_dot2_f32_bf16 v64, v36, v85, v64\n"
    "v_dot2_f32_bf16 v65, v36, v86, v65\n"
    "v_dot2_f32_bf16 v66, v37, v85, v66\n"
    "v_dot2_f32_bf16 v67, v37, v86, v67\n"
    "v_dot2_f32_bf16 v68, v38, v85, v68\n"
    "v_dot2_f32_bf16 v69, v38, v86, v69\n"
    "v_dot2_f32_bf16 v70, v39, v85, v70\n"
    "v_dot2_f32_bf16 v71, v39, v86, v71\n"
    "v_pk_fma_f32 v[64:65], v[64:65], v[112:113], v[96:97]\n"
    "v_pk_fma_f32 v[66:67], v[66:67], v[114:115], v[98:99]\n"
    "v_pk_fma_f32 v[68:69], v[68:69], v[116:117], v[100:101]\n"
    "v_pk_fma_f32 v[70:71], v[70:71], v[118:119], v[102:103]\n"
    "v_cvt_pk_bf16_f32 v64, v64, v65\n"
    "v_cvt_pk_bf16_f32 v65, v66, v67\n"
    "v_cvt_pk_bf16_f32 v66, v68, v69\n"
    "v_cvt_pk_bf16_f32 v67, v70, v71\n"
    "buffer_store_dwordx4 v[64:67], v82, s[32:35], s46 offen\n"
    ".Lk_final_71:\n"
    "s_or_b32 s41, %[bid], s36\n"
    "s_cbranch_scc1 .Lk_ep_85\n"
    "s_mov_b32 s41, 253952\n"
    "v_mov_b32 v8, s95\n"
    "v_mov_b32 v9, 0\n"
    "s_mov_b64 exec, 1\n"
    "buffer_store_dword v8, v9, s[20:23], s41 offen sc1\n"
    "s_mov_b64 exec, -1\n"
    ".Lk_ep_85:\n"
    "s_branch .Lk_end_66\n"
    ".Lk_fb_64:\n"
    "s_waitcnt vmcnt(0) lgkmcnt(0)\n"
    "s_barrier\n"
    "v_mov_b32 v185, 0\n"
    "v_mov_b32 v20, 0xff800000\n"
    "v_mov_b32 v21, 0xff800000\n"
    "v_mov_b32 v15, 0\n"
    "v_mov_b32 v16, 161280\n"
    "ds_write_b32 v16, v15\n"
    "ds_write_b32 v16, v15 offset:4\n"
    "ds_write_b32 v16, v15 offset:8\n"
    "ds_write_b32 v16, v15 offset:12\n"
    "ds_write_b32 v16, v15 offset:16\n"
    "ds_write_b32 v16, v15 offset:20\n"
    "ds_write_b32 v16, v15 offset:24\n"
    "ds_write_b32 v16, v15 offset:28\n"
    "v_mov_b32 v128, 0\n"
    "v_mov_b32 v129, 0\n"
    "v_mov_b32 v130, 0\n"
    "v_mov_b32 v131, 0\n"
    "v_mov_b32 v132, 0\n"
    "v_mov_b32 v133, 0\n"
    "v_mov_b32 v134, 0\n"
    "v_mov_b32 v135, 0\n"
    "v_mov_b32 v136, 0\n"
    "v_mov_b32 v137, 0\n"
    "v_mov_b32 v138, 0\n"
    "v_mov_b32 v139, 0\n"
    "v_mov_b32 v140, 0\n"
    "v_mov_b32 v141, 0\n"
    "v_mov_b32 v142, 0\n"
    "v_mov_b32 v143, 0\n"
    "v_mov_b32 v144, 0\n"
    "v_mov_b32 v145, 0\n"
    "v_mov_b32 v146, 0\n"
    "v_mov_b32 v147, 0\n"
    "v_mov_b32 v148, 0\n"
    "v_mov_b32 v149, 0\n"
    "v_mov_b32 v150, 0\n"
    "v_mov_b32 v151, 0\n"
    "v_mov_b32 v152, 0\n"
    "v_mov_b32 v153, 0\n"
    "v_mov_b32 v154, 0\n"
    "v_mov_b32 v155, 0\n"
    "v_mov_b32 v156, 0\n"
    "v_mov_b32 v157, 0\n"
    "v_mov_b32 v158, 0\n"
    "v_mov_b32 v159, 0\n"
    "s_mov_b32 s60, 32\n"
    ".Lk_fbl_86:\n"
    "s_add_u32 s58, s58, 2432\n"
    "s_cmp_eq_u32 s58, s59\n"
    "s_cselect_b32 s41, 77824, 0\n"
    "s_sub_u32 s58, s58, s41\n"
    "buffer_load_dword v200, v4, s[20:23], s58 offen offset:2304 sc1\n"
    "buffer_load_dwordx4 v[188:191], v1, s[20:23], s58 offen sc1\n"
    "s_waitcnt vmcnt(1)\n"
    "v_mfma_f32_32x32x4_2b_bf16 v[64:95], v[200:201], v[184:185], 0\n"
    "s_waitcnt vmcnt(0)\n"
    "s_nop 15\n"
    "s_nop 0\n"
    "v_mfma_f32_32x32x16_bf16 v[64:79], v[188:191], v[176:179], v[64:79]\n"
    "v_mfma_f32_32x32x16_bf16 v[80:95], v[188:191], v[180:183], v[80:95]\n"
    "s_nop 10\n"
    "v_max3_f32 v20, v20, v64, v65\n"
    "v_max3_f32 v20, v20, v66, v67\n"
    "v_max3_f32 v20, v20, v68, v69\n"
    "v_max3_f32 v20, v20, v70, v71\n"
    "v_max3_f32 v20, v20, v72, v73\n"
    "v_max3_f32 v20, v20, v74, v75\n"
    "v_max3_f32 v20, v20, v76, v77\n"
    "v_max3_f32 v20, v20, v78, v79\n"
    "v_max3_f32 v21, v21, v80, v81\n"
    "v_max3_f32 v21, v21, v82, v83\n"
    "v_max3_f32 v21, v21, v84, v85\n"
    "v_max3_f32 v21, v21, v86, v87\n"
    "v_max3_f32 v21, v21, v88, v89\n"
    "v_max3_f32 v21, v21, v90, v91\n"
    "v_max3_f32 v21, v21, v92, v93\n"
    "v_max3_f32 v21, v21, v94, v95\n"
    "s_sub_u32 s60, s60, 1\n"
    "s_cmp_lg_u32 s60, 0\n"
    "s_cbranch_scc1 .Lk_fbl_86\n"
    "v_mov_b32 v22, v20\n"
    "s_nop 1\n"
    "v_permlane32_swap_b32 v20, v22\n"
    "v_max_f32 v20, v20, v22\n"
    "v_mov_b32 v22, v21\n"
    "s_nop 1\n"
    "v_permlane32_swap_b32 v21, v22\n"
    "v_max_f32 v21, v21, v22\n"
    "ds_write_b64 v13, v[20:21]\n"
    "s_waitcnt lgkmcnt(0)\n"
    "s_barrier\n"
    "ds_read_b64 v[22:23], v14\n"
    "s_waitcnt lgkmcnt(0)\n"
    "v_max_f32 v20, v20, v23\n"
    "v_max_f32 v21, v21, v22\n"
    "s_waitcnt lgkmcnt(0)\n"
    "s_barrier\n"
    "s_mov_b32 s60, 32\n"
    ".Lk_fbl_87:\n"
    "s_add_u32 s58, s58, 2432\n"
    "s_cmp_eq_u32 s58, s59\n"
    "s_cselect_b32 s41, 77824, 0\n"
    "s_sub_u32 s58, s58, s41\n"
    "buffer_load_dword v200, v4, s[20:23], s58 offen offset:2304 sc1\n"
    "buffer_load_dwordx4 v[188:191], v1, s[20:23], s58 offen sc1\n"
    "buffer_load_dwordx4 v[208:211], v3, s[20:23], s58 offen offset:1024 sc1\n"
    "buffer_load_dwordx4 v[212:215], v3, s[20:23], s58 offen offset:1664 sc1\n"
    "s_waitcnt vmcnt(3)\n"
    "v_mfma_f32_32x32x4_2b_bf16 v[64:95], v[200:201], v[184:185], 0\n"
    "s_waitcnt vmcnt(2)\n"
    "s_nop 15\n"
    "s_nop 0\n"
    "v_mfma_f32_32x32x16_bf16 v[64:79], v[188:191], v[176:179], v[64:79]\n"
    "v_mfma_f32_32x32x16_bf16 v[80:95], v[188:191], v[180:183], v[80:95]\n"
    "s_nop 10\n"
    "v_sub_f32 v64, v64, v20\n"
    "v_sub_f32 v65, v65, v20\n"
    "v_sub_f32 v66, v66, v20\n"
    "v_sub_f32 v67, v67, v20\n"
    "v_sub_f32 v68, v68, v20\n"
    "v_sub_f32 v69, v69, v20\n"
    "v_sub_f32 v70, v70, v20\n"
    "v_sub_f32 v71, v71, v20\n"
    "v_sub_f32 v72, v72, v20\n"
    "v_sub_f32 v73, v73, v20\n"
    "v_sub_f32 v74, v74, v20\n"
    "v_sub_f32 v75, v75, v20\n"
    "v_sub_f32 v76, v76, v20\n"
    "v_sub_f32 v77, v77, v20\n"
    "v_sub_f32 v78, v78, v20\n"
    "v_sub_f32 v79, v79, v20\n"
    "v_sub_f32 v80, v80, v21\n"
    "v_sub_f32 v81, v81, v21\n"
    "v_sub_f32 v82, v82, v21\n"
    "v_sub_f32 v83, v83, v21\n"
    "v_sub_f32 v84, v84, v21\n"
    "v_sub_f32 v85, v85, v21\n"
    "v_sub_f32 v86, v86, v21\n"
    "v_sub_f32 v87, v87, v21\n"
    "v_sub_f32 v88, v88, v21\n"
    "v_sub_f32 v89, v89, v21\n"
    "v_sub_f32 v90, v90, v21\n"
    "v_sub_f32 v91, v91, v21\n"
    "v_sub_f32 v92, v92, v21\n"
    "v_sub_f32 v93, v93, v21\n"
    "v_sub_f32 v94, v94, v21\n"
    "v_sub_f32 v95, v95, v21\n"
    "v_exp_f32 v64, v64\n"
    "v_exp_f32 v65, v65\n"
    "v_exp_f32 v66, v66\n"
    "v_cvt_pk_bf16_f32 v160, v64, v65\n"
    "v_exp_f32 v67, v67\n"
    "v_exp_f32 v68, v68\n"
    "v_cvt_pk_bf16_f32 v161, v66, v67\n"
    "v_exp_f32 v69, v69\n"
    "v_exp_f32 v70, v70\n"
    "v_cvt_pk_bf16_f32 v162, v68, v69\n"
    "v_exp_f32 v71, v71\n"
    "v_exp_f32 v72, v72\n"
    "v_cvt_pk_bf16_f32 v163, v70, v71\n"
    "v_exp_f32 v73, v73\n"
    "v_exp_f32 v74, v74\n"
    "v_cvt_pk_bf16_f32 v164, v72, v73\n"
    "v_exp_f32 v75, v75\n"
    "v_exp_f32 v76, v76\n"
    "v_cvt_pk_bf16_f32 v165, v74, v75\n"
    "v_exp_f32 v77, v77\n"
    "v_exp_f32 v78, v78\n"
    "v_cvt_pk_bf16_f32 v166, v76, v77\n"
    "v_exp_f32 v79, v79\n"
    "v_exp_f32 v80, v80\n"
    "v_cvt_pk_bf16_f32 v167, v78, v79\n"
    "v_exp_f32 v81, v81\n"
    "v_exp_f32 v82, v82\n"
    "v_cvt_pk_bf16_f32 v168, v80, v81\n"
    "v_exp_f32 v83, v83\n"
    "v_exp_f32 v84, v84\n"
    "v_cvt_pk_bf16_f32 v169, v82, v83\n"
    "v_exp_f32 v85, v85\n"
    "v_exp_f32 v86, v86\n"
    "v_cvt_pk_bf16_f32 v170, v84, v85\n"
    "v_exp_f32 v87, v87\n"
    "v_exp_f32 v88, v88\n"
    "v_cvt_pk_bf16_f32 v171, v86, v87\n"
    "v_exp_f32 v89, v89\n"
    "v_exp_f32 v90, v90\n"
    "v_cvt_pk_bf16_f32 v172, v88, v89\n"
    "v_exp_f32 v91, v91\n"
    "v_exp_f32 v92, v92\n"
    "v_cvt_pk_bf16_f32 v173, v90, v91\n"
    "v_exp_f32 v93, v93\n"
    "v_exp_f32 v94, v94\n"
    "v_cvt_pk_bf16_f32 v174, v92, v93\n"
    "v_exp_f32 v95, v95\n"
    "s_nop 0\n"
    "v_cvt_pk_bf16_f32 v175, v94, v95\n"
    "s_waitcnt vmcnt(1)\n"
    "v_mfma_f32_32x32x16_bf16 v[128:143], v[208:211], v[160:163], v[128:143]\n"
    "s_waitcnt vmcnt(0)\n"
    "v_mfma_f32_32x32x16_bf16 v[128:143], v[212:215], v[164:167], v[128:143]\n"
    "v_mfma_f32_32x32x16_bf16 v[144:159], v[208:211], v[168:171], v[144:159]\n"
    "v_mfma_f32_32x32x16_bf16 v[144:159], v[212:215], v[172:175], v[144:159]\n"
    "s_sub_u32 s60, s60, 1\n"
    "s_cmp_lg_u32 s60, 0\n"
    "s_cbranch_scc1 .Lk_fbl_87\n"
    "s_branch .Lk_cmb_63\n"
    ".Lk_end_66:\n"
    :
    : [ka] "s"(__builtin_amdgcn_kernarg_segment_ptr()), [bid] "s"(blockIdx.x), [tid] "{v0}"(threadIdx.x)
    : "v1", "v2", "v3", "v4", "v5", "v6", "v7", "v8", "v9", "v10", "v11", "v12", "v13", "v14", "v15", "v16", "v17", "v18", "v19", "v20", "v21", "v22", "v23", "v24", "v25", "v26", "v27", "v28", "v29", "v30", "v31", "v32", "v33", "v34", "v35", "v36", "v37", "v38", "v39", "v40", "v41", "v42", "v43", "v44", "v45", "v46", "v47", "v48", "v49", "v50", "v51", "v52", "v53", "v54", "v55", "v56", "v57", "v58", "v59", "v60", "v61", "v62", "v63", "v64", "v65", "v66", "v67", "v68", "v69", "v70", "v71", "v72", "v73", "v74", "v75", "v76", "v77", "v78", "v79", "v80", "v81", "v82", "v83", "v84", "v85", "v86", "v87", "v88", "v89", "v90", "v91", "v92", "v93", "v94", "v95", "v96", "v97", "v98", "v99", "v100", "v101", "v102", "v103", "v104", "v105", "v106", "v107", "v108", "v109", "v110", "v111", "v112", "v113", "v114", "v115", "v116", "v117", "v118", "v119", "v120", "v121", "v122", "v123", "v124", "v125", "v126", "v127", "v128", "v129", "v130", "v131", "v132", "v133", "v134", "v135", "v136", "v137", "v138", "v139", "v140", "v141", "v142", "v143", "v144", "v145", "v146", "v147", "v148", "v149", "v150", "v151", "v152", "v153", "v154", "v155", "v156", "v157", "v158", "v159", "v160", "v161", "v162", "v163", "v164", "v165", "v166", "v167", "v168", "v169", "v170", "v171", "v172", "v173", "v174", "v175", "v176", "v177", "v178", "v179", "v180", "v181", "v182", "v183", "v184", "v185", "v186", "v187", "v188", "v189", "v190", "v191", "v192", "v193", "v194", "v195", "v196", "v197", "v198", "v199", "v200", "v201", "v202", "v203", "v204", "v205", "v206", "v207", "v208", "v209", "v210", "v211", "v212", "v213", "v214", "v215", "v216", "v217", "v218", "v219", "v220", "v221", "v222", "v223", "v224", "v225", "v226", "v227", "v228", "v229", "v230", "v231", "v232", "v233", "v234", "v235", "v236", "v237", "v238", "v239", "v240", "v241", "v242", "v243", "v244", "v245", "v246", "v247", "v248", "v249", "v250", "v251", "v252", "v253", "v254", "v255", "s4", "s5", "s6", "s7", "s8", "s9", "s10", "s11", "s12", "s13", "s14", "s15", "s16", "s17", "s18", "s19", "s20", "s21", "s22", "s23", "s24", "s25", "s26", "s27", "s28", "s29", "s30", "s31", "s33", "s34", "s35", "s36", "s37", "s38", "s39", "s40", "s41", "s42", "s43", "s44", "s45", "s46", "s47", "s48", "s49", "s50", "s51", "s52", "s53", "s54", "s55", "s56", "s57", "s58", "s59", "s60", "s61", "s62", "s63", "s64", "s65", "s66", "s67", "s68", "s69", "s70", "s71", "s72", "s73", "s74", "s75", "s76", "s77", "s78", "s79", "s80", "s81", "s82", "s83", "s84", "s85", "s86", "s87", "s88", "s89", "s90", "s91", "s92", "s93", "s94", "s95", "s96", "s97", "s98", "s99", "vcc", "scc", "memory");
}


extern "C" void kernel_launch(void* const* d_in, const int* in_sizes, int n_in, void* d_out, int out_size, void* d_ws, size_t ws_size,
                              hipStream_t stream) {
  (void)hipFuncSetAttribute((const void*)k2, hipFuncAttributeMaxDynamicSharedMemorySize, 163840);
  k2<<<256, 512, 163840, stream>>>((const u16*)d_in[0], (const u16*)d_in[1], (const u16*)d_in[2], (const u16*)d_in[4], (const u16*)d_in[5],
                                   (const u16*)d_in[6], (const u16*)d_in[7], (u16*)d_out, (char*)d_ws);
}
```
